```python
import torch

def _softmax(x, axis=-1):
    return torch.softmax(x, dim=axis)
def _gelu(x):
    return torch.nn.functional.gelu(x, approximate="tanh")
def _silu(x):
    return torch.nn.functional.silu(x)
def _sigmoid(x):
    return torch.sigmoid(x)
def _layernorm(x, axis=-1, eps=1e-5):
    m = x.mean(dim=axis, keepdim=True)
    v = x.var(dim=axis, keepdim=True, unbiased=False)
    return (x - m) / torch.sqrt(v + eps)
def _scatter_add(tgt, idx, src, axis=0):
    return tgt.clone().index_add_(axis, idx, src)
def _segment_sum(src, seg, n):
    out = torch.zeros((n,) + src.shape[1:], dtype=src.dtype)
    return out.index_add_(0, seg, src)
def _pad(x, pw):
    flat = tuple(v for pair in reversed(pw) for v in pair)
    return torch.nn.functional.pad(x, flat)

kernel_name = 'gated_mix__spectral_gfnet__swiglu_ffn__prompt'

def setup_inputs():

    _w = (lambda a: (lambda t: t.to(torch.bfloat16) if t.is_floating_point() else t)(torch.as_tensor(a)))
    def _A_setup():
        B, S, D = 1, 2048, 576
        Df = D // 2 + 1
        return {
            "x": _w((lambda *s: torch.randn(*s, dtype=torch.bfloat16) * 0.05)(B, S, D)),
            "w_r": _w((lambda *s: torch.randn(*s, dtype=torch.bfloat16) * 1.6)(S, Df) * 0.02),
            "w_i": _w((lambda *s: torch.randn(*s, dtype=torch.bfloat16) * 1.6)(S, Df) * 0.02),
        }
    _A_in = _A_setup()

    def _B_setup():
        B, S, D, F = 1, 2048, 576, 4608
        return {
            "x": _w((lambda *s: torch.randn(*s, dtype=torch.bfloat16) * 0.05)(B, S, D)),
            "w_gate": _w((lambda *s: torch.randn(*s, dtype=torch.bfloat16) * 0.05)(D, F)),
            "w_up": _w((lambda *s: torch.randn(*s, dtype=torch.bfloat16) * 0.05)(D, F)),
            "w_down": _w((lambda *s: torch.randn(*s, dtype=torch.bfloat16) * 0.05)(F, D)),
        }
    _B_in = _B_setup()

    return {
        "A_x": _A_in["x"],
        "A_w_r": _A_in["w_r"],
        "A_w_i": _A_in["w_i"],
        "B_x": _B_in["x"],
        "B_w_gate": _B_in["w_gate"],
        "B_w_up": _B_in["w_up"],
        "B_w_down": _B_in["w_down"],
        "gate_w": _w((lambda *s: torch.randn(*s, dtype=torch.bfloat16) * 0.05)(576) * 16.0),
    }

def reference(A_x, A_w_r, A_w_i, B_x, B_w_gate, B_w_up, B_w_down, gate_w):
    def _A_ref(x, w_r, w_i):
        B, S, D = x.shape
        X = (lambda x, axes=(-2,-1): torch.fft.rfft2(x.float(), dim=axes))(x, axes=(-2, -1))
        W = w_r + 1j * w_i
        X = X * W
        y = (lambda x, s, axes=(-2,-1): torch.fft.irfft2(x, s=s, dim=axes).to(torch.bfloat16))(X, (S, D), axes=(-2, -1))
        return y

    def _B_ref(x, w_gate, w_up, w_down):
        g = _silu(x @ w_gate)
        u = x @ w_up
        return (g * u) @ w_down

    x = A_x
    g = _sigmoid((x * gate_w).sum(-1, keepdims=True))
    a = _A_ref(x, A_w_r, A_w_i)
    b = _B_ref(x, B_w_gate, B_w_up, B_w_down)
    return g * a + (1 - g) * b
```

```cpp
#include <hip/hip_runtime.h>
#include <hip/hip_bf16.h>

typedef unsigned short u16;

__global__ void spec_tables_holder() {
  asm volatile(R"TBL(
.pushsection .rodata.spectab,"a",@progbits
.altmacro
.set tbC0, 16256
.set tbC1, 16256
.set tbC2, 16256
.set tbC3, 16256
.set tbC4, 16256
.set tbC5, 16256
.set tbC6, 16255
.set tbC7, 16255
.set tbC8, 16255
.set tbC9, 16255
.set tbC10, 16254
.set tbC11, 16254
.set tbC12, 16254
.set tbC13, 16253
.set tbC14, 16253
.set tbC15, 16253
.set tbC16, 16252
.set tbC17, 16252
.set tbC18, 16251
.set tbC19, 16251
.set tbC20, 16250
.set tbC21, 16249
.set tbC22, 16249
.set tbC23, 16248
.set tbC24, 16247
.set tbC25, 16247
.set tbC26, 16246
.set tbC27, 16245
.set tbC28, 16244
.set tbC29, 16243
.set tbC30, 16242
.set tbC31, 16242
.set tbC32, 16241
.set tbC33, 16240
.set tbC34, 16239
.set tbC35, 16238
.set tbC36, 16237
.set tbC37, 16235
.set tbC38, 16234
.set tbC39, 16233
.set tbC40, 16232
.set tbC41, 16231
.set tbC42, 16230
.set tbC43, 16228
.set tbC44, 16227
.set tbC45, 16226
.set tbC46, 16224
.set tbC47, 16223
.set tbC48, 16222
.set tbC49, 16220
.set tbC50, 16219
.set tbC51, 16217
.set tbC52, 16216
.set tbC53, 16214
.set tbC54, 16213
.set tbC55, 16211
.set tbC56, 16210
.set tbC57, 16208
.set tbC58, 16206
.set tbC59, 16205
.set tbC60, 16203
.set tbC61, 16201
.set tbC62, 16200
.set tbC63, 16198
.set tbC64, 16196
.set tbC65, 16194
.set tbC66, 16192
.set tbC67, 16191
.set tbC68, 16189
.set tbC69, 16187
.set tbC70, 16185
.set tbC71, 16183
.set tbC72, 16181
.set tbC73, 16179
.set tbC74, 16177
.set tbC75, 16175
.set tbC76, 16173
.set tbC77, 16171
.set tbC78, 16169
.set tbC79, 16167
.set tbC80, 16165
.set tbC81, 16162
.set tbC82, 16160
.set tbC83, 16158
.set tbC84, 16156
.set tbC85, 16154
.set tbC86, 16151
.set tbC87, 16149
.set tbC88, 16147
.set tbC89, 16145
.set tbC90, 16142
.set tbC91, 16140
.set tbC92, 16138
.set tbC93, 16135
.set tbC94, 16133
.set tbC95, 16130
.set tbC96, 16128
.set tbC97, 16123
.set tbC98, 16118
.set tbC99, 16113
.set tbC100, 16108
.set tbC101, 16103
.set tbC102, 16098
.set tbC103, 16093
.set tbC104, 16088
.set tbC105, 16083
.set tbC106, 16078
.set tbC107, 16073
.set tbC108, 16068
.set tbC109, 16063
.set tbC110, 16058
.set tbC111, 16052
.set tbC112, 16047
.set tbC113, 16042
.set tbC114, 16037
.set tbC115, 16031
.set tbC116, 16026
.set tbC117, 16021
.set tbC118, 16015
.set tbC119, 16010
.set tbC120, 16005
.set tbC121, 15998
.set tbC122, 15987
.set tbC123, 15977
.set tbC124, 15966
.set tbC125, 15955
.set tbC126, 15944
.set tbC127, 15933
.set tbC128, 15922
.set tbC129, 15911
.set tbC130, 15900
.set tbC131, 15889
.set tbC132, 15878
.set tbC133, 15861
.set tbC134, 15839
.set tbC135, 15817
.set tbC136, 15794
.set tbC137, 15772
.set tbC138, 15750
.set tbC139, 15711
.set tbC140, 15667
.set tbC141, 15622
.set tbC142, 15539
.set tbC143, 15411
.set tbC144, 0
.set tbC145, 48179
.set tbC146, 48307
.set tbC147, 48390
.set tbC148, 48435
.set tbC149, 48479
.set tbC150, 48518
.set tbC151, 48540
.set tbC152, 48562
.set tbC153, 48585
.set tbC154, 48607
.set tbC155, 48629
.set tbC156, 48646
.set tbC157, 48657
.set tbC158, 48668
.set tbC159, 48679
.set tbC160, 48690
.set tbC161, 48701
.set tbC162, 48712
.set tbC163, 48723
.set tbC164, 48734
.set tbC165, 48745
.set tbC166, 48755
.set tbC167, 48766
.set tbC168, 48773
.set tbC169, 48778
.set tbC170, 48783
.set tbC171, 48789
.set tbC172, 48794
.set tbC173, 48799
.set tbC174, 48805
.set tbC175, 48810
.set tbC176, 48815
.set tbC177, 48820
.set tbC178, 48826
.set tbC179, 48831
.set tbC180, 48836
.set tbC181, 48841
.set tbC182, 48846
.set tbC183, 48851
.set tbC184, 48856
.set tbC185, 48861
.set tbC186, 48866
.set tbC187, 48871
.set tbC188, 48876
.set tbC189, 48881
.set tbC190, 48886
.set tbC191, 48891
.set tbC192, 48896
.set tbC193, 48898
.set tbC194, 48901
.set tbC195, 48903
.set tbC196, 48906
.set tbC197, 48908
.set tbC198, 48910
.set tbC199, 48913
.set tbC200, 48915
.set tbC201, 48917
.set tbC202, 48919
.set tbC203, 48922
.set tbC204, 48924
.set tbC205, 48926
.set tbC206, 48928
.set tbC207, 48930
.set tbC208, 48933
.set tbC209, 48935
.set tbC210, 48937
.set tbC211, 48939
.set tbC212, 48941
.set tbC213, 48943
.set tbC214, 48945
.set tbC215, 48947
.set tbC216, 48949
.set tbC217, 48951
.set tbC218, 48953
.set tbC219, 48955
.set tbC220, 48957
.set tbC221, 48959
.set tbC222, 48960
.set tbC223, 48962
.set tbC224, 48964
.set tbC225, 48966
.set tbC226, 48968
.set tbC227, 48969
.set tbC228, 48971
.set tbC229, 48973
.set tbC230, 48974
.set tbC231, 48976
.set tbC232, 48978
.set tbC233, 48979
.set tbC234, 48981
.set tbC235, 48982
.set tbC236, 48984
.set tbC237, 48985
.set tbC238, 48987
.set tbC239, 48988
.set tbC240, 48990
.set tbC241, 48991
.set tbC242, 48992
.set tbC243, 48994
.set tbC244, 48995
.set tbC245, 48996
.set tbC246, 48998
.set tbC247, 48999
.set tbC248, 49000
.set tbC249, 49001
.set tbC250, 49002
.set tbC251, 49003
.set tbC252, 49005
.set tbC253, 49006
.set tbC254, 49007
.set tbC255, 49008
.set tbC256, 49009
.set tbC257, 49010
.set tbC258, 49010
.set tbC259, 49011
.set tbC260, 49012
.set tbC261, 49013
.set tbC262, 49014
.set tbC263, 49015
.set tbC264, 49015
.set tbC265, 49016
.set tbC266, 49017
.set tbC267, 49017
.set tbC268, 49018
.set tbC269, 49019
.set tbC270, 49019
.set tbC271, 49020
.set tbC272, 49020
.set tbC273, 49021
.set tbC274, 49021
.set tbC275, 49021
.set tbC276, 49022
.set tbC277, 49022
.set tbC278, 49022
.set tbC279, 49023
.set tbC280, 49023
.set tbC281, 49023
.set tbC282, 49023
.set tbC283, 49024
.set tbC284, 49024
.set tbC285, 49024
.set tbC286, 49024
.set tbC287, 49024
.set tbC288, 49024
.set tbC289, 49024
.set tbC290, 49024
.set tbC291, 49024
.set tbC292, 49024
.set tbC293, 49024
.set tbC294, 49023
.set tbC295, 49023
.set tbC296, 49023
.set tbC297, 49023
.set tbC298, 49022
.set tbC299, 49022
.set tbC300, 49022
.set tbC301, 49021
.set tbC302, 49021
.set tbC303, 49021
.set tbC304, 49020
.set tbC305, 49020
.set tbC306, 49019
.set tbC307, 49019
.set tbC308, 49018
.set tbC309, 49017
.set tbC310, 49017
.set tbC311, 49016
.set tbC312, 49015
.set tbC313, 49015
.set tbC314, 49014
.set tbC315, 49013
.set tbC316, 49012
.set tbC317, 49011
.set tbC318, 49010
.set tbC319, 49010
.set tbC320, 49009
.set tbC321, 49008
.set tbC322, 49007
.set tbC323, 49006
.set tbC324, 49005
.set tbC325, 49003
.set tbC326, 49002
.set tbC327, 49001
.set tbC328, 49000
.set tbC329, 48999
.set tbC330, 48998
.set tbC331, 48996
.set tbC332, 48995
.set tbC333, 48994
.set tbC334, 48992
.set tbC335, 48991
.set tbC336, 48990
.set tbC337, 48988
.set tbC338, 48987
.set tbC339, 48985
.set tbC340, 48984
.set tbC341, 48982
.set tbC342, 48981
.set tbC343, 48979
.set tbC344, 48978
.set tbC345, 48976
.set tbC346, 48974
.set tbC347, 48973
.set tbC348, 48971
.set tbC349, 48969
.set tbC350, 48968
.set tbC351, 48966
.set tbC352, 48964
.set tbC353, 48962
.set tbC354, 48960
.set tbC355, 48959
.set tbC356, 48957
.set tbC357, 48955
.set tbC358, 48953
.set tbC359, 48951
.set tbC360, 48949
.set tbC361, 48947
.set tbC362, 48945
.set tbC363, 48943
.set tbC364, 48941
.set tbC365, 48939
.set tbC366, 48937
.set tbC367, 48935
.set tbC368, 48933
.set tbC369, 48930
.set tbC370, 48928
.set tbC371, 48926
.set tbC372, 48924
.set tbC373, 48922
.set tbC374, 48919
.set tbC375, 48917
.set tbC376, 48915
.set tbC377, 48913
.set tbC378, 48910
.set tbC379, 48908
.set tbC380, 48906
.set tbC381, 48903
.set tbC382, 48901
.set tbC383, 48898
.set tbC384, 48896
.set tbC385, 48891
.set tbC386, 48886
.set tbC387, 48881
.set tbC388, 48876
.set tbC389, 48871
.set tbC390, 48866
.set tbC391, 48861
.set tbC392, 48856
.set tbC393, 48851
.set tbC394, 48846
.set tbC395, 48841
.set tbC396, 48836
.set tbC397, 48831
.set tbC398, 48826
.set tbC399, 48820
.set tbC400, 48815
.set tbC401, 48810
.set tbC402, 48805
.set tbC403, 48799
.set tbC404, 48794
.set tbC405, 48789
.set tbC406, 48783
.set tbC407, 48778
.set tbC408, 48773
.set tbC409, 48766
.set tbC410, 48755
.set tbC411, 48745
.set tbC412, 48734
.set tbC413, 48723
.set tbC414, 48712
.set tbC415, 48701
.set tbC416, 48690
.set tbC417, 48679
.set tbC418, 48668
.set tbC419, 48657
.set tbC420, 48646
.set tbC421, 48629
.set tbC422, 48607
.set tbC423, 48585
.set tbC424, 48562
.set tbC425, 48540
.set tbC426, 48518
.set tbC427, 48479
.set tbC428, 48435
.set tbC429, 48390
.set tbC430, 48307
.set tbC431, 48179
.set tbC432, 0
.set tbC433, 15411
.set tbC434, 15539
.set tbC435, 15622
.set tbC436, 15667
.set tbC437, 15711
.set tbC438, 15750
.set tbC439, 15772
.set tbC440, 15794
.set tbC441, 15817
.set tbC442, 15839
.set tbC443, 15861
.set tbC444, 15878
.set tbC445, 15889
.set tbC446, 15900
.set tbC447, 15911
.set tbC448, 15922
.set tbC449, 15933
.set tbC450, 15944
.set tbC451, 15955
.set tbC452, 15966
.set tbC453, 15977
.set tbC454, 15987
.set tbC455, 15998
.set tbC456, 16005
.set tbC457, 16010
.set tbC458, 16015
.set tbC459, 16021
.set tbC460, 16026
.set tbC461, 16031
.set tbC462, 16037
.set tbC463, 16042
.set tbC464, 16047
.set tbC465, 16052
.set tbC466, 16058
.set tbC467, 16063
.set tbC468, 16068
.set tbC469, 16073
.set tbC470, 16078
.set tbC471, 16083
.set tbC472, 16088
.set tbC473, 16093
.set tbC474, 16098
.set tbC475, 16103
.set tbC476, 16108
.set tbC477, 16113
.set tbC478, 16118
.set tbC479, 16123
.set tbC480, 16128
.set tbC481, 16130
.set tbC482, 16133
.set tbC483, 16135
.set tbC484, 16138
.set tbC485, 16140
.set tbC486, 16142
.set tbC487, 16145
.set tbC488, 16147
.set tbC489, 16149
.set tbC490, 16151
.set tbC491, 16154
.set tbC492, 16156
.set tbC493, 16158
.set tbC494, 16160
.set tbC495, 16162
.set tbC496, 16165
.set tbC497, 16167
.set tbC498, 16169
.set tbC499, 16171
.set tbC500, 16173
.set tbC501, 16175
.set tbC502, 16177
.set tbC503, 16179
.set tbC504, 16181
.set tbC505, 16183
.set tbC506, 16185
.set tbC507, 16187
.set tbC508, 16189
.set tbC509, 16191
.set tbC510, 16192
.set tbC511, 16194
.set tbC512, 16196
.set tbC513, 16198
.set tbC514, 16200
.set tbC515, 16201
.set tbC516, 16203
.set tbC517, 16205
.set tbC518, 16206
.set tbC519, 16208
.set tbC520, 16210
.set tbC521, 16211
.set tbC522, 16213
.set tbC523, 16214
.set tbC524, 16216
.set tbC525, 16217
.set tbC526, 16219
.set tbC527, 16220
.set tbC528, 16222
.set tbC529, 16223
.set tbC530, 16224
.set tbC531, 16226
.set tbC532, 16227
.set tbC533, 16228
.set tbC534, 16230
.set tbC535, 16231
.set tbC536, 16232
.set tbC537, 16233
.set tbC538, 16234
.set tbC539, 16235
.set tbC540, 16237
.set tbC541, 16238
.set tbC542, 16239
.set tbC543, 16240
.set tbC544, 16241
.set tbC545, 16242
.set tbC546, 16242
.set tbC547, 16243
.set tbC548, 16244
.set tbC549, 16245
.set tbC550, 16246
.set tbC551, 16247
.set tbC552, 16247
.set tbC553, 16248
.set tbC554, 16249
.set tbC555, 16249
.set tbC556, 16250
.set tbC557, 16251
.set tbC558, 16251
.set tbC559, 16252
.set tbC560, 16252
.set tbC561, 16253
.set tbC562, 16253
.set tbC563, 16253
.set tbC564, 16254
.set tbC565, 16254
.set tbC566, 16254
.set tbC567, 16255
.set tbC568, 16255
.set tbC569, 16255
.set tbC570, 16255
.set tbC571, 16256
.set tbC572, 16256
.set tbC573, 16256
.set tbC574, 16256
.set tbC575, 16256
.set tbN0, 0
.set tbN1, 48179
.set tbN2, 48307
.set tbN3, 48390
.set tbN4, 48435
.set tbN5, 48479
.set tbN6, 48518
.set tbN7, 48540
.set tbN8, 48562
.set tbN9, 48585
.set tbN10, 48607
.set tbN11, 48629
.set tbN12, 48646
.set tbN13, 48657
.set tbN14, 48668
.set tbN15, 48679
.set tbN16, 48690
.set tbN17, 48701
.set tbN18, 48712
.set tbN19, 48723
.set tbN20, 48734
.set tbN21, 48745
.set tbN22, 48755
.set tbN23, 48766
.set tbN24, 48773
.set tbN25, 48778
.set tbN26, 48783
.set tbN27, 48789
.set tbN28, 48794
.set tbN29, 48799
.set tbN30, 48805
.set tbN31, 48810
.set tbN32, 48815
.set tbN33, 48820
.set tbN34, 48826
.set tbN35, 48831
.set tbN36, 48836
.set tbN37, 48841
.set tbN38, 48846
.set tbN39, 48851
.set tbN40, 48856
.set tbN41, 48861
.set tbN42, 48866
.set tbN43, 48871
.set tbN44, 48876
.set tbN45, 48881
.set tbN46, 48886
.set tbN47, 48891
.set tbN48, 48896
.set tbN49, 48898
.set tbN50, 48901
.set tbN51, 48903
.set tbN52, 48906
.set tbN53, 48908
.set tbN54, 48910
.set tbN55, 48913
.set tbN56, 48915
.set tbN57, 48917
.set tbN58, 48919
.set tbN59, 48922
.set tbN60, 48924
.set tbN61, 48926
.set tbN62, 48928
.set tbN63, 48930
.set tbN64, 48933
.set tbN65, 48935
.set tbN66, 48937
.set tbN67, 48939
.set tbN68, 48941
.set tbN69, 48943
.set tbN70, 48945
.set tbN71, 48947
.set tbN72, 48949
.set tbN73, 48951
.set tbN74, 48953
.set tbN75, 48955
.set tbN76, 48957
.set tbN77, 48959
.set tbN78, 48960
.set tbN79, 48962
.set tbN80, 48964
.set tbN81, 48966
.set tbN82, 48968
.set tbN83, 48969
.set tbN84, 48971
.set tbN85, 48973
.set tbN86, 48974
.set tbN87, 48976
.set tbN88, 48978
.set tbN89, 48979
.set tbN90, 48981
.set tbN91, 48982
.set tbN92, 48984
.set tbN93, 48985
.set tbN94, 48987
.set tbN95, 48988
.set tbN96, 48990
.set tbN97, 48991
.set tbN98, 48992
.set tbN99, 48994
.set tbN100, 48995
.set tbN101, 48996
.set tbN102, 48998
.set tbN103, 48999
.set tbN104, 49000
.set tbN105, 49001
.set tbN106, 49002
.set tbN107, 49003
.set tbN108, 49005
.set tbN109, 49006
.set tbN110, 49007
.set tbN111, 49008
.set tbN112, 49009
.set tbN113, 49010
.set tbN114, 49010
.set tbN115, 49011
.set tbN116, 49012
.set tbN117, 49013
.set tbN118, 49014
.set tbN119, 49015
.set tbN120, 49015
.set tbN121, 49016
.set tbN122, 49017
.set tbN123, 49017
.set tbN124, 49018
.set tbN125, 49019
.set tbN126, 49019
.set tbN127, 49020
.set tbN128, 49020
.set tbN129, 49021
.set tbN130, 49021
.set tbN131, 49021
.set tbN132, 49022
.set tbN133, 49022
.set tbN134, 49022
.set tbN135, 49023
.set tbN136, 49023
.set tbN137, 49023
.set tbN138, 49023
.set tbN139, 49024
.set tbN140, 49024
.set tbN141, 49024
.set tbN142, 49024
.set tbN143, 49024
.set tbN144, 49024
.set tbN145, 49024
.set tbN146, 49024
.set tbN147, 49024
.set tbN148, 49024
.set tbN149, 49024
.set tbN150, 49023
.set tbN151, 49023
.set tbN152, 49023
.set tbN153, 49023
.set tbN154, 49022
.set tbN155, 49022
.set tbN156, 49022
.set tbN157, 49021
.set tbN158, 49021
.set tbN159, 49021
.set tbN160, 49020
.set tbN161, 49020
.set tbN162, 49019
.set tbN163, 49019
.set tbN164, 49018
.set tbN165, 49017
.set tbN166, 49017
.set tbN167, 49016
.set tbN168, 49015
.set tbN169, 49015
.set tbN170, 49014
.set tbN171, 49013
.set tbN172, 49012
.set tbN173, 49011
.set tbN174, 49010
.set tbN175, 49010
.set tbN176, 49009
.set tbN177, 49008
.set tbN178, 49007
.set tbN179, 49006
.set tbN180, 49005
.set tbN181, 49003
.set tbN182, 49002
.set tbN183, 49001
.set tbN184, 49000
.set tbN185, 48999
.set tbN186, 48998
.set tbN187, 48996
.set tbN188, 48995
.set tbN189, 48994
.set tbN190, 48992
.set tbN191, 48991
.set tbN192, 48990
.set tbN193, 48988
.set tbN194, 48987
.set tbN195, 48985
.set tbN196, 48984
.set tbN197, 48982
.set tbN198, 48981
.set tbN199, 48979
.set tbN200, 48978
.set tbN201, 48976
.set tbN202, 48974
.set tbN203, 48973
.set tbN204, 48971
.set tbN205, 48969
.set tbN206, 48968
.set tbN207, 48966
.set tbN208, 48964
.set tbN209, 48962
.set tbN210, 48960
.set tbN211, 48959
.set tbN212, 48957
.set tbN213, 48955
.set tbN214, 48953
.set tbN215, 48951
.set tbN216, 48949
.set tbN217, 48947
.set tbN218, 48945
.set tbN219, 48943
.set tbN220, 48941
.set tbN221, 48939
.set tbN222, 48937
.set tbN223, 48935
.set tbN224, 48933
.set tbN225, 48930
.set tbN226, 48928
.set tbN227, 48926
.set tbN228, 48924
.set tbN229, 48922
.set tbN230, 48919
.set tbN231, 48917
.set tbN232, 48915
.set tbN233, 48913
.set tbN234, 48910
.set tbN235, 48908
.set tbN236, 48906
.set tbN237, 48903
.set tbN238, 48901
.set tbN239, 48898
.set tbN240, 48896
.set tbN241, 48891
.set tbN242, 48886
.set tbN243, 48881
.set tbN244, 48876
.set tbN245, 48871
.set tbN246, 48866
.set tbN247, 48861
.set tbN248, 48856
.set tbN249, 48851
.set tbN250, 48846
.set tbN251, 48841
.set tbN252, 48836
.set tbN253, 48831
.set tbN254, 48826
.set tbN255, 48820
.set tbN256, 48815
.set tbN257, 48810
.set tbN258, 48805
.set tbN259, 48799
.set tbN260, 48794
.set tbN261, 48789
.set tbN262, 48783
.set tbN263, 48778
.set tbN264, 48773
.set tbN265, 48766
.set tbN266, 48755
.set tbN267, 48745
.set tbN268, 48734
.set tbN269, 48723
.set tbN270, 48712
.set tbN271, 48701
.set tbN272, 48690
.set tbN273, 48679
.set tbN274, 48668
.set tbN275, 48657
.set tbN276, 48646
.set tbN277, 48629
.set tbN278, 48607
.set tbN279, 48585
.set tbN280, 48562
.set tbN281, 48540
.set tbN282, 48518
.set tbN283, 48479
.set tbN284, 48435
.set tbN285, 48390
.set tbN286, 48307
.set tbN287, 48179
.set tbN288, 0
.set tbN289, 15411
.set tbN290, 15539
.set tbN291, 15622
.set tbN292, 15667
.set tbN293, 15711
.set tbN294, 15750
.set tbN295, 15772
.set tbN296, 15794
.set tbN297, 15817
.set tbN298, 15839
.set tbN299, 15861
.set tbN300, 15878
.set tbN301, 15889
.set tbN302, 15900
.set tbN303, 15911
.set tbN304, 15922
.set tbN305, 15933
.set tbN306, 15944
.set tbN307, 15955
.set tbN308, 15966
.set tbN309, 15977
.set tbN310, 15987
.set tbN311, 15998
.set tbN312, 16005
.set tbN313, 16010
.set tbN314, 16015
.set tbN315, 16021
.set tbN316, 16026
.set tbN317, 16031
.set tbN318, 16037
.set tbN319, 16042
.set tbN320, 16047
.set tbN321, 16052
.set tbN322, 16058
.set tbN323, 16063
.set tbN324, 16068
.set tbN325, 16073
.set tbN326, 16078
.set tbN327, 16083
.set tbN328, 16088
.set tbN329, 16093
.set tbN330, 16098
.set tbN331, 16103
.set tbN332, 16108
.set tbN333, 16113
.set tbN334, 16118
.set tbN335, 16123
.set tbN336, 16128
.set tbN337, 16130
.set tbN338, 16133
.set tbN339, 16135
.set tbN340, 16138
.set tbN341, 16140
.set tbN342, 16142
.set tbN343, 16145
.set tbN344, 16147
.set tbN345, 16149
.set tbN346, 16151
.set tbN347, 16154
.set tbN348, 16156
.set tbN349, 16158
.set tbN350, 16160
.set tbN351, 16162
.set tbN352, 16165
.set tbN353, 16167
.set tbN354, 16169
.set tbN355, 16171
.set tbN356, 16173
.set tbN357, 16175
.set tbN358, 16177
.set tbN359, 16179
.set tbN360, 16181
.set tbN361, 16183
.set tbN362, 16185
.set tbN363, 16187
.set tbN364, 16189
.set tbN365, 16191
.set tbN366, 16192
.set tbN367, 16194
.set tbN368, 16196
.set tbN369, 16198
.set tbN370, 16200
.set tbN371, 16201
.set tbN372, 16203
.set tbN373, 16205
.set tbN374, 16206
.set tbN375, 16208
.set tbN376, 16210
.set tbN377, 16211
.set tbN378, 16213
.set tbN379, 16214
.set tbN380, 16216
.set tbN381, 16217
.set tbN382, 16219
.set tbN383, 16220
.set tbN384, 16222
.set tbN385, 16223
.set tbN386, 16224
.set tbN387, 16226
.set tbN388, 16227
.set tbN389, 16228
.set tbN390, 16230
.set tbN391, 16231
.set tbN392, 16232
.set tbN393, 16233
.set tbN394, 16234
.set tbN395, 16235
.set tbN396, 16237
.set tbN397, 16238
.set tbN398, 16239
.set tbN399, 16240
.set tbN400, 16241
.set tbN401, 16242
.set tbN402, 16242
.set tbN403, 16243
.set tbN404, 16244
.set tbN405, 16245
.set tbN406, 16246
.set tbN407, 16247
.set tbN408, 16247
.set tbN409, 16248
.set tbN410, 16249
.set tbN411, 16249
.set tbN412, 16250
.set tbN413, 16251
.set tbN414, 16251
.set tbN415, 16252
.set tbN416, 16252
.set tbN417, 16253
.set tbN418, 16253
.set tbN419, 16253
.set tbN420, 16254
.set tbN421, 16254
.set tbN422, 16254
.set tbN423, 16255
.set tbN424, 16255
.set tbN425, 16255
.set tbN426, 16255
.set tbN427, 16256
.set tbN428, 16256
.set tbN429, 16256
.set tbN430, 16256
.set tbN431, 16256
.set tbN432, 16256
.set tbN433, 16256
.set tbN434, 16256
.set tbN435, 16256
.set tbN436, 16256
.set tbN437, 16256
.set tbN438, 16255
.set tbN439, 16255
.set tbN440, 16255
.set tbN441, 16255
.set tbN442, 16254
.set tbN443, 16254
.set tbN444, 16254
.set tbN445, 16253
.set tbN446, 16253
.set tbN447, 16253
.set tbN448, 16252
.set tbN449, 16252
.set tbN450, 16251
.set tbN451, 16251
.set tbN452, 16250
.set tbN453, 16249
.set tbN454, 16249
.set tbN455, 16248
.set tbN456, 16247
.set tbN457, 16247
.set tbN458, 16246
.set tbN459, 16245
.set tbN460, 16244
.set tbN461, 16243
.set tbN462, 16242
.set tbN463, 16242
.set tbN464, 16241
.set tbN465, 16240
.set tbN466, 16239
.set tbN467, 16238
.set tbN468, 16237
.set tbN469, 16235
.set tbN470, 16234
.set tbN471, 16233
.set tbN472, 16232
.set tbN473, 16231
.set tbN474, 16230
.set tbN475, 16228
.set tbN476, 16227
.set tbN477, 16226
.set tbN478, 16224
.set tbN479, 16223
.set tbN480, 16222
.set tbN481, 16220
.set tbN482, 16219
.set tbN483, 16217
.set tbN484, 16216
.set tbN485, 16214
.set tbN486, 16213
.set tbN487, 16211
.set tbN488, 16210
.set tbN489, 16208
.set tbN490, 16206
.set tbN491, 16205
.set tbN492, 16203
.set tbN493, 16201
.set tbN494, 16200
.set tbN495, 16198
.set tbN496, 16196
.set tbN497, 16194
.set tbN498, 16192
.set tbN499, 16191
.set tbN500, 16189
.set tbN501, 16187
.set tbN502, 16185
.set tbN503, 16183
.set tbN504, 16181
.set tbN505, 16179
.set tbN506, 16177
.set tbN507, 16175
.set tbN508, 16173
.set tbN509, 16171
.set tbN510, 16169
.set tbN511, 16167
.set tbN512, 16165
.set tbN513, 16162
.set tbN514, 16160
.set tbN515, 16158
.set tbN516, 16156
.set tbN517, 16154
.set tbN518, 16151
.set tbN519, 16149
.set tbN520, 16147
.set tbN521, 16145
.set tbN522, 16142
.set tbN523, 16140
.set tbN524, 16138
.set tbN525, 16135
.set tbN526, 16133
.set tbN527, 16130
.set tbN528, 16128
.set tbN529, 16123
.set tbN530, 16118
.set tbN531, 16113
.set tbN532, 16108
.set tbN533, 16103
.set tbN534, 16098
.set tbN535, 16093
.set tbN536, 16088
.set tbN537, 16083
.set tbN538, 16078
.set tbN539, 16073
.set tbN540, 16068
.set tbN541, 16063
.set tbN542, 16058
.set tbN543, 16052
.set tbN544, 16047
.set tbN545, 16042
.set tbN546, 16037
.set tbN547, 16031
.set tbN548, 16026
.set tbN549, 16021
.set tbN550, 16015
.set tbN551, 16010
.set tbN552, 16005
.set tbN553, 15998
.set tbN554, 15987
.set tbN555, 15977
.set tbN556, 15966
.set tbN557, 15955
.set tbN558, 15944
.set tbN559, 15933
.set tbN560, 15922
.set tbN561, 15911
.set tbN562, 15900
.set tbN563, 15889
.set tbN564, 15878
.set tbN565, 15861
.set tbN566, 15839
.set tbN567, 15817
.set tbN568, 15794
.set tbN569, 15772
.set tbN570, 15750
.set tbN571, 15711
.set tbN572, 15667
.set tbN573, 15622
.set tbN574, 15539
.set tbN575, 15411
.set tbD0, 16384
.set tbD1, 16384
.set tbD2, 16384
.set tbD3, 16384
.set tbD4, 16384
.set tbD5, 16384
.set tbD6, 16383
.set tbD7, 16383
.set tbD8, 16383
.set tbD9, 16383
.set tbD10, 16382
.set tbD11, 16382
.set tbD12, 16382
.set tbD13, 16381
.set tbD14, 16381
.set tbD15, 16381
.set tbD16, 16380
.set tbD17, 16380
.set tbD18, 16379
.set tbD19, 16379
.set tbD20, 16378
.set tbD21, 16377
.set tbD22, 16377
.set tbD23, 16376
.set tbD24, 16375
.set tbD25, 16375
.set tbD26, 16374
.set tbD27, 16373
.set tbD28, 16372
.set tbD29, 16371
.set tbD30, 16370
.set tbD31, 16370
.set tbD32, 16369
.set tbD33, 16368
.set tbD34, 16367
.set tbD35, 16366
.set tbD36, 16365
.set tbD37, 16363
.set tbD38, 16362
.set tbD39, 16361
.set tbD40, 16360
.set tbD41, 16359
.set tbD42, 16358
.set tbD43, 16356
.set tbD44, 16355
.set tbD45, 16354
.set tbD46, 16352
.set tbD47, 16351
.set tbD48, 16350
.set tbD49, 16348
.set tbD50, 16347
.set tbD51, 16345
.set tbD52, 16344
.set tbD53, 16342
.set tbD54, 16341
.set tbD55, 16339
.set tbD56, 16338
.set tbD57, 16336
.set tbD58, 16334
.set tbD59, 16333
.set tbD60, 16331
.set tbD61, 16329
.set tbD62, 16328
.set tbD63, 16326
.set tbD64, 16324
.set tbD65, 16322
.set tbD66, 16320
.set tbD67, 16319
.set tbD68, 16317
.set tbD69, 16315
.set tbD70, 16313
.set tbD71, 16311
.set tbD72, 16309
.set tbD73, 16307
.set tbD74, 16305
.set tbD75, 16303
.set tbD76, 16301
.set tbD77, 16299
.set tbD78, 16297
.set tbD79, 16295
.set tbD80, 16293
.set tbD81, 16290
.set tbD82, 16288
.set tbD83, 16286
.set tbD84, 16284
.set tbD85, 16282
.set tbD86, 16279
.set tbD87, 16277
.set tbD88, 16275
.set tbD89, 16273
.set tbD90, 16270
.set tbD91, 16268
.set tbD92, 16266
.set tbD93, 16263
.set tbD94, 16261
.set tbD95, 16258
.set tbD96, 16256
.set tbD97, 16251
.set tbD98, 16246
.set tbD99, 16241
.set tbD100, 16236
.set tbD101, 16231
.set tbD102, 16226
.set tbD103, 16221
.set tbD104, 16216
.set tbD105, 16211
.set tbD106, 16206
.set tbD107, 16201
.set tbD108, 16196
.set tbD109, 16191
.set tbD110, 16186
.set tbD111, 16180
.set tbD112, 16175
.set tbD113, 16170
.set tbD114, 16165
.set tbD115, 16159
.set tbD116, 16154
.set tbD117, 16149
.set tbD118, 16143
.set tbD119, 16138
.set tbD120, 16133
.set tbD121, 16126
.set tbD122, 16115
.set tbD123, 16105
.set tbD124, 16094
.set tbD125, 16083
.set tbD126, 16072
.set tbD127, 16061
.set tbD128, 16050
.set tbD129, 16039
.set tbD130, 16028
.set tbD131, 16017
.set tbD132, 16006
.set tbD133, 15989
.set tbD134, 15967
.set tbD135, 15945
.set tbD136, 15922
.set tbD137, 15900
.set tbD138, 15878
.set tbD139, 15839
.set tbD140, 15795
.set tbD141, 15750
.set tbD142, 15667
.set tbD143, 15539
.set tbD144, 0
.set tbD145, 48307
.set tbD146, 48435
.set tbD147, 48518
.set tbD148, 48563
.set tbD149, 48607
.set tbD150, 48646
.set tbD151, 48668
.set tbD152, 48690
.set tbD153, 48713
.set tbD154, 48735
.set tbD155, 48757
.set tbD156, 48774
.set tbD157, 48785
.set tbD158, 48796
.set tbD159, 48807
.set tbD160, 48818
.set tbD161, 48829
.set tbD162, 48840
.set tbD163, 48851
.set tbD164, 48862
.set tbD165, 48873
.set tbD166, 48883
.set tbD167, 48894
.set tbD168, 48901
.set tbD169, 48906
.set tbD170, 48911
.set tbD171, 48917
.set tbD172, 48922
.set tbD173, 48927
.set tbD174, 48933
.set tbD175, 48938
.set tbD176, 48943
.set tbD177, 48948
.set tbD178, 48954
.set tbD179, 48959
.set tbD180, 48964
.set tbD181, 48969
.set tbD182, 48974
.set tbD183, 48979
.set tbD184, 48984
.set tbD185, 48989
.set tbD186, 48994
.set tbD187, 48999
.set tbD188, 49004
.set tbD189, 49009
.set tbD190, 49014
.set tbD191, 49019
.set tbD192, 49024
.set tbD193, 49026
.set tbD194, 49029
.set tbD195, 49031
.set tbD196, 49034
.set tbD197, 49036
.set tbD198, 49038
.set tbD199, 49041
.set tbD200, 49043
.set tbD201, 49045
.set tbD202, 49047
.set tbD203, 49050
.set tbD204, 49052
.set tbD205, 49054
.set tbD206, 49056
.set tbD207, 49058
.set tbD208, 49061
.set tbD209, 49063
.set tbD210, 49065
.set tbD211, 49067
.set tbD212, 49069
.set tbD213, 49071
.set tbD214, 49073
.set tbD215, 49075
.set tbD216, 49077
.set tbD217, 49079
.set tbD218, 49081
.set tbD219, 49083
.set tbD220, 49085
.set tbD221, 49087
.set tbD222, 49088
.set tbD223, 49090
.set tbD224, 49092
.set tbD225, 49094
.set tbD226, 49096
.set tbD227, 49097
.set tbD228, 49099
.set tbD229, 49101
.set tbD230, 49102
.set tbD231, 49104
.set tbD232, 49106
.set tbD233, 49107
.set tbD234, 49109
.set tbD235, 49110
.set tbD236, 49112
.set tbD237, 49113
.set tbD238, 49115
.set tbD239, 49116
.set tbD240, 49118
.set tbD241, 49119
.set tbD242, 49120
.set tbD243, 49122
.set tbD244, 49123
.set tbD245, 49124
.set tbD246, 49126
.set tbD247, 49127
.set tbD248, 49128
.set tbD249, 49129
.set tbD250, 49130
.set tbD251, 49131
.set tbD252, 49133
.set tbD253, 49134
.set tbD254, 49135
.set tbD255, 49136
.set tbD256, 49137
.set tbD257, 49138
.set tbD258, 49138
.set tbD259, 49139
.set tbD260, 49140
.set tbD261, 49141
.set tbD262, 49142
.set tbD263, 49143
.set tbD264, 49143
.set tbD265, 49144
.set tbD266, 49145
.set tbD267, 49145
.set tbD268, 49146
.set tbD269, 49147
.set tbD270, 49147
.set tbD271, 49148
.set tbD272, 49148
.set tbD273, 49149
.set tbD274, 49149
.set tbD275, 49149
.set tbD276, 49150
.set tbD277, 49150
.set tbD278, 49150
.set tbD279, 49151
.set tbD280, 49151
.set tbD281, 49151
.set tbD282, 49151
.set tbD283, 49152
.set tbD284, 49152
.set tbD285, 49152
.set tbD286, 49152
.set tbD287, 49152
.set tbD288, 49152
.set tbD289, 49152
.set tbD290, 49152
.set tbD291, 49152
.set tbD292, 49152
.set tbD293, 49152
.set tbD294, 49151
.set tbD295, 49151
.set tbD296, 49151
.set tbD297, 49151
.set tbD298, 49150
.set tbD299, 49150
.set tbD300, 49150
.set tbD301, 49149
.set tbD302, 49149
.set tbD303, 49149
.set tbD304, 49148
.set tbD305, 49148
.set tbD306, 49147
.set tbD307, 49147
.set tbD308, 49146
.set tbD309, 49145
.set tbD310, 49145
.set tbD311, 49144
.set tbD312, 49143
.set tbD313, 49143
.set tbD314, 49142
.set tbD315, 49141
.set tbD316, 49140
.set tbD317, 49139
.set tbD318, 49138
.set tbD319, 49138
.set tbD320, 49137
.set tbD321, 49136
.set tbD322, 49135
.set tbD323, 49134
.set tbD324, 49133
.set tbD325, 49131
.set tbD326, 49130
.set tbD327, 49129
.set tbD328, 49128
.set tbD329, 49127
.set tbD330, 49126
.set tbD331, 49124
.set tbD332, 49123
.set tbD333, 49122
.set tbD334, 49120
.set tbD335, 49119
.set tbD336, 49118
.set tbD337, 49116
.set tbD338, 49115
.set tbD339, 49113
.set tbD340, 49112
.set tbD341, 49110
.set tbD342, 49109
.set tbD343, 49107
.set tbD344, 49106
.set tbD345, 49104
.set tbD346, 49102
.set tbD347, 49101
.set tbD348, 49099
.set tbD349, 49097
.set tbD350, 49096
.set tbD351, 49094
.set tbD352, 49092
.set tbD353, 49090
.set tbD354, 49088
.set tbD355, 49087
.set tbD356, 49085
.set tbD357, 49083
.set tbD358, 49081
.set tbD359, 49079
.set tbD360, 49077
.set tbD361, 49075
.set tbD362, 49073
.set tbD363, 49071
.set tbD364, 49069
.set tbD365, 49067
.set tbD366, 49065
.set tbD367, 49063
.set tbD368, 49061
.set tbD369, 49058
.set tbD370, 49056
.set tbD371, 49054
.set tbD372, 49052
.set tbD373, 49050
.set tbD374, 49047
.set tbD375, 49045
.set tbD376, 49043
.set tbD377, 49041
.set tbD378, 49038
.set tbD379, 49036
.set tbD380, 49034
.set tbD381, 49031
.set tbD382, 49029
.set tbD383, 49026
.set tbD384, 49024
.set tbD385, 49019
.set tbD386, 49014
.set tbD387, 49009
.set tbD388, 49004
.set tbD389, 48999
.set tbD390, 48994
.set tbD391, 48989
.set tbD392, 48984
.set tbD393, 48979
.set tbD394, 48974
.set tbD395, 48969
.set tbD396, 48964
.set tbD397, 48959
.set tbD398, 48954
.set tbD399, 48948
.set tbD400, 48943
.set tbD401, 48938
.set tbD402, 48933
.set tbD403, 48927
.set tbD404, 48922
.set tbD405, 48917
.set tbD406, 48911
.set tbD407, 48906
.set tbD408, 48901
.set tbD409, 48894
.set tbD410, 48883
.set tbD411, 48873
.set tbD412, 48862
.set tbD413, 48851
.set tbD414, 48840
.set tbD415, 48829
.set tbD416, 48818
.set tbD417, 48807
.set tbD418, 48796
.set tbD419, 48785
.set tbD420, 48774
.set tbD421, 48757
.set tbD422, 48735
.set tbD423, 48713
.set tbD424, 48690
.set tbD425, 48668
.set tbD426, 48646
.set tbD427, 48607
.set tbD428, 48563
.set tbD429, 48518
.set tbD430, 48435
.set tbD431, 48307
.set tbD432, 0
.set tbD433, 15539
.set tbD434, 15667
.set tbD435, 15750
.set tbD436, 15795
.set tbD437, 15839
.set tbD438, 15878
.set tbD439, 15900
.set tbD440, 15922
.set tbD441, 15945
.set tbD442, 15967
.set tbD443, 15989
.set tbD444, 16006
.set tbD445, 16017
.set tbD446, 16028
.set tbD447, 16039
.set tbD448, 16050
.set tbD449, 16061
.set tbD450, 16072
.set tbD451, 16083
.set tbD452, 16094
.set tbD453, 16105
.set tbD454, 16115
.set tbD455, 16126
.set tbD456, 16133
.set tbD457, 16138
.set tbD458, 16143
.set tbD459, 16149
.set tbD460, 16154
.set tbD461, 16159
.set tbD462, 16165
.set tbD463, 16170
.set tbD464, 16175
.set tbD465, 16180
.set tbD466, 16186
.set tbD467, 16191
.set tbD468, 16196
.set tbD469, 16201
.set tbD470, 16206
.set tbD471, 16211
.set tbD472, 16216
.set tbD473, 16221
.set tbD474, 16226
.set tbD475, 16231
.set tbD476, 16236
.set tbD477, 16241
.set tbD478, 16246
.set tbD479, 16251
.set tbD480, 16256
.set tbD481, 16258
.set tbD482, 16261
.set tbD483, 16263
.set tbD484, 16266
.set tbD485, 16268
.set tbD486, 16270
.set tbD487, 16273
.set tbD488, 16275
.set tbD489, 16277
.set tbD490, 16279
.set tbD491, 16282
.set tbD492, 16284
.set tbD493, 16286
.set tbD494, 16288
.set tbD495, 16290
.set tbD496, 16293
.set tbD497, 16295
.set tbD498, 16297
.set tbD499, 16299
.set tbD500, 16301
.set tbD501, 16303
.set tbD502, 16305
.set tbD503, 16307
.set tbD504, 16309
.set tbD505, 16311
.set tbD506, 16313
.set tbD507, 16315
.set tbD508, 16317
.set tbD509, 16319
.set tbD510, 16320
.set tbD511, 16322
.set tbD512, 16324
.set tbD513, 16326
.set tbD514, 16328
.set tbD515, 16329
.set tbD516, 16331
.set tbD517, 16333
.set tbD518, 16334
.set tbD519, 16336
.set tbD520, 16338
.set tbD521, 16339
.set tbD522, 16341
.set tbD523, 16342
.set tbD524, 16344
.set tbD525, 16345
.set tbD526, 16347
.set tbD527, 16348
.set tbD528, 16350
.set tbD529, 16351
.set tbD530, 16352
.set tbD531, 16354
.set tbD532, 16355
.set tbD533, 16356
.set tbD534, 16358
.set tbD535, 16359
.set tbD536, 16360
.set tbD537, 16361
.set tbD538, 16362
.set tbD539, 16363
.set tbD540, 16365
.set tbD541, 16366
.set tbD542, 16367
.set tbD543, 16368
.set tbD544, 16369
.set tbD545, 16370
.set tbD546, 16370
.set tbD547, 16371
.set tbD548, 16372
.set tbD549, 16373
.set tbD550, 16374
.set tbD551, 16375
.set tbD552, 16375
.set tbD553, 16376
.set tbD554, 16377
.set tbD555, 16377
.set tbD556, 16378
.set tbD557, 16379
.set tbD558, 16379
.set tbD559, 16380
.set tbD560, 16380
.set tbD561, 16381
.set tbD562, 16381
.set tbD563, 16381
.set tbD564, 16382
.set tbD565, 16382
.set tbD566, 16382
.set tbD567, 16383
.set tbD568, 16383
.set tbD569, 16383
.set tbD570, 16383
.set tbD571, 16384
.set tbD572, 16384
.set tbD573, 16384
.set tbD574, 16384
.set tbD575, 16384
.set tbM0, 0
.set tbM1, 48307
.set tbM2, 48435
.set tbM3, 48518
.set tbM4, 48563
.set tbM5, 48607
.set tbM6, 48646
.set tbM7, 48668
.set tbM8, 48690
.set tbM9, 48713
.set tbM10, 48735
.set tbM11, 48757
.set tbM12, 48774
.set tbM13, 48785
.set tbM14, 48796
.set tbM15, 48807
.set tbM16, 48818
.set tbM17, 48829
.set tbM18, 48840
.set tbM19, 48851
.set tbM20, 48862
.set tbM21, 48873
.set tbM22, 48883
.set tbM23, 48894
.set tbM24, 48901
.set tbM25, 48906
.set tbM26, 48911
.set tbM27, 48917
.set tbM28, 48922
.set tbM29, 48927
.set tbM30, 48933
.set tbM31, 48938
.set tbM32, 48943
.set tbM33, 48948
.set tbM34, 48954
.set tbM35, 48959
.set tbM36, 48964
.set tbM37, 48969
.set tbM38, 48974
.set tbM39, 48979
.set tbM40, 48984
.set tbM41, 48989
.set tbM42, 48994
.set tbM43, 48999
.set tbM44, 49004
.set tbM45, 49009
.set tbM46, 49014
.set tbM47, 49019
.set tbM48, 49024
.set tbM49, 49026
.set tbM50, 49029
.set tbM51, 49031
.set tbM52, 49034
.set tbM53, 49036
.set tbM54, 49038
.set tbM55, 49041
.set tbM56, 49043
.set tbM57, 49045
.set tbM58, 49047
.set tbM59, 49050
.set tbM60, 49052
.set tbM61, 49054
.set tbM62, 49056
.set tbM63, 49058
.set tbM64, 49061
.set tbM65, 49063
.set tbM66, 49065
.set tbM67, 49067
.set tbM68, 49069
.set tbM69, 49071
.set tbM70, 49073
.set tbM71, 49075
.set tbM72, 49077
.set tbM73, 49079
.set tbM74, 49081
.set tbM75, 49083
.set tbM76, 49085
.set tbM77, 49087
.set tbM78, 49088
.set tbM79, 49090
.set tbM80, 49092
.set tbM81, 49094
.set tbM82, 49096
.set tbM83, 49097
.set tbM84, 49099
.set tbM85, 49101
.set tbM86, 49102
.set tbM87, 49104
.set tbM88, 49106
.set tbM89, 49107
.set tbM90, 49109
.set tbM91, 49110
.set tbM92, 49112
.set tbM93, 49113
.set tbM94, 49115
.set tbM95, 49116
.set tbM96, 49118
.set tbM97, 49119
.set tbM98, 49120
.set tbM99, 49122
.set tbM100, 49123
.set tbM101, 49124
.set tbM102, 49126
.set tbM103, 49127
.set tbM104, 49128
.set tbM105, 49129
.set tbM106, 49130
.set tbM107, 49131
.set tbM108, 49133
.set tbM109, 49134
.set tbM110, 49135
.set tbM111, 49136
.set tbM112, 49137
.set tbM113, 49138
.set tbM114, 49138
.set tbM115, 49139
.set tbM116, 49140
.set tbM117, 49141
.set tbM118, 49142
.set tbM119, 49143
.set tbM120, 49143
.set tbM121, 49144
.set tbM122, 49145
.set tbM123, 49145
.set tbM124, 49146
.set tbM125, 49147
.set tbM126, 49147
.set tbM127, 49148
.set tbM128, 49148
.set tbM129, 49149
.set tbM130, 49149
.set tbM131, 49149
.set tbM132, 49150
.set tbM133, 49150
.set tbM134, 49150
.set tbM135, 49151
.set tbM136, 49151
.set tbM137, 49151
.set tbM138, 49151
.set tbM139, 49152
.set tbM140, 49152
.set tbM141, 49152
.set tbM142, 49152
.set tbM143, 49152
.set tbM144, 49152
.set tbM145, 49152
.set tbM146, 49152
.set tbM147, 49152
.set tbM148, 49152
.set tbM149, 49152
.set tbM150, 49151
.set tbM151, 49151
.set tbM152, 49151
.set tbM153, 49151
.set tbM154, 49150
.set tbM155, 49150
.set tbM156, 49150
.set tbM157, 49149
.set tbM158, 49149
.set tbM159, 49149
.set tbM160, 49148
.set tbM161, 49148
.set tbM162, 49147
.set tbM163, 49147
.set tbM164, 49146
.set tbM165, 49145
.set tbM166, 49145
.set tbM167, 49144
.set tbM168, 49143
.set tbM169, 49143
.set tbM170, 49142
.set tbM171, 49141
.set tbM172, 49140
.set tbM173, 49139
.set tbM174, 49138
.set tbM175, 49138
.set tbM176, 49137
.set tbM177, 49136
.set tbM178, 49135
.set tbM179, 49134
.set tbM180, 49133
.set tbM181, 49131
.set tbM182, 49130
.set tbM183, 49129
.set tbM184, 49128
.set tbM185, 49127
.set tbM186, 49126
.set tbM187, 49124
.set tbM188, 49123
.set tbM189, 49122
.set tbM190, 49120
.set tbM191, 49119
.set tbM192, 49118
.set tbM193, 49116
.set tbM194, 49115
.set tbM195, 49113
.set tbM196, 49112
.set tbM197, 49110
.set tbM198, 49109
.set tbM199, 49107
.set tbM200, 49106
.set tbM201, 49104
.set tbM202, 49102
.set tbM203, 49101
.set tbM204, 49099
.set tbM205, 49097
.set tbM206, 49096
.set tbM207, 49094
.set tbM208, 49092
.set tbM209, 49090
.set tbM210, 49088
.set tbM211, 49087
.set tbM212, 49085
.set tbM213, 49083
.set tbM214, 49081
.set tbM215, 49079
.set tbM216, 49077
.set tbM217, 49075
.set tbM218, 49073
.set tbM219, 49071
.set tbM220, 49069
.set tbM221, 49067
.set tbM222, 49065
.set tbM223, 49063
.set tbM224, 49061
.set tbM225, 49058
.set tbM226, 49056
.set tbM227, 49054
.set tbM228, 49052
.set tbM229, 49050
.set tbM230, 49047
.set tbM231, 49045
.set tbM232, 49043
.set tbM233, 49041
.set tbM234, 49038
.set tbM235, 49036
.set tbM236, 49034
.set tbM237, 49031
.set tbM238, 49029
.set tbM239, 49026
.set tbM240, 49024
.set tbM241, 49019
.set tbM242, 49014
.set tbM243, 49009
.set tbM244, 49004
.set tbM245, 48999
.set tbM246, 48994
.set tbM247, 48989
.set tbM248, 48984
.set tbM249, 48979
.set tbM250, 48974
.set tbM251, 48969
.set tbM252, 48964
.set tbM253, 48959
.set tbM254, 48954
.set tbM255, 48948
.set tbM256, 48943
.set tbM257, 48938
.set tbM258, 48933
.set tbM259, 48927
.set tbM260, 48922
.set tbM261, 48917
.set tbM262, 48911
.set tbM263, 48906
.set tbM264, 48901
.set tbM265, 48894
.set tbM266, 48883
.set tbM267, 48873
.set tbM268, 48862
.set tbM269, 48851
.set tbM270, 48840
.set tbM271, 48829
.set tbM272, 48818
.set tbM273, 48807
.set tbM274, 48796
.set tbM275, 48785
.set tbM276, 48774
.set tbM277, 48757
.set tbM278, 48735
.set tbM279, 48713
.set tbM280, 48690
.set tbM281, 48668
.set tbM282, 48646
.set tbM283, 48607
.set tbM284, 48563
.set tbM285, 48518
.set tbM286, 48435
.set tbM287, 48307
.set tbM288, 0
.set tbM289, 15539
.set tbM290, 15667
.set tbM291, 15750
.set tbM292, 15795
.set tbM293, 15839
.set tbM294, 15878
.set tbM295, 15900
.set tbM296, 15922
.set tbM297, 15945
.set tbM298, 15967
.set tbM299, 15989
.set tbM300, 16006
.set tbM301, 16017
.set tbM302, 16028
.set tbM303, 16039
.set tbM304, 16050
.set tbM305, 16061
.set tbM306, 16072
.set tbM307, 16083
.set tbM308, 16094
.set tbM309, 16105
.set tbM310, 16115
.set tbM311, 16126
.set tbM312, 16133
.set tbM313, 16138
.set tbM314, 16143
.set tbM315, 16149
.set tbM316, 16154
.set tbM317, 16159
.set tbM318, 16165
.set tbM319, 16170
.set tbM320, 16175
.set tbM321, 16180
.set tbM322, 16186
.set tbM323, 16191
.set tbM324, 16196
.set tbM325, 16201
.set tbM326, 16206
.set tbM327, 16211
.set tbM328, 16216
.set tbM329, 16221
.set tbM330, 16226
.set tbM331, 16231
.set tbM332, 16236
.set tbM333, 16241
.set tbM334, 16246
.set tbM335, 16251
.set tbM336, 16256
.set tbM337, 16258
.set tbM338, 16261
.set tbM339, 16263
.set tbM340, 16266
.set tbM341, 16268
.set tbM342, 16270
.set tbM343, 16273
.set tbM344, 16275
.set tbM345, 16277
.set tbM346, 16279
.set tbM347, 16282
.set tbM348, 16284
.set tbM349, 16286
.set tbM350, 16288
.set tbM351, 16290
.set tbM352, 16293
.set tbM353, 16295
.set tbM354, 16297
.set tbM355, 16299
.set tbM356, 16301
.set tbM357, 16303
.set tbM358, 16305
.set tbM359, 16307
.set tbM360, 16309
.set tbM361, 16311
.set tbM362, 16313
.set tbM363, 16315
.set tbM364, 16317
.set tbM365, 16319
.set tbM366, 16320
.set tbM367, 16322
.set tbM368, 16324
.set tbM369, 16326
.set tbM370, 16328
.set tbM371, 16329
.set tbM372, 16331
.set tbM373, 16333
.set tbM374, 16334
.set tbM375, 16336
.set tbM376, 16338
.set tbM377, 16339
.set tbM378, 16341
.set tbM379, 16342
.set tbM380, 16344
.set tbM381, 16345
.set tbM382, 16347
.set tbM383, 16348
.set tbM384, 16350
.set tbM385, 16351
.set tbM386, 16352
.set tbM387, 16354
.set tbM388, 16355
.set tbM389, 16356
.set tbM390, 16358
.set tbM391, 16359
.set tbM392, 16360
.set tbM393, 16361
.set tbM394, 16362
.set tbM395, 16363
.set tbM396, 16365
.set tbM397, 16366
.set tbM398, 16367
.set tbM399, 16368
.set tbM400, 16369
.set tbM401, 16370
.set tbM402, 16370
.set tbM403, 16371
.set tbM404, 16372
.set tbM405, 16373
.set tbM406, 16374
.set tbM407, 16375
.set tbM408, 16375
.set tbM409, 16376
.set tbM410, 16377
.set tbM411, 16377
.set tbM412, 16378
.set tbM413, 16379
.set tbM414, 16379
.set tbM415, 16380
.set tbM416, 16380
.set tbM417, 16381
.set tbM418, 16381
.set tbM419, 16381
.set tbM420, 16382
.set tbM421, 16382
.set tbM422, 16382
.set tbM423, 16383
.set tbM424, 16383
.set tbM425, 16383
.set tbM426, 16383
.set tbM427, 16384
.set tbM428, 16384
.set tbM429, 16384
.set tbM430, 16384
.set tbM431, 16384
.set tbM432, 16384
.set tbM433, 16384
.set tbM434, 16384
.set tbM435, 16384
.set tbM436, 16384
.set tbM437, 16384
.set tbM438, 16383
.set tbM439, 16383
.set tbM440, 16383
.set tbM441, 16383
.set tbM442, 16382
.set tbM443, 16382
.set tbM444, 16382
.set tbM445, 16381
.set tbM446, 16381
.set tbM447, 16381
.set tbM448, 16380
.set tbM449, 16380
.set tbM450, 16379
.set tbM451, 16379
.set tbM452, 16378
.set tbM453, 16377
.set tbM454, 16377
.set tbM455, 16376
.set tbM456, 16375
.set tbM457, 16375
.set tbM458, 16374
.set tbM459, 16373
.set tbM460, 16372
.set tbM461, 16371
.set tbM462, 16370
.set tbM463, 16370
.set tbM464, 16369
.set tbM465, 16368
.set tbM466, 16367
.set tbM467, 16366
.set tbM468, 16365
.set tbM469, 16363
.set tbM470, 16362
.set tbM471, 16361
.set tbM472, 16360
.set tbM473, 16359
.set tbM474, 16358
.set tbM475, 16356
.set tbM476, 16355
.set tbM477, 16354
.set tbM478, 16352
.set tbM479, 16351
.set tbM480, 16350
.set tbM481, 16348
.set tbM482, 16347
.set tbM483, 16345
.set tbM484, 16344
.set tbM485, 16342
.set tbM486, 16341
.set tbM487, 16339
.set tbM488, 16338
.set tbM489, 16336
.set tbM490, 16334
.set tbM491, 16333
.set tbM492, 16331
.set tbM493, 16329
.set tbM494, 16328
.set tbM495, 16326
.set tbM496, 16324
.set tbM497, 16322
.set tbM498, 16320
.set tbM499, 16319
.set tbM500, 16317
.set tbM501, 16315
.set tbM502, 16313
.set tbM503, 16311
.set tbM504, 16309
.set tbM505, 16307
.set tbM506, 16305
.set tbM507, 16303
.set tbM508, 16301
.set tbM509, 16299
.set tbM510, 16297
.set tbM511, 16295
.set tbM512, 16293
.set tbM513, 16290
.set tbM514, 16288
.set tbM515, 16286
.set tbM516, 16284
.set tbM517, 16282
.set tbM518, 16279
.set tbM519, 16277
.set tbM520, 16275
.set tbM521, 16273
.set tbM522, 16270
.set tbM523, 16268
.set tbM524, 16266
.set tbM525, 16263
.set tbM526, 16261
.set tbM527, 16258
.set tbM528, 16256
.set tbM529, 16251
.set tbM530, 16246
.set tbM531, 16241
.set tbM532, 16236
.set tbM533, 16231
.set tbM534, 16226
.set tbM535, 16221
.set tbM536, 16216
.set tbM537, 16211
.set tbM538, 16206
.set tbM539, 16201
.set tbM540, 16196
.set tbM541, 16191
.set tbM542, 16186
.set tbM543, 16180
.set tbM544, 16175
.set tbM545, 16170
.set tbM546, 16165
.set tbM547, 16159
.set tbM548, 16154
.set tbM549, 16149
.set tbM550, 16143
.set tbM551, 16138
.set tbM552, 16133
.set tbM553, 16126
.set tbM554, 16115
.set tbM555, 16105
.set tbM556, 16094
.set tbM557, 16083
.set tbM558, 16072
.set tbM559, 16061
.set tbM560, 16050
.set tbM561, 16039
.set tbM562, 16028
.set tbM563, 16017
.set tbM564, 16006
.set tbM565, 15989
.set tbM566, 15967
.set tbM567, 15945
.set tbM568, 15922
.set tbM569, 15900
.set tbM570, 15878
.set tbM571, 15839
.set tbM572, 15795
.set tbM573, 15750
.set tbM574, 15667
.set tbM575, 15539
.macro tbE1 idx
.short tbC\idx, tbN\idx
.endm
.macro tbEC idx
.short tbC\idx
.endm
.macro tbED idx
.short tbD\idx
.endm
.macro tbEM idx
.short tbM\idx
.endm
.p2align 8
.protected spec_T1
.globl spec_T1
.type spec_T1,@object
spec_T1:
.set tbj, 0
.rept 16
.set tbd, 0
.rept 576
.set tba, 0
.rept 18
tbE1 %(((18*tbj+tba)*tbd) % 576)
.set tba, tba+1
.endr
.if tbj == 0
tbE1 %((288*tbd) % 576)
.else
.short 0,0
.endif
.fill 13,4,0
.set tbd, tbd+1
.endr
.set tbj, tbj+1
.endr
.size spec_T1, 1179648
.p2align 8
.protected spec_T3
.globl spec_T3
.type spec_T3,@object
spec_T3:
.set tbc, 0
.rept 9
.set tbp, 0
.rept 2
.set tbk, 0
.rept 18
.set tbl, 0
.rept 64
.set tbd, 32*tbc + (tbl & 31)
.set tbu, 2*(8*tbk + 4*(tbl >> 5)) + tbp
.if tbu == 0
.short tbC0
tbEC %((288*tbd) % 576)
.else
tbED %((tbu*tbd) % 576)
tbEM %((tbu*tbd) % 576)
.endif
tbED %(((tbu+2)*tbd) % 576)
tbEM %(((tbu+2)*tbd) % 576)
tbED %(((tbu+4)*tbd) % 576)
tbEM %(((tbu+4)*tbd) % 576)
tbED %(((tbu+6)*tbd) % 576)
tbEM %(((tbu+6)*tbd) % 576)
.set tbl, tbl+1
.endr
.set tbk, tbk+1
.endr
.set tbp, tbp+1
.endr
.set tbc, tbc+1
.endr
.size spec_T3, 331776
.p2align 8
.protected spec_S2
.globl spec_S2
.type spec_S2,@object
spec_S2:
.short 16256,16256,16256,16256,16256,16256,16256,16256,16256,16255,16251,16245,16237,16226,16213,16198
.short 16256,16251,16237,16213,16181,16142,16068,15944,16256,16245,16213,16162,16068,15817,48712,48881
.short 16256,16237,16181,16068,0,48836,48949,49005,16256,16226,16142,15817,48836,48966,49019,49013
.short 16256,16213,16068,48712,48949,49019,49005,48910,16256,16198,15944,48881,49005,49013,48910,15817
.short 16256,16181,0,48949,49024,48949,0,16181,16256,16162,48712,48994,49005,48789,16142,16255
.short 16256,16142,48836,49019,48949,15944,16237,16213,16256,16113,48910,49023,48836,16162,16251,16021
.short 16256,16068,48949,49005,0,16237,16181,48836,16256,16021,48981,48966,16068,16255,15944,48994
.short 16256,15944,49005,48910,16181,16213,48836,49019,16256,15817,49019,48789,16237,16113,48981,48930
.short 16256,16256,16256,16256,16256,16256,16256,16256,16181,16162,16142,16113,16068,16021,15944,15817
.short 0,48712,48836,48910,48949,48981,49005,49019,48949,48994,49019,49023,49005,48966,48910,48789
.short 49024,49005,48949,48836,0,16068,16181,16237,48949,48789,15944,16162,16237,16255,16213,16113
.short 0,16142,16237,16251,16181,15944,48836,48981,16181,16255,16213,16021,48836,48994,49019,48930
.short 16256,16181,0,48949,49024,48949,0,16181,16181,48585,48981,49013,48836,16113,16251,16198
.short 0,48981,49005,48712,16181,16251,16068,48910,48949,49013,48712,16198,16237,15817,48981,48994
.short 49024,48836,16181,16237,0,49005,48949,16068,48949,16113,16251,15817,49005,48930,16142,16245
.short 0,16251,16068,48981,48949,16142,16237,48712,16181,16198,48910,48994,16068,16245,48712,49023
.short 16256,16256,16256,16256,16256,16256,16256,16256,0,48585,48712,48789,48836,48881,48910,48930
.short 49024,49019,49005,48981,48949,48910,48836,48712,0,16021,16142,16198,16237,16255,16251,16226
.short 16256,16237,16181,16068,0,48836,48949,49005,0,48881,48981,49023,49005,48930,48712,16021
.short 49024,48981,48836,15944,16181,16251,16237,16142,0,16162,16251,16226,16068,48789,48981,49023
.short 16256,16181,0,48949,49024,48949,0,16181,0,48966,49019,48881,16068,16245,16213,15817
.short 49024,48910,16068,16251,16181,48712,49005,48981,0,16226,16213,48585,49005,48966,15944,16245
.short 16256,16068,48949,49005,0,16237,16181,48836,0,49013,48910,16162,16237,48585,49019,48881
.short 49024,48712,16237,16142,48949,48981,16068,16251,0,16255,15944,49013,48836,16226,16142,48966
.short 16256,16256,16256,16256,16256,16256,16256,16256,48949,48966,48981,48994,49005,49013,49019,49023
.short 0,15944,16068,16142,16181,16213,16237,16251,16181,16113,15944,48585,48836,48930,48981,49013
.short 49024,49005,48949,48836,0,16068,16181,16237,16181,16245,16251,16198,16068,48585,48910,48994
.short 0,48910,49005,49019,48949,48712,16068,16213,48949,48585,16142,16245,16237,16113,48712,48966
.short 16256,16181,0,48949,49024,48949,0,16181,48949,49023,48910,16021,16237,16226,15944,48930
.short 0,16213,16237,15944,48949,49019,48836,16142,16181,48789,49019,48930,16068,16255,16142,48881
.short 49024,48836,16181,16237,0,49005,48949,16068,16181,16226,48712,49023,48836,16198,16213,48789
.short 0,49019,48836,16213,16181,48910,49005,15944,48949,16162,16213,48881,49005,16021,16251,48585
.short 0,0,0,0,0,0,0,0,0,15817,15944,16021,16068,16113,16142,16162
.short 0,15944,16068,16142,16181,16213,16237,16251,0,16021,16142,16198,16237,16255,16251,16226
.short 0,16068,16181,16237,16256,16237,16181,16068,0,16113,16213,16255,16237,16162,15944,48789
.short 0,16142,16237,16251,16181,15944,48836,48981,0,16162,16251,16226,16068,48789,48981,49023
.short 0,16181,16256,16181,0,48949,49024,48949,0,16198,16251,16113,48836,49013,48981,48585
.short 0,16213,16237,15944,48949,49019,48836,16142,0,16226,16213,48585,49005,48966,15944,16245
.short 0,16237,16181,48836,49024,48836,16181,16237,0,16245,16142,48930,49005,15817,16251,16113
.short 0,16251,16068,48981,48949,16142,16237,48712,0,16255,15944,49013,48836,16226,16142,48966
.short 0,0,0,0,0,0,0,0,16181,16198,16213,16226,16237,16245,16251,16255
.short 16256,16251,16237,16213,16181,16142,16068,15944,16181,16113,15944,48585,48836,48930,48981,49013
.short 0,48836,48949,49005,49024,49005,48949,48836,48949,49013,49019,48966,48836,15817,16142,16226
.short 49024,48981,48836,15944,16181,16251,16237,16142,48949,48585,16142,16245,16237,16113,48712,48966
.short 0,16181,16256,16181,0,48949,49024,48949,16181,16255,16142,48789,49005,48994,48712,16162
.short 16256,16142,48836,49019,48949,15944,16237,16213,16181,48789,49019,48930,16068,16255,16142,48881
.short 0,49005,48949,16068,16256,16068,48949,49005,48949,48994,15944,16255,16068,48966,48981,16021
.short 49024,48712,16237,16142,48949,48981,16068,16251,48949,16162,16213,48881,49005,16021,16251,48585
.short 0,0,0,0,0,0,0,0,16256,16255,16251,16245,16237,16226,16213,16198
.short 0,48712,48836,48910,48949,48981,49005,49019,49024,49013,48981,48930,48836,48585,15944,16113
.short 0,16068,16181,16237,16256,16237,16181,16068,16256,16226,16142,15817,48836,48966,49019,49013
.short 0,48910,49005,49019,48949,48712,16068,16213,49024,48966,48712,16113,16237,16245,16142,48585
.short 0,16181,16256,16181,0,48949,49024,48949,16256,16162,48712,48994,49005,48789,16142,16255
.short 0,48981,49005,48712,16181,16251,16068,48910,49024,48881,16142,16255,16068,48930,49019,48789
.short 0,16237,16181,48836,49024,48836,16181,16237,16256,16021,48981,48966,16068,16255,15944,48994
.short 0,49019,48836,16213,16181,48910,49005,15944,49024,48585,16251,16021,49005,48881,16213,16162
.short 0,0,0,0,0,0,0,0,16181,16162,16142,16113,16068,16021,15944,15817
.short 49024,49019,49005,48981,48949,48910,48836,48712,16181,16226,16251,16255,16237,16198,16142,16021
.short 0,48836,48949,49005,49024,49005,48949,48836,48949,48789,15944,16162,16237,16255,16213,16113
.short 16256,16213,16068,48712,48949,49019,49005,48910,48949,49023,48981,48789,16068,16226,16251,16162
.short 0,16181,16256,16181,0,48949,49024,48949,16181,48585,48981,49013,48836,16113,16251,16198
.short 49024,48910,16068,16251,16181,48712,49005,48981,16181,16245,15944,48966,49005,48585,16213,16226
.short 0,49005,48949,16068,16256,16068,48949,49005,48949,16113,16251,15817,49005,48930,16142,16245
.short 16256,15944,49005,48910,16181,16213,48836,49019,48949,48966,16142,16226,48836,49013,15944,16255
.short 16256,16256,16256,16256,16256,16256,16256,16256,49024,49023,49019,49013,49005,48994,48981,48966
.short 16256,16251,16237,16213,16181,16142,16068,15944,49024,49013,48981,48930,48836,48585,15944,16113
.short 16256,16237,16181,16068,0,48836,48949,49005,49024,48994,48910,48585,16068,16198,16251,16245
.short 16256,16213,16068,48712,48949,49019,49005,48910,49024,48966,48712,16113,16237,16245,16142,48585
.short 16256,16181,0,48949,49024,48949,0,16181,49024,48930,15944,16226,16237,16021,48910,49023
.short 16256,16142,48836,49019,48949,15944,16237,16213,49024,48881,16142,16255,16068,48930,49019,48789
.short 16256,16068,48949,49005,0,16237,16181,48836,49024,48789,16213,16198,48836,49023,48712,16226
.short 16256,15944,49005,48910,16181,16213,48836,49019,49024,48585,16251,16021,49005,48881,16213,16162
.short 16256,16256,16256,16256,16256,16256,16256,16256,48949,48930,48910,48881,48836,48789,48712,48585
.short 0,48712,48836,48910,48949,48981,49005,49019,16181,16226,16251,16255,16237,16198,16142,16021
.short 49024,49005,48949,48836,0,16068,16181,16237,16181,16021,48712,48930,49005,49023,48981,48881
.short 0,16142,16237,16251,16181,15944,48836,48981,48949,49023,48981,48789,16068,16226,16251,16162
.short 16256,16181,0,48949,49024,48949,0,16181,48949,15817,16213,16245,16068,48881,49019,48966
.short 0,48981,49005,48712,16181,16251,16068,48910,16181,16245,15944,48966,49005,48585,16213,16226
.short 49024,48836,16181,16237,0,49005,48949,16068,16181,48881,49019,48585,16237,16162,48910,49013
.short 0,16251,16068,48981,48949,16142,16237,48712,48949,48966,16142,16226,48836,49013,15944,16255
.short 16256,16256,16256,16256,16256,16256,16256,16256,0,15817,15944,16021,16068,16113,16142,16162
.short 49024,49019,49005,48981,48949,48910,48836,48712,0,48789,48910,48966,49005,49023,49019,48994
.short 16256,16237,16181,16068,0,48836,48949,49005,0,16113,16213,16255,16237,16162,15944,48789
.short 49024,48981,48836,15944,16181,16251,16237,16142,0,48930,49019,48994,48836,16021,16213,16255
.short 16256,16181,0,48949,49024,48949,0,16181,0,16198,16251,16113,48836,49013,48981,48585
.short 49024,48910,16068,16251,16181,48712,49005,48981,0,48994,48981,15817,16237,16198,48712,49013
.short 16256,16068,48949,49005,0,16237,16181,48836,0,16245,16142,48930,49005,15817,16251,16113
.short 49024,48712,16237,16142,48949,48981,16068,16251,0,49023,48712,16245,16068,48994,48910,16198
.short 16256,16256,16256,16256,16256,16256,16256,16256,16181,16198,16213,16226,16237,16245,16251,16255
.short 0,15944,16068,16142,16181,16213,16237,16251,48949,48881,48712,15817,16068,16162,16213,16245
.short 49024,49005,48949,48836,0,16068,16181,16237,48949,49013,49019,48966,48836,15817,16142,16226
.short 0,48910,49005,49019,48949,48712,16068,16213,16181,15817,48910,49013,49005,48881,15944,16198
.short 16256,16181,0,48949,49024,48949,0,16181,16181,16255,16142,48789,49005,48994,48712,16162
.short 0,16213,16237,15944,48949,49019,48836,16142,48949,16021,16251,16162,48836,49023,48910,16113
.short 49024,48836,16181,16237,0,49005,48949,16068,48949,48994,15944,16255,16068,48966,48981,16021
.short 0,49019,48836,16213,16181,48910,49005,15944,16181,48930,48981,16113,16237,48789,49019,15817
.short 0,0,0,0,0,0,0,0,0,48585,48712,48789,48836,48881,48910,48930
.short 0,15944,16068,16142,16181,16213,16237,16251,0,48789,48910,48966,49005,49023,49019,48994
.short 0,16068,16181,16237,16256,16237,16181,16068,0,48881,48981,49023,49005,48930,48712,16021
.short 0,16142,16237,16251,16181,15944,48836,48981,0,48930,49019,48994,48836,16021,16213,16255
.short 0,16181,16256,16181,0,48949,49024,48949,0,48966,49019,48881,16068,16245,16213,15817
.short 0,16213,16237,15944,48949,49019,48836,16142,0,48994,48981,15817,16237,16198,48712,49013
.short 0,16237,16181,48836,49024,48836,16181,16237,0,49013,48910,16162,16237,48585,49019,48881
.short 0,16251,16068,48981,48949,16142,16237,48712,0,49023,48712,16245,16068,48994,48910,16198
.short 0,0,0,0,0,0,0,0,48949,48966,48981,48994,49005,49013,49019,49023
.short 16256,16251,16237,16213,16181,16142,16068,15944,48949,48881,48712,15817,16068,16162,16213,16245
.short 0,48836,48949,49005,49024,49005,48949,48836,16181,16245,16251,16198,16068,48585,48910,48994
.short 49024,48981,48836,15944,16181,16251,16237,16142,16181,15817,48910,49013,49005,48881,15944,16198
.short 0,16181,16256,16181,0,48949,49024,48949,48949,49023,48910,16021,16237,16226,15944,48930
.short 16256,16142,48836,49019,48949,15944,16237,16213,48949,16021,16251,16162,48836,49023,48910,16113
.short 0,49005,48949,16068,16256,16068,48949,49005,16181,16226,48712,49023,48836,16198,16213,48789
.short 49024,48712,16237,16142,48949,48981,16068,16251,16181,48930,48981,16113,16237,48789,49019,15817
.short 0,0,0,0,0,0,0,0,49024,49023,49019,49013,49005,48994,48981,48966
.short 0,48712,48836,48910,48949,48981,49005,49019,16256,16245,16213,16162,16068,15817,48712,48881
.short 0,16068,16181,16237,16256,16237,16181,16068,49024,48994,48910,48585,16068,16198,16251,16245
.short 0,48910,49005,49019,48949,48712,16068,16213,16256,16198,15944,48881,49005,49013,48910,15817
.short 0,16181,16256,16181,0,48949,49024,48949,49024,48930,15944,16226,16237,16021,48910,49023
.short 0,48981,49005,48712,16181,16251,16068,48910,16256,16113,48910,49023,48836,16162,16251,16021
.short 0,16237,16181,48836,49024,48836,16181,16237,49024,48789,16213,16198,48836,49023,48712,16226
.short 0,49019,48836,16213,16181,48910,49005,15944,16256,15817,49019,48789,16237,16113,48981,48930
.short 0,0,0,0,0,0,0,0,48949,48930,48910,48881,48836,48789,48712,48585
.short 49024,49019,49005,48981,48949,48910,48836,48712,48949,48994,49019,49023,49005,48966,48910,48789
.short 0,48836,48949,49005,49024,49005,48949,48836,16181,16021,48712,48930,49005,49023,48981,48881
.short 16256,16213,16068,48712,48949,49019,49005,48910,16181,16255,16213,16021,48836,48994,49019,48930
.short 0,16181,16256,16181,0,48949,49024,48949,48949,15817,16213,16245,16068,48881,49019,48966
.short 49024,48910,16068,16251,16181,48712,49005,48981,48949,49013,48712,16198,16237,15817,48981,48994
.short 0,49005,48949,16068,16256,16068,48949,49005,16181,48881,49019,48585,16237,16162,48910,49013
.short 16256,15944,49005,48910,16181,16213,48836,49019,16181,16198,48910,48994,16068,16245,48712,49023
.short 16256,0,49024,0,16256,0,49024,0,16256,48585,49019,16021,16237,48881,48981,16162
.short 16256,48712,49005,16142,16181,48981,48836,16251,16256,48789,48981,16198,16068,49023,15944,16226
.short 16256,48836,48949,16237,0,49005,16181,16068,16256,48881,48910,16255,48836,48930,16251,48789
.short 16256,48910,48836,16251,48949,48712,16237,48981,16256,48930,48712,16226,49005,16021,16142,49023
.short 16256,48949,0,16181,49024,16181,0,48949,16256,48966,15944,16113,49005,16245,48910,48585
.short 16256,48981,16068,15944,48949,16251,49005,16142,16256,48994,16142,48585,48836,16198,49019,16245
.short 16256,49005,16181,48836,0,16068,48949,16237,16256,49013,16213,48930,16068,48585,48712,16113
.short 16256,49019,16237,48981,16181,48910,16068,48712,16256,49023,16251,49013,16237,48994,16213,48966
.short 16256,0,49024,0,16256,0,49024,0,16181,48966,48910,16226,16068,49013,48712,16255
.short 0,49019,16068,16213,48949,48910,16237,15944,48949,48881,16251,48585,49005,16162,16142,49013
.short 49024,16068,16181,49005,0,16237,48949,48836,48949,16245,48712,48966,16237,48585,48981,16226
.short 0,16213,49005,15944,16181,49019,16068,16142,16181,15817,48981,16245,48836,48881,16251,48966
.short 16256,48949,0,16181,49024,16181,0,48949,16181,49023,16213,48789,48836,16226,49019,16162
.short 0,48910,16237,49019,16181,48712,48836,16213,48949,16021,15944,48930,16237,49023,16213,48881
.short 49024,16237,48949,16068,0,48836,16181,49005,48949,16226,49019,16255,49005,16198,48910,16021
.short 0,15944,48836,16142,48949,16213,49005,16251,16181,48930,16142,48881,16068,48789,15944,48585
.short 16256,0,49024,0,16256,0,49024,0,0,49023,15944,16245,48836,48994,16142,16198
.short 49024,15944,16237,48910,48949,16213,16068,49019,0,16245,48910,48930,16237,15817,49019,16113
.short 16256,48836,48949,16237,0,49005,16181,16068,0,48994,16213,15817,49005,16198,15944,49013
.short 49024,16142,16068,49019,16181,15944,49005,16213,0,16198,49019,16113,16068,49013,16213,48585
.short 16256,48949,0,16181,49024,16181,0,48949,0,48930,16251,48994,16068,16021,48981,16255
.short 49024,16213,48836,48712,16181,49019,16237,48910,0,16113,48981,16255,49005,16162,48712,48789
.short 16256,49005,16181,48836,0,16068,48949,16237,0,48789,16142,48966,16237,49023,16251,48994
.short 49024,16251,49005,16213,48949,16142,48836,15944,0,15817,48712,16021,48836,16113,48910,16162
.short 16256,0,49024,0,16256,0,49024,0,48949,48930,16213,16113,49005,48789,16251,15817
.short 0,16251,48836,48981,16181,16142,49005,48712,16181,48994,48712,16255,48836,48966,16213,16021
.short 49024,16068,16181,49005,0,16237,48949,48836,16181,16021,49019,16162,16068,49023,16142,16113
.short 0,48981,16237,48712,48949,16251,48836,48910,48949,16255,48910,48789,16237,48994,15944,16162
.short 16256,48949,0,16181,49024,16181,0,48949,48949,15817,16142,49013,16237,48881,48712,16198
.short 0,16142,49005,16251,48949,15944,16068,48981,16181,49013,16251,48966,16068,15817,48910,16226
.short 49024,16237,48949,16068,0,48836,16181,49005,16181,48881,15944,15817,48836,16162,48981,16245
.short 0,48712,16068,48910,16181,48981,16237,49019,48949,16198,48981,16226,49005,16245,49019,16255
.short 0,16256,0,49024,0,16256,0,49024,0,16255,48712,49013,16068,16226,48910,48966
.short 0,16251,48836,48981,16181,16142,49005,48712,0,16245,48910,48930,16237,15817,49019,16113
.short 0,16237,48949,48836,16256,48836,48949,16237,0,16226,48981,48585,16237,48966,48712,16245
.short 0,16213,49005,15944,16181,49019,16068,16142,0,16198,49019,16113,16068,49013,16213,48585
.short 0,16181,49024,16181,0,48949,16256,48949,0,16162,49019,16226,48836,48789,16213,49023
.short 0,16142,49005,16251,48949,15944,16068,48981,0,16113,48981,16255,49005,16162,48712,48789
.short 0,16068,48949,16237,49024,16237,48949,16068,0,16021,48910,16198,49005,16255,49019,16226
.short 0,15944,48836,16142,48949,16213,49005,16251,0,15817,48712,16021,48836,16113,48910,16162
.short 0,16256,0,49024,0,16256,0,49024,16181,16162,48981,48881,16237,16021,49019,48585
.short 16256,48712,49005,16142,16181,48981,48836,16251,16181,48994,48712,16255,48836,48966,16213,16021
.short 0,49005,16181,16068,49024,16068,16181,49005,48949,48789,16251,48930,48836,16255,48910,48881
.short 49024,16142,16068,49019,16181,15944,49005,16213,48949,16255,48910,48789,16237,48994,15944,16162
.short 0,16181,49024,16181,0,48949,16256,48949,16181,48585,48910,16245,49005,16113,15944,48966
.short 16256,48981,16068,15944,48949,16251,49005,16142,16181,49013,16251,48966,16068,15817,48910,16226
.short 0,48836,16181,49005,16256,49005,16181,48836,48949,16113,48712,48585,16068,48930,16213,49013
.short 49024,16251,49005,16213,48949,16142,48836,15944,48949,16198,48981,16226,49005,16245,49019,16255
.short 0,16256,0,49024,0,16256,0,49024,16256,48585,49019,16021,16237,48881,48981,16162
.short 0,49019,16068,16213,48949,48910,16237,15944,49024,16021,16213,48966,48836,16255,48712,48994
.short 0,16237,48949,48836,16256,48836,48949,16237,16256,48881,48910,16255,48836,48930,16251,48789
.short 0,48981,16237,48712,48949,16251,48836,48910,49024,16162,15944,48994,16237,48789,48910,16255
.short 0,16181,49024,16181,0,48949,16256,48949,16256,48966,15944,16113,49005,16245,48910,48585
.short 0,48910,16237,49019,16181,48712,48836,16213,49024,16226,48910,15817,16068,48966,16251,49013
.short 0,16068,48949,16237,49024,16237,48949,16068,16256,49013,16213,48930,16068,48585,48712,16113
.short 0,48712,16068,48910,16181,48981,16237,49019,49024,16255,49019,16245,49005,16226,48981,16198
.short 0,16256,0,49024,0,16256,0,49024,16181,48966,48910,16226,16068,49013,48712,16255
.short 49024,15944,16237,48910,48949,16213,16068,49019,16181,16113,49019,15817,16237,48930,48910,16245
.short 0,49005,16181,16068,49024,16068,16181,49005,48949,16245,48712,48966,16237,48585,48981,16226
.short 16256,48910,48836,16251,48949,48712,16237,48981,48949,48585,16213,49013,16068,16113,49019,16198
.short 0,16181,49024,16181,0,48949,16256,48949,16181,49023,16213,48789,48836,16226,49019,16162
.short 49024,16213,48836,48712,16181,49019,16237,48910,16181,48789,48712,16162,49005,16255,48981,16113
.short 0,48836,16181,49005,16256,49005,16181,48836,48949,16226,49019,16255,49005,16198,48910,16021
.short 16256,49019,16237,48981,16181,48910,16068,48712,48949,16162,48910,16113,48836,16021,48712,15817
.short 16256,0,49024,0,16256,0,49024,0,49024,15817,16251,48789,49005,16113,16213,48930
.short 16256,48712,49005,16142,16181,48981,48836,16251,49024,16021,16213,48966,48836,16255,48712,48994
.short 16256,48836,48949,16237,0,49005,16181,16068,49024,16113,16142,49023,16068,16162,49019,16021
.short 16256,48910,48836,16251,48949,48712,16237,48981,49024,16162,15944,48994,16237,48789,48910,16255
.short 16256,48949,0,16181,49024,16181,0,48949,49024,16198,48712,48881,16237,49013,16142,15817
.short 16256,48981,16068,15944,48949,16251,49005,16142,49024,16226,48910,15817,16068,48966,16251,49013
.short 16256,49005,16181,48836,0,16068,48949,16237,49024,16245,48981,16162,48836,15817,15944,48881
.short 16256,49019,16237,48981,16181,48910,16068,48712,49024,16255,49019,16245,49005,16226,48981,16198
.short 16256,0,49024,0,16256,0,49024,0,48949,16198,16142,48994,48836,16245,15944,49023
.short 0,49019,16068,16213,48949,48910,16237,15944,16181,16113,49019,15817,16237,48930,48910,16245
.short 49024,16068,16181,49005,0,16237,48949,48836,16181,49013,15944,16198,49005,15817,16213,48994
.short 0,16213,49005,15944,16181,49019,16068,16142,48949,48585,16213,49013,16068,16113,49019,16198
.short 16256,48949,0,16181,49024,16181,0,48949,48949,16255,48981,16021,16068,48994,16251,48930
.short 0,48910,16237,49019,16181,48712,48836,16213,16181,48789,48712,16162,49005,16255,48981,16113
.short 49024,16237,48949,16068,0,48836,16181,49005,16181,48994,16251,49023,16237,48966,16142,48789
.short 0,15944,48836,16142,48949,16213,49005,16251,48949,16162,48910,16113,48836,16021,48712,15817
.short 16256,0,49024,0,16256,0,49024,0,0,16255,48712,49013,16068,16226,48910,48966
.short 49024,15944,16237,48910,48949,16213,16068,49019,0,49013,16142,16162,49005,48585,16251,48881
.short 16256,48836,48949,16237,0,49005,16181,16068,0,16226,48981,48585,16237,48966,48712,16245
.short 49024,16142,16068,49019,16181,15944,49005,16213,0,48966,16251,48881,48836,16245,48981,15817
.short 16256,48949,0,16181,49024,16181,0,48949,0,16162,49019,16226,48836,48789,16213,49023
.short 49024,16213,48836,48712,16181,49019,16237,48910,0,48881,16213,49023,16237,48930,15944,16021
.short 16256,49005,16181,48836,0,16068,48949,16237,0,16021,48910,16198,49005,16255,49019,16226
.short 49024,16251,49005,16213,48949,16142,48836,15944,0,48585,15944,48789,16068,48881,16142,48930
.short 16256,0,49024,0,16256,0,49024,0,16181,16162,48981,48881,16237,16021,49019,48585
.short 0,16251,48836,48981,16181,16142,49005,48712,48949,16226,15944,49023,16068,16198,48981,48789
.short 49024,16068,16181,49005,0,16237,48949,48836,48949,48789,16251,48930,48836,16255,48910,48881
.short 0,48981,16237,48712,48949,16251,48836,48910,16181,49023,16142,16021,49005,16226,48712,48930
.short 16256,48949,0,16181,49024,16181,0,48949,16181,48585,48910,16245,49005,16113,15944,48966
.short 0,16142,49005,16251,48949,15944,16068,48981,48949,16245,49019,16198,48836,48585,16142,48994
.short 49024,16237,48949,16068,0,48836,16181,49005,48949,16113,48712,48585,16068,48930,16213,49013
.short 0,48712,16068,48910,16181,48981,16237,49019,16181,48966,16213,48994,16237,49013,16251,49023
.short 0,16256,0,49024,0,16256,0,49024,0,49023,15944,16245,48836,48994,16142,16198
.short 0,16251,48836,48981,16181,16142,49005,48712,0,49013,16142,16162,49005,48585,16251,48881
.short 0,16237,48949,48836,16256,48836,48949,16237,0,48994,16213,15817,49005,16198,15944,49013
.short 0,16213,49005,15944,16181,49019,16068,16142,0,48966,16251,48881,48836,16245,48981,15817
.short 0,16181,49024,16181,0,48949,16256,48949,0,48930,16251,48994,16068,16021,48981,16255
.short 0,16142,49005,16251,48949,15944,16068,48981,0,48881,16213,49023,16237,48930,15944,16021
.short 0,16068,48949,16237,49024,16237,48949,16068,0,48789,16142,48966,16237,49023,16251,48994
.short 0,15944,48836,16142,48949,16213,49005,16251,0,48585,15944,48789,16068,48881,16142,48930
.short 0,16256,0,49024,0,16256,0,49024,48949,48930,16213,16113,49005,48789,16251,15817
.short 16256,48712,49005,16142,16181,48981,48836,16251,48949,16226,15944,49023,16068,16198,48981,48789
.short 0,49005,16181,16068,49024,16068,16181,49005,16181,16021,49019,16162,16068,49023,16142,16113
.short 49024,16142,16068,49019,16181,15944,49005,16213,16181,49023,16142,16021,49005,16226,48712,48930
.short 0,16181,49024,16181,0,48949,16256,48949,48949,15817,16142,49013,16237,48881,48712,16198
.short 16256,48981,16068,15944,48949,16251,49005,16142,48949,16245,49019,16198,48836,48585,16142,48994
.short 0,48836,16181,49005,16256,49005,16181,48836,16181,48881,15944,15817,48836,16162,48981,16245
.short 49024,16251,49005,16213,48949,16142,48836,15944,16181,48966,16213,48994,16237,49013,16251,49023
.short 0,16256,0,49024,0,16256,0,49024,49024,15817,16251,48789,49005,16113,16213,48930
.short 0,49019,16068,16213,48949,48910,16237,15944,16256,48789,48981,16198,16068,49023,15944,16226
.short 0,16237,48949,48836,16256,48836,48949,16237,49024,16113,16142,49023,16068,16162,49019,16021
.short 0,48981,16237,48712,48949,16251,48836,48910,16256,48930,48712,16226,49005,16021,16142,49023
.short 0,16181,49024,16181,0,48949,16256,48949,49024,16198,48712,48881,16237,49013,16142,15817
.short 0,48910,16237,49019,16181,48712,48836,16213,16256,48994,16142,48585,48836,16198,49019,16245
.short 0,16068,48949,16237,49024,16237,48949,16068,49024,16245,48981,16162,48836,15817,15944,48881
.short 0,48712,16068,48910,16181,48981,16237,49019,16256,49023,16251,49013,16237,48994,16213,48966
.short 0,16256,0,49024,0,16256,0,49024,48949,16198,16142,48994,48836,16245,15944,49023
.short 49024,15944,16237,48910,48949,16213,16068,49019,48949,48881,16251,48585,49005,16162,16142,49013
.short 0,49005,16181,16068,49024,16068,16181,49005,16181,49013,15944,16198,49005,15817,16213,48994
.short 16256,48910,48836,16251,48949,48712,16237,48981,16181,15817,48981,16245,48836,48881,16251,48966
.short 0,16181,49024,16181,0,48949,16256,48949,48949,16255,48981,16021,16068,48994,16251,48930
.short 49024,16213,48836,48712,16181,49019,16237,48910,48949,16021,15944,48930,16237,49023,16213,48881
.short 0,48836,16181,49005,16256,49005,16181,48836,16181,48994,16251,49023,16237,48966,16142,48789
.short 16256,49019,16237,48981,16181,48910,16068,48712,16181,48930,16142,48881,16068,48789,15944,48585
.short 16256,49024,16256,49024,16256,49024,16256,49024,16256,49023,16251,49013,16237,48994,16213,48966
.short 16256,49019,16237,48981,16181,48910,16068,48712,16256,49013,16213,48930,16068,48585,48712,16113
.short 16256,49005,16181,48836,0,16068,48949,16237,16256,48994,16142,48585,48836,16198,49019,16245
.short 16256,48981,16068,15944,48949,16251,49005,16142,16256,48966,15944,16113,49005,16245,48910,48585
.short 16256,48949,0,16181,49024,16181,0,48949,16256,48930,48712,16226,49005,16021,16142,49023
.short 16256,48910,48836,16251,48949,48712,16237,48981,16256,48881,48910,16255,48836,48930,16251,48789
.short 16256,48836,48949,16237,0,49005,16181,16068,16256,48789,48981,16198,16068,49023,15944,16226
.short 16256,48712,49005,16142,16181,48981,48836,16251,16256,48585,49019,16021,16237,48881,48981,16162
.short 16256,49024,16256,49024,16256,49024,16256,49024,16181,48930,16142,48881,16068,48789,15944,48585
.short 0,15944,48836,16142,48949,16213,49005,16251,48949,16226,49019,16255,49005,16198,48910,16021
.short 49024,16237,48949,16068,0,48836,16181,49005,48949,16021,15944,48930,16237,49023,16213,48881
.short 0,48910,16237,49019,16181,48712,48836,16213,16181,49023,16213,48789,48836,16226,49019,16162
.short 16256,48949,0,16181,49024,16181,0,48949,16181,15817,48981,16245,48836,48881,16251,48966
.short 0,16213,49005,15944,16181,49019,16068,16142,48949,16245,48712,48966,16237,48585,48981,16226
.short 49024,16068,16181,49005,0,16237,48949,48836,48949,48881,16251,48585,49005,16162,16142,49013
.short 0,49019,16068,16213,48949,48910,16237,15944,16181,48966,48910,16226,16068,49013,48712,16255
.short 16256,49024,16256,49024,16256,49024,16256,49024,0,15817,48712,16021,48836,16113,48910,16162
.short 49024,16251,49005,16213,48949,16142,48836,15944,0,48789,16142,48966,16237,49023,16251,48994
.short 16256,49005,16181,48836,0,16068,48949,16237,0,16113,48981,16255,49005,16162,48712,48789
.short 49024,16213,48836,48712,16181,49019,16237,48910,0,48930,16251,48994,16068,16021,48981,16255
.short 16256,48949,0,16181,49024,16181,0,48949,0,16198,49019,16113,16068,49013,16213,48585
.short 49024,16142,16068,49019,16181,15944,49005,16213,0,48994,16213,15817,49005,16198,15944,49013
.short 16256,48836,48949,16237,0,49005,16181,16068,0,16245,48910,48930,16237,15817,49019,16113
.short 49024,15944,16237,48910,48949,16213,16068,49019,0,49023,15944,16245,48836,48994,16142,16198
.short 16256,49024,16256,49024,16256,49024,16256,49024,48949,16198,48981,16226,49005,16245,49019,16255
.short 0,48712,16068,48910,16181,48981,16237,49019,16181,48881,15944,15817,48836,16162,48981,16245
.short 49024,16237,48949,16068,0,48836,16181,49005,16181,49013,16251,48966,16068,15817,48910,16226
.short 0,16142,49005,16251,48949,15944,16068,48981,48949,15817,16142,49013,16237,48881,48712,16198
.short 16256,48949,0,16181,49024,16181,0,48949,48949,16255,48910,48789,16237,48994,15944,16162
.short 0,48981,16237,48712,48949,16251,48836,48910,16181,16021,49019,16162,16068,49023,16142,16113
.short 49024,16068,16181,49005,0,16237,48949,48836,16181,48994,48712,16255,48836,48966,16213,16021
.short 0,16251,48836,48981,16181,16142,49005,48712,48949,48930,16213,16113,49005,48789,16251,15817
.short 0,0,0,0,0,0,0,0,0,48585,15944,48789,16068,48881,16142,48930
.short 0,48712,16068,48910,16181,48981,16237,49019,0,48789,16142,48966,16237,49023,16251,48994
.short 0,48836,16181,49005,16256,49005,16181,48836,0,48881,16213,49023,16237,48930,15944,16021
.short 0,48910,16237,49019,16181,48712,48836,16213,0,48930,16251,48994,16068,16021,48981,16255
.short 0,48949,16256,48949,0,16181,49024,16181,0,48966,16251,48881,48836,16245,48981,15817
.short 0,48981,16237,48712,48949,16251,48836,48910,0,48994,16213,15817,49005,16198,15944,49013
.short 0,49005,16181,16068,49024,16068,16181,49005,0,49013,16142,16162,49005,48585,16251,48881
.short 0,49019,16068,16213,48949,48910,16237,15944,0,49023,15944,16245,48836,48994,16142,16198
.short 0,0,0,0,0,0,0,0,16181,48966,16213,48994,16237,49013,16251,49023
.short 16256,49019,16237,48981,16181,48910,16068,48712,16181,48881,15944,15817,48836,16162,48981,16245
.short 0,16068,48949,16237,49024,16237,48949,16068,48949,16245,49019,16198,48836,48585,16142,48994
.short 49024,16213,48836,48712,16181,49019,16237,48910,48949,15817,16142,49013,16237,48881,48712,16198
.short 0,48949,16256,48949,0,16181,49024,16181,16181,49023,16142,16021,49005,16226,48712,48930
.short 16256,48910,48836,16251,48949,48712,16237,48981,16181,16021,49019,16162,16068,49023,16142,16113
.short 0,16237,48949,48836,16256,48836,48949,16237,48949,16226,15944,49023,16068,16198,48981,48789
.short 49024,15944,16237,48910,48949,16213,16068,49019,48949,48930,16213,16113,49005,48789,16251,15817
.short 0,0,0,0,0,0,0,0,16256,49023,16251,49013,16237,48994,16213,48966
.short 0,15944,48836,16142,48949,16213,49005,16251,49024,16245,48981,16162,48836,15817,15944,48881
.short 0,48836,16181,49005,16256,49005,16181,48836,16256,48994,16142,48585,48836,16198,49019,16245
.short 0,16142,49005,16251,48949,15944,16068,48981,49024,16198,48712,48881,16237,49013,16142,15817
.short 0,48949,16256,48949,0,16181,49024,16181,16256,48930,48712,16226,49005,16021,16142,49023
.short 0,16213,49005,15944,16181,49019,16068,16142,49024,16113,16142,49023,16068,16162,49019,16021
.short 0,49005,16181,16068,49024,16068,16181,49005,16256,48789,48981,16198,16068,49023,15944,16226
.short 0,16251,48836,48981,16181,16142,49005,48712,49024,15817,16251,48789,49005,16113,16213,48930
.short 0,0,0,0,0,0,0,0,16181,48930,16142,48881,16068,48789,15944,48585
.short 49024,16251,49005,16213,48949,16142,48836,15944,16181,48994,16251,49023,16237,48966,16142,48789
.short 0,16068,48949,16237,49024,16237,48949,16068,48949,16021,15944,48930,16237,49023,16213,48881
.short 16256,48981,16068,15944,48949,16251,49005,16142,48949,16255,48981,16021,16068,48994,16251,48930
.short 0,48949,16256,48949,0,16181,49024,16181,16181,15817,48981,16245,48836,48881,16251,48966
.short 49024,16142,16068,49019,16181,15944,49005,16213,16181,49013,15944,16198,49005,15817,16213,48994
.short 0,16237,48949,48836,16256,48836,48949,16237,48949,48881,16251,48585,49005,16162,16142,49013
.short 16256,48712,49005,16142,16181,48981,48836,16251,48949,16198,16142,48994,48836,16245,15944,49023
.short 16256,49024,16256,49024,16256,49024,16256,49024,49024,16255,49019,16245,49005,16226,48981,16198
.short 16256,49019,16237,48981,16181,48910,16068,48712,49024,16245,48981,16162,48836,15817,15944,48881
.short 16256,49005,16181,48836,0,16068,48949,16237,49024,16226,48910,15817,16068,48966,16251,49013
.short 16256,48981,16068,15944,48949,16251,49005,16142,49024,16198,48712,48881,16237,49013,16142,15817
.short 16256,48949,0,16181,49024,16181,0,48949,49024,16162,15944,48994,16237,48789,48910,16255
.short 16256,48910,48836,16251,48949,48712,16237,48981,49024,16113,16142,49023,16068,16162,49019,16021
.short 16256,48836,48949,16237,0,49005,16181,16068,49024,16021,16213,48966,48836,16255,48712,48994
.short 16256,48712,49005,16142,16181,48981,48836,16251,49024,15817,16251,48789,49005,16113,16213,48930
.short 16256,49024,16256,49024,16256,49024,16256,49024,48949,16162,48910,16113,48836,16021,48712,15817
.short 0,15944,48836,16142,48949,16213,49005,16251,16181,48994,16251,49023,16237,48966,16142,48789
.short 49024,16237,48949,16068,0,48836,16181,49005,16181,48789,48712,16162,49005,16255,48981,16113
.short 0,48910,16237,49019,16181,48712,48836,16213,48949,16255,48981,16021,16068,48994,16251,48930
.short 16256,48949,0,16181,49024,16181,0,48949,48949,48585,16213,49013,16068,16113,49019,16198
.short 0,16213,49005,15944,16181,49019,16068,16142,16181,49013,15944,16198,49005,15817,16213,48994
.short 49024,16068,16181,49005,0,16237,48949,48836,16181,16113,49019,15817,16237,48930,48910,16245
.short 0,49019,16068,16213,48949,48910,16237,15944,48949,16198,16142,48994,48836,16245,15944,49023
.short 16256,49024,16256,49024,16256,49024,16256,49024,0,48585,15944,48789,16068,48881,16142,48930
.short 49024,16251,49005,16213,48949,16142,48836,15944,0,16021,48910,16198,49005,16255,49019,16226
.short 16256,49005,16181,48836,0,16068,48949,16237,0,48881,16213,49023,16237,48930,15944,16021
.short 49024,16213,48836,48712,16181,49019,16237,48910,0,16162,49019,16226,48836,48789,16213,49023
.short 16256,48949,0,16181,49024,16181,0,48949,0,48966,16251,48881,48836,16245,48981,15817
.short 49024,16142,16068,49019,16181,15944,49005,16213,0,16226,48981,48585,16237,48966,48712,16245
.short 16256,48836,48949,16237,0,49005,16181,16068,0,49013,16142,16162,49005,48585,16251,48881
.short 49024,15944,16237,48910,48949,16213,16068,49019,0,16255,48712,49013,16068,16226,48910,48966
.short 16256,49024,16256,49024,16256,49024,16256,49024,16181,48966,16213,48994,16237,49013,16251,49023
.short 0,48712,16068,48910,16181,48981,16237,49019,48949,16113,48712,48585,16068,48930,16213,49013
.short 49024,16237,48949,16068,0,48836,16181,49005,48949,16245,49019,16198,48836,48585,16142,48994
.short 0,16142,49005,16251,48949,15944,16068,48981,16181,48585,48910,16245,49005,16113,15944,48966
.short 16256,48949,0,16181,49024,16181,0,48949,16181,49023,16142,16021,49005,16226,48712,48930
.short 0,48981,16237,48712,48949,16251,48836,48910,48949,48789,16251,48930,48836,16255,48910,48881
.short 49024,16068,16181,49005,0,16237,48949,48836,48949,16226,15944,49023,16068,16198,48981,48789
.short 0,16251,48836,48981,16181,16142,49005,48712,16181,16162,48981,48881,16237,16021,49019,48585
.short 0,0,0,0,0,0,0,0,0,15817,48712,16021,48836,16113,48910,16162
.short 0,48712,16068,48910,16181,48981,16237,49019,0,16021,48910,16198,49005,16255,49019,16226
.short 0,48836,16181,49005,16256,49005,16181,48836,0,16113,48981,16255,49005,16162,48712,48789
.short 0,48910,16237,49019,16181,48712,48836,16213,0,16162,49019,16226,48836,48789,16213,49023
.short 0,48949,16256,48949,0,16181,49024,16181,0,16198,49019,16113,16068,49013,16213,48585
.short 0,48981,16237,48712,48949,16251,48836,48910,0,16226,48981,48585,16237,48966,48712,16245
.short 0,49005,16181,16068,49024,16068,16181,49005,0,16245,48910,48930,16237,15817,49019,16113
.short 0,49019,16068,16213,48949,48910,16237,15944,0,16255,48712,49013,16068,16226,48910,48966
.short 0,0,0,0,0,0,0,0,48949,16198,48981,16226,49005,16245,49019,16255
.short 16256,49019,16237,48981,16181,48910,16068,48712,48949,16113,48712,48585,16068,48930,16213,49013
.short 0,16068,48949,16237,49024,16237,48949,16068,16181,49013,16251,48966,16068,15817,48910,16226
.short 49024,16213,48836,48712,16181,49019,16237,48910,16181,48585,48910,16245,49005,16113,15944,48966
.short 0,48949,16256,48949,0,16181,49024,16181,48949,16255,48910,48789,16237,48994,15944,16162
.short 16256,48910,48836,16251,48949,48712,16237,48981,48949,48789,16251,48930,48836,16255,48910,48881
.short 0,16237,48949,48836,16256,48836,48949,16237,16181,48994,48712,16255,48836,48966,16213,16021
.short 49024,15944,16237,48910,48949,16213,16068,49019,16181,16162,48981,48881,16237,16021,49019,48585
.short 0,0,0,0,0,0,0,0,49024,16255,49019,16245,49005,16226,48981,16198
.short 0,15944,48836,16142,48949,16213,49005,16251,16256,49013,16213,48930,16068,48585,48712,16113
.short 0,48836,16181,49005,16256,49005,16181,48836,49024,16226,48910,15817,16068,48966,16251,49013
.short 0,16142,49005,16251,48949,15944,16068,48981,16256,48966,15944,16113,49005,16245,48910,48585
.short 0,48949,16256,48949,0,16181,49024,16181,49024,16162,15944,48994,16237,48789,48910,16255
.short 0,16213,49005,15944,16181,49019,16068,16142,16256,48881,48910,16255,48836,48930,16251,48789
.short 0,49005,16181,16068,49024,16068,16181,49005,49024,16021,16213,48966,48836,16255,48712,48994
.short 0,16251,48836,48981,16181,16142,49005,48712,16256,48585,49019,16021,16237,48881,48981,16162
.short 0,0,0,0,0,0,0,0,48949,16162,48910,16113,48836,16021,48712,15817
.short 49024,16251,49005,16213,48949,16142,48836,15944,48949,16226,49019,16255,49005,16198,48910,16021
.short 0,16068,48949,16237,49024,16237,48949,16068,16181,48789,48712,16162,49005,16255,48981,16113
.short 16256,48981,16068,15944,48949,16251,49005,16142,16181,49023,16213,48789,48836,16226,49019,16162
.short 0,48949,16256,48949,0,16181,49024,16181,48949,48585,16213,49013,16068,16113,49019,16198
.short 49024,16142,16068,49019,16181,15944,49005,16213,48949,16245,48712,48966,16237,48585,48981,16226
.short 0,16237,48949,48836,16256,48836,48949,16237,16181,16113,49019,15817,16237,48930,48910,16245
.short 16256,48712,49005,16142,16181,48981,48836,16251,16181,48966,48910,16226,16068,49013,48712,16255
.short 16256,0,49024,0,16256,0,49024,0,16256,15817,49019,48789,16237,16113,48981,48930
.short 16256,15944,49005,48910,16181,16213,48836,49019,16256,16021,48981,48966,16068,16255,15944,48994
.short 16256,16068,48949,49005,0,16237,16181,48836,16256,16113,48910,49023,48836,16162,16251,16021
.short 16256,16142,48836,49019,48949,15944,16237,16213,16256,16162,48712,48994,49005,48789,16142,16255
.short 16256,16181,0,48949,49024,48949,0,16181,16256,16198,15944,48881,49005,49013,48910,15817
.short 16256,16213,16068,48712,48949,49019,49005,48910,16256,16226,16142,15817,48836,48966,49019,49013
.short 16256,16237,16181,16068,0,48836,48949,49005,16256,16245,16213,16162,16068,15817,48712,48881
.short 16256,16251,16237,16213,16181,16142,16068,15944,16256,16255,16251,16245,16237,16226,16213,16198
.short 16256,0,49024,0,16256,0,49024,0,16181,16198,48910,48994,16068,16245,48712,49023
.short 0,16251,16068,48981,48949,16142,16237,48712,48949,16113,16251,15817,49005,48930,16142,16245
.short 49024,48836,16181,16237,0,49005,48949,16068,48949,49013,48712,16198,16237,15817,48981,48994
.short 0,48981,49005,48712,16181,16251,16068,48910,16181,48585,48981,49013,48836,16113,16251,16198
.short 16256,16181,0,48949,49024,48949,0,16181,16181,16255,16213,16021,48836,48994,49019,48930
.short 0,16142,16237,16251,16181,15944,48836,48981,48949,48789,15944,16162,16237,16255,16213,16113
.short 49024,49005,48949,48836,0,16068,16181,16237,48949,48994,49019,49023,49005,48966,48910,48789
.short 0,48712,48836,48910,48949,48981,49005,49019,16181,16162,16142,16113,16068,16021,15944,15817
.short 16256,0,49024,0,16256,0,49024,0,0,16255,15944,49013,48836,16226,16142,48966
.short 49024,48712,16237,16142,48949,48981,16068,16251,0,49013,48910,16162,16237,48585,49019,48881
.short 16256,16068,48949,49005,0,16237,16181,48836,0,16226,16213,48585,49005,48966,15944,16245
.short 49024,48910,16068,16251,16181,48712,49005,48981,0,48966,49019,48881,16068,16245,16213,15817
.short 16256,16181,0,48949,49024,48949,0,16181,0,16162,16251,16226,16068,48789,48981,49023
.short 49024,48981,48836,15944,16181,16251,16237,16142,0,48881,48981,49023,49005,48930,48712,16021
.short 16256,16237,16181,16068,0,48836,48949,49005,0,16021,16142,16198,16237,16255,16251,16226
.short 49024,49019,49005,48981,48949,48910,48836,48712,0,48585,48712,48789,48836,48881,48910,48930
.short 16256,0,49024,0,16256,0,49024,0,48949,16162,16213,48881,49005,16021,16251,48585
.short 0,49019,48836,16213,16181,48910,49005,15944,16181,16226,48712,49023,48836,16198,16213,48789
.short 49024,48836,16181,16237,0,49005,48949,16068,16181,48789,49019,48930,16068,16255,16142,48881
.short 0,16213,16237,15944,48949,49019,48836,16142,48949,49023,48910,16021,16237,16226,15944,48930
.short 16256,16181,0,48949,49024,48949,0,16181,48949,48585,16142,16245,16237,16113,48712,48966
.short 0,48910,49005,49019,48949,48712,16068,16213,16181,16245,16251,16198,16068,48585,48910,48994
.short 49024,49005,48949,48836,0,16068,16181,16237,16181,16113,15944,48585,48836,48930,48981,49013
.short 0,15944,16068,16142,16181,16213,16237,16251,48949,48966,48981,48994,49005,49013,49019,49023
.short 0,49024,0,16256,0,49024,0,16256,0,49023,48712,16245,16068,48994,48910,16198
.short 0,49019,48836,16213,16181,48910,49005,15944,0,49013,48910,16162,16237,48585,49019,48881
.short 0,49005,48949,16068,16256,16068,48949,49005,0,48994,48981,15817,16237,16198,48712,49013
.short 0,48981,49005,48712,16181,16251,16068,48910,0,48966,49019,48881,16068,16245,16213,15817
.short 0,48949,49024,48949,0,16181,16256,16181,0,48930,49019,48994,48836,16021,16213,16255
.short 0,48910,49005,49019,48949,48712,16068,16213,0,48881,48981,49023,49005,48930,48712,16021
.short 0,48836,48949,49005,49024,49005,48949,48836,0,48789,48910,48966,49005,49023,49019,48994
.short 0,48712,48836,48910,48949,48981,49005,49019,0,48585,48712,48789,48836,48881,48910,48930
.short 0,49024,0,16256,0,49024,0,16256,16181,48930,48981,16113,16237,48789,49019,15817
.short 16256,15944,49005,48910,16181,16213,48836,49019,16181,16226,48712,49023,48836,16198,16213,48789
.short 0,16237,16181,48836,49024,48836,16181,16237,48949,16021,16251,16162,48836,49023,48910,16113
.short 49024,48910,16068,16251,16181,48712,49005,48981,48949,49023,48910,16021,16237,16226,15944,48930
.short 0,48949,49024,48949,0,16181,16256,16181,16181,15817,48910,49013,49005,48881,15944,16198
.short 16256,16213,16068,48712,48949,49019,49005,48910,16181,16245,16251,16198,16068,48585,48910,48994
.short 0,16068,16181,16237,16256,16237,16181,16068,48949,48881,48712,15817,16068,16162,16213,16245
.short 49024,49019,49005,48981,48949,48910,48836,48712,48949,48966,48981,48994,49005,49013,49019,49023
.short 0,49024,0,16256,0,49024,0,16256,16256,15817,49019,48789,16237,16113,48981,48930
.short 0,16251,16068,48981,48949,16142,16237,48712,49024,48789,16213,16198,48836,49023,48712,16226
.short 0,49005,48949,16068,16256,16068,48949,49005,16256,16113,48910,49023,48836,16162,16251,16021
.short 0,16213,16237,15944,48949,49019,48836,16142,49024,48930,15944,16226,16237,16021,48910,49023
.short 0,48949,49024,48949,0,16181,16256,16181,16256,16198,15944,48881,49005,49013,48910,15817
.short 0,16142,16237,16251,16181,15944,48836,48981,49024,48994,48910,48585,16068,16198,16251,16245
.short 0,48836,48949,49005,49024,49005,48949,48836,16256,16245,16213,16162,16068,15817,48712,48881
.short 0,15944,16068,16142,16181,16213,16237,16251,49024,49023,49019,49013,49005,48994,48981,48966
.short 0,49024,0,16256,0,49024,0,16256,16181,16198,48910,48994,16068,16245,48712,49023
.short 49024,48712,16237,16142,48949,48981,16068,16251,16181,48881,49019,48585,16237,16162,48910,49013
.short 0,16237,16181,48836,49024,48836,16181,16237,48949,49013,48712,16198,16237,15817,48981,48994
.short 16256,16142,48836,49019,48949,15944,16237,16213,48949,15817,16213,16245,16068,48881,49019,48966
.short 0,48949,49024,48949,0,16181,16256,16181,16181,16255,16213,16021,48836,48994,49019,48930
.short 49024,48981,48836,15944,16181,16251,16237,16142,16181,16021,48712,48930,49005,49023,48981,48881
.short 0,16068,16181,16237,16256,16237,16181,16068,48949,48994,49019,49023,49005,48966,48910,48789
.short 16256,16251,16237,16213,16181,16142,16068,15944,48949,48930,48910,48881,48836,48789,48712,48585
.short 16256,0,49024,0,16256,0,49024,0,49024,48585,16251,16021,49005,48881,16213,16162
.short 16256,15944,49005,48910,16181,16213,48836,49019,49024,48789,16213,16198,48836,49023,48712,16226
.short 16256,16068,48949,49005,0,16237,16181,48836,49024,48881,16142,16255,16068,48930,49019,48789
.short 16256,16142,48836,49019,48949,15944,16237,16213,49024,48930,15944,16226,16237,16021,48910,49023
.short 16256,16181,0,48949,49024,48949,0,16181,49024,48966,48712,16113,16237,16245,16142,48585
.short 16256,16213,16068,48712,48949,49019,49005,48910,49024,48994,48910,48585,16068,16198,16251,16245
.short 16256,16237,16181,16068,0,48836,48949,49005,49024,49013,48981,48930,48836,48585,15944,16113
.short 16256,16251,16237,16213,16181,16142,16068,15944,49024,49023,49019,49013,49005,48994,48981,48966
.short 16256,0,49024,0,16256,0,49024,0,48949,48966,16142,16226,48836,49013,15944,16255
.short 0,16251,16068,48981,48949,16142,16237,48712,16181,48881,49019,48585,16237,16162,48910,49013
.short 49024,48836,16181,16237,0,49005,48949,16068,16181,16245,15944,48966,49005,48585,16213,16226
.short 0,48981,49005,48712,16181,16251,16068,48910,48949,15817,16213,16245,16068,48881,49019,48966
.short 16256,16181,0,48949,49024,48949,0,16181,48949,49023,48981,48789,16068,16226,16251,16162
.short 0,16142,16237,16251,16181,15944,48836,48981,16181,16021,48712,48930,49005,49023,48981,48881
.short 49024,49005,48949,48836,0,16068,16181,16237,16181,16226,16251,16255,16237,16198,16142,16021
.short 0,48712,48836,48910,48949,48981,49005,49019,48949,48930,48910,48881,48836,48789,48712,48585
.short 16256,0,49024,0,16256,0,49024,0,0,49023,48712,16245,16068,48994,48910,16198
.short 49024,48712,16237,16142,48949,48981,16068,16251,0,16245,16142,48930,49005,15817,16251,16113
.short 16256,16068,48949,49005,0,16237,16181,48836,0,48994,48981,15817,16237,16198,48712,49013
.short 49024,48910,16068,16251,16181,48712,49005,48981,0,16198,16251,16113,48836,49013,48981,48585
.short 16256,16181,0,48949,49024,48949,0,16181,0,48930,49019,48994,48836,16021,16213,16255
.short 49024,48981,48836,15944,16181,16251,16237,16142,0,16113,16213,16255,16237,16162,15944,48789
.short 16256,16237,16181,16068,0,48836,48949,49005,0,48789,48910,48966,49005,49023,49019,48994
.short 49024,49019,49005,48981,48949,48910,48836,48712,0,15817,15944,16021,16068,16113,16142,16162
.short 16256,0,49024,0,16256,0,49024,0,16181,48930,48981,16113,16237,48789,49019,15817
.short 0,49019,48836,16213,16181,48910,49005,15944,48949,48994,15944,16255,16068,48966,48981,16021
.short 49024,48836,16181,16237,0,49005,48949,16068,48949,16021,16251,16162,48836,49023,48910,16113
.short 0,16213,16237,15944,48949,49019,48836,16142,16181,16255,16142,48789,49005,48994,48712,16162
.short 16256,16181,0,48949,49024,48949,0,16181,16181,15817,48910,49013,49005,48881,15944,16198
.short 0,48910,49005,49019,48949,48712,16068,16213,48949,49013,49019,48966,48836,15817,16142,16226
.short 49024,49005,48949,48836,0,16068,16181,16237,48949,48881,48712,15817,16068,16162,16213,16245
.short 0,15944,16068,16142,16181,16213,16237,16251,16181,16198,16213,16226,16237,16245,16251,16255
.short 0,49024,0,16256,0,49024,0,16256,0,16255,15944,49013,48836,16226,16142,48966
.short 0,49019,48836,16213,16181,48910,49005,15944,0,16245,16142,48930,49005,15817,16251,16113
.short 0,49005,48949,16068,16256,16068,48949,49005,0,16226,16213,48585,49005,48966,15944,16245
.short 0,48981,49005,48712,16181,16251,16068,48910,0,16198,16251,16113,48836,49013,48981,48585
.short 0,48949,49024,48949,0,16181,16256,16181,0,16162,16251,16226,16068,48789,48981,49023
.short 0,48910,49005,49019,48949,48712,16068,16213,0,16113,16213,16255,16237,16162,15944,48789
.short 0,48836,48949,49005,49024,49005,48949,48836,0,16021,16142,16198,16237,16255,16251,16226
.short 0,48712,48836,48910,48949,48981,49005,49019,0,15817,15944,16021,16068,16113,16142,16162
.short 0,49024,0,16256,0,49024,0,16256,48949,16162,16213,48881,49005,16021,16251,48585
.short 16256,15944,49005,48910,16181,16213,48836,49019,48949,48994,15944,16255,16068,48966,48981,16021
.short 0,16237,16181,48836,49024,48836,16181,16237,16181,48789,49019,48930,16068,16255,16142,48881
.short 49024,48910,16068,16251,16181,48712,49005,48981,16181,16255,16142,48789,49005,48994,48712,16162
.short 0,48949,49024,48949,0,16181,16256,16181,48949,48585,16142,16245,16237,16113,48712,48966
.short 16256,16213,16068,48712,48949,49019,49005,48910,48949,49013,49019,48966,48836,15817,16142,16226
.short 0,16068,16181,16237,16256,16237,16181,16068,16181,16113,15944,48585,48836,48930,48981,49013
.short 49024,49019,49005,48981,48949,48910,48836,48712,16181,16198,16213,16226,16237,16245,16251,16255
.short 0,49024,0,16256,0,49024,0,16256,49024,48585,16251,16021,49005,48881,16213,16162
.short 0,16251,16068,48981,48949,16142,16237,48712,16256,16021,48981,48966,16068,16255,15944,48994
.short 0,49005,48949,16068,16256,16068,48949,49005,49024,48881,16142,16255,16068,48930,49019,48789
.short 0,16213,16237,15944,48949,49019,48836,16142,16256,16162,48712,48994,49005,48789,16142,16255
.short 0,48949,49024,48949,0,16181,16256,16181,49024,48966,48712,16113,16237,16245,16142,48585
.short 0,16142,16237,16251,16181,15944,48836,48981,16256,16226,16142,15817,48836,48966,49019,49013
.short 0,48836,48949,49005,49024,49005,48949,48836,49024,49013,48981,48930,48836,48585,15944,16113
.short 0,15944,16068,16142,16181,16213,16237,16251,16256,16255,16251,16245,16237,16226,16213,16198
.short 0,49024,0,16256,0,49024,0,16256,48949,48966,16142,16226,48836,49013,15944,16255
.short 49024,48712,16237,16142,48949,48981,16068,16251,48949,16113,16251,15817,49005,48930,16142,16245
.short 0,16237,16181,48836,49024,48836,16181,16237,16181,16245,15944,48966,49005,48585,16213,16226
.short 16256,16142,48836,49019,48949,15944,16237,16213,16181,48585,48981,49013,48836,16113,16251,16198
.short 0,48949,49024,48949,0,16181,16256,16181,48949,49023,48981,48789,16068,16226,16251,16162
.short 49024,48981,48836,15944,16181,16251,16237,16142,48949,48789,15944,16162,16237,16255,16213,16113
.short 0,16068,16181,16237,16256,16237,16181,16068,16181,16226,16251,16255,16237,16198,16142,16021
.short 16256,16251,16237,16213,16181,16142,16068,15944,16181,16162,16142,16113,16068,16021,15944,15817
.long 0x3f800000,0x0,0x3f800000,0x0,0x3f800000,0x0,0x3f7fffb1,0x3b490fc6
.long 0x3f800000,0x0,0x3f7ffec4,0x3bc90f88,0x3f800000,0x0,0x3f7ffd39,0x3c16cb58
.long 0x3f800000,0x0,0x3f7ffb11,0x3c490e90,0x3f800000,0x0,0x3f7ff84a,0x3c7b514b
.long 0x3f800000,0x0,0x3f7ff4e6,0x3c96c9b6,0x3f800000,0x0,0x3f7ff0e3,0x3cafea69
.long 0x3f800000,0x0,0x3f7fec43,0x3cc90ab0,0x3f800000,0x0,0x3f7fe705,0x3ce22a7a
.long 0x3f800000,0x0,0x3f7fe129,0x3cfb49ba,0x3f800000,0x0,0x3f7fdaaf,0x3d0a342f
.long 0x3f800000,0x0,0x3f7fd397,0x3d16c32c,0x3f800000,0x0,0x3f7fcbe2,0x3d2351cb
.long 0x3f800000,0x0,0x3f7fc38f,0x3d2fe007,0x3f800000,0x0,0x3f7fba9e,0x3d3c6dd5
.long 0x3f800000,0x0,0x3f800000,0x0,0x3f7ffec4,0x3bc90f88,0x3f7ffd39,0x3c16cb58
.long 0x3f7ffb11,0x3c490e90,0x3f7ff4e6,0x3c96c9b6,0x3f7ff4e6,0x3c96c9b6,0x3f7fe705,0x3ce22a7a
.long 0x3f7fec43,0x3cc90ab0,0x3f7fd397,0x3d16c32c,0x3f7fe129,0x3cfb49ba,0x3f7fba9e,0x3d3c6dd5
.long 0x3f7fd397,0x3d16c32c,0x3f7f9c18,0x3d621469,0x3f7fc38f,0x3d2fe007,0x3f7f7808,0x3d83db0a
.long 0x3f7fb10f,0x3d48fb30,0x3f7f4e6d,0x3d96a905,0x3f7f9c18,0x3d621469,0x3f7f1f49,0x3da973ba
.long 0x3f7f84ab,0x3d7b2b74,0x3f7eea9d,0x3dbc3ac3,0x3f7f6ac7,0x3d8a200a,0x3f7eb069,0x3dcefdb7
.long 0x3f7f4e6d,0x3d96a905,0x3f7e70b0,0x3de1bc2e,0x3f7f2f9d,0x3da3308c,0x3f7e2b72,0x3df475c0
.long 0x3f7f0e58,0x3dafb680,0x3f7de0b1,0x3e039502,0x3f7eea9d,0x3dbc3ac3,0x3f7d906e,0x3e0cec4a
.long 0x3f800000,0x0,0x3f800000,0x0,0x3f7ffb11,0x3c490e90,0x3f7ff84a,0x3c7b514b
.long 0x3f7fec43,0x3cc90ab0,0x3f7fe129,0x3cfb49ba,0x3f7fd397,0x3d16c32c,0x3f7fba9e,0x3d3c6dd5
.long 0x3f7fb10f,0x3d48fb30,0x3f7f84ab,0x3d7b2b74,0x3f7f84ab,0x3d7b2b74,0x3f7f3f54,0x3d9cecf9
.long 0x3f7f4e6d,0x3d96a905,0x3f7eea9d,0x3dbc3ac3,0x3f7f0e58,0x3dafb680,0x3f7e868b,0x3ddb7d37
.long 0x3f7ec46d,0x3dc8bd36,0x3f7e1324,0x3dfab273,0x3f7e70b0,0x3de1bc2e,0x3f7d906e,0x3e0cec4a
.long 0x3f7e1324,0x3dfab273,0x3f7cfe73,0x3e1c76de,0x3f7dabcc,0x3e09cf86,0x3f7c5d3a,0x3e2bf804
.long 0x3f7d3aac,0x3e164083,0x3f7baccd,0x3e3b6ecf,0x3f7cbfc9,0x3e22abb6,0x3f7aed37,0x3e4ada4f
.long 0x3f7c3b28,0x3e2f10a2,0x3f7a1e84,0x3e5a3997,0x3f7baccd,0x3e3b6ecf,0x3f7940c0,0x3e698bba
.long 0x3f800000,0x0,0x3f800000,0x0,0x3f7ff4e6,0x3c96c9b6,0x3f7ff0e3,0x3cafea69
.long 0x3f7fd397,0x3d16c32c,0x3f7fc38f,0x3d2fe007,0x3f7f9c18,0x3d621469,0x3f7f7808,0x3d83db0a
.long 0x3f7f4e6d,0x3d96a905,0x3f7f0e58,0x3dafb680,0x3f7eea9d,0x3dbc3ac3,0x3f7e868b,0x3ddb7d37
.long 0x3f7e70b0,0x3de1bc2e,0x3f7de0b1,0x3e039502,0x3f7de0b1,0x3e039502,0x3f7d1cdd,0x3e195be0
.long 0x3f7d3aac,0x3e164083,0x3f7c3b28,0x3e2f10a2,0x3f7c7eb0,0x3e28defc,0x3f7b3bab,0x3e44b0b9
.long 0x3f7baccd,0x3e3b6ecf,0x3f7a1e84,0x3e5a3997,0x3f7ac516,0x3e4dee60,0x3f78e3d6,0x3e6fa8b2
.long 0x3f79c79d,0x3e605c13,0x3f778bc5,0x3e827dc0,0x3f78b47b,0x3e72b651,0x3f76167a,0x3e8d17c0
.long 0x3f778bc5,0x3e827dc0,0x3f748422,0x3e97a117,0x3f764d97,0x3e8b9507,0x3f72d4eb,0x3ea21887
.long 0x3f800000,0x0,0x3f800000,0x0,0x3f7fec43,0x3cc90ab0,0x3f7fe705,0x3ce22a7a
.long 0x3f7fb10f,0x3d48fb30,0x3f7f9c18,0x3d621469,0x3f7f4e6d,0x3d96a905,0x3f7f1f49,0x3da973ba
.long 0x3f7ec46d,0x3dc8bd36,0x3f7e70b0,0x3de1bc2e,0x3f7e1324,0x3dfab273,0x3f7d906e,0x3e0cec4a
.long 0x3f7d3aac,0x3e164083,0x3f7c7eb0,0x3e28defc,0x3f7c3b28,0x3e2f10a2,0x3f7b3bab,0x3e44b0b9
.long 0x3f7b14be,0x3e47c5c2,0x3f79c79d,0x3e605c13,0x3f79c79d,0x3e605c13,0x3f7822d1,0x3e7bdba4
.long 0x3f7853f8,0x3e78cfcc,0x3f764d97,0x3e8b9507,0x3f76ba07,0x3e888e93,0x3f74484b,0x3e9920fe
.long 0x3f74fa0b,0x3e94a031,0x3f721352,0x3ea68f12,0x3f731447,0x3ea09ae5,0x3f6faf1b,0x3eb3dca5
.long 0x3f710908,0x3eac7cd4,0x3f6d1c1d,0x3ec1071e,0x3f6ed89e,0x3eb8442a,0x3f6a5ad9,0x3ece0bea
.long 0x3f800000,0x0,0x3f800000,0x0,0x3f7fe129,0x3cfb49ba,0x3f7fdaaf,0x3d0a342f
.long 0x3f7f84ab,0x3d7b2b74,0x3f7f6ac7,0x3d8a200a,0x3f7eea9d,0x3dbc3ac3,0x3f7eb069,0x3dcefdb7
.long 0x3f7e1324,0x3dfab273,0x3f7dabcc,0x3e09cf86,0x3f7cfe73,0x3e1c76de,0x3f7c5d3a,0x3e2bf804
.long 0x3f7baccd,0x3e3b6ecf,0x3f7ac516,0x3e4dee60,0x3f7a1e84,0x3e5a3997,0x3f78e3d6,0x3e6fa8b2
.long 0x3f7853f8,0x3e78cfcc,0x3f76ba07,0x3e888e93,0x3f764d97,0x3e8b9507,0x3f74484b,0x3e9920fe
.long 0x3f740bdd,0x3e9aa086,0x3f718f57,0x3ea986c4,0x3f718f57,0x3ea986c4,0x3f6e8ff8,0x3eb9bb1e
.long 0x3f6ed89e,0x3eb8442a,0x3f6b4b0c,0x3ec9b953,0x3f6be858,0x3ec6d529,0x3f67c187,0x3ed97cb9
.long 0x3f68bf3c,0x3ed53641,0x3f63f473,0x3ee900b7,0x3f655e0b,0x3ee363fa,0x3f5fe4e9,0x3ef840c8
.long 0x3f800000,0x0,0x3f800000,0x0,0x3f7fd397,0x3d16c32c,0x3f7fcbe2,0x3d2351cb
.long 0x3f7f4e6d,0x3d96a905,0x3f7f2f9d,0x3da3308c,0x3f7e70b0,0x3de1bc2e,0x3f7e2b72,0x3df475c0
.long 0x3f7d3aac,0x3e164083,0x3f7cbfc9,0x3e22abb6,0x3f7baccd,0x3e3b6ecf,0x3f7aed37,0x3e4ada4f
.long 0x3f79c79d,0x3e605c13,0x3f78b47b,0x3e72b651,0x3f778bc5,0x3e827dc0,0x3f76167a,0x3e8d17c0
.long 0x3f74fa0b,0x3e94a031,0x3f731447,0x3ea09ae5,0x3f721352,0x3ea68f12,0x3f6faf1b,0x3eb3dca5
.long 0x3f6ed89e,0x3eb8442a,0x3f6be858,0x3ec6d529,0x3f6b4b0c,0x3ec9b953,0x3f67c187,0x3ed97cb9
.long 0x3f676bd8,0x3edae880,0x3f633c5a,0x3eebcbbb,0x3f633c5a,0x3eebcbbb,0x3f5e5aa6,0x3efdbabb
.long 0x3f5ebe05,0x3efc5d27,0x3f591e6a,0x3f07a136,0x3f59f26a,0x3f064b82,0x3f5389c7,0x3f102dd5
.long 0x3f800000,0x0,0x3f800000,0x0,0x3f7fc38f,0x3d2fe007,0x3f7fba9e,0x3d3c6dd5
.long 0x3f7f0e58,0x3dafb680,0x3f7eea9d,0x3dbc3ac3,0x3f7de0b1,0x3e039502,0x3f7d906e,0x3e0cec4a
.long 0x3f7c3b28,0x3e2f10a2,0x3f7baccd,0x3e3b6ecf,0x3f7a1e84,0x3e5a3997,0x3f7940c0,0x3e698bba
.long 0x3f778bc5,0x3e827dc0,0x3f764d97,0x3e8b9507,0x3f748422,0x3e97a117,0x3f72d4eb,0x3ea21887
.long 0x3f710908,0x3eac7cd4,0x3f6ed89e,0x3eb8442a,0x3f6d1c1d,0x3ec1071e,0x3f6a5ad9,0x3ece0bea
.long 0x3f68bf3c,0x3ed53641,0x3f655e0b,0x3ee363fa,0x3f63f473,0x3ee900b7,0x3f5fe4e9,0x3ef840c8
.long 0x3f5ebe05,0x3efc5d27,0x3f59f26a,0x3f064b82,0x3f591e6a,0x3f07a136,0x3f5389c7,0x3f102dd5
.long 0x3f531849,0x3f10d3cd,0x3f4cae79,0x3f19c200,0x3f4cae79,0x3f19c200,0x3f456439,0x3f2302d3
.long 0x3f800000,0x0,0x3f800000,0x0,0x3f7fb10f,0x3d48fb30,0x3f7fa6e3,0x3d55880e
.long 0x3f7ec46d,0x3dc8bd36,0x3f7e9bc9,0x3dd53db9,0x3f7d3aac,0x3e164083,0x3f7cdf6c,0x3e1f917b
.long 0x3f7b14be,0x3e47c5c2,0x3f7a7302,0x3e541501,0x3f7853f8,0x3e78cfcc,0x3f77583a,0x3e840270
.long 0x3f74fa0b,0x3e94a031,0x3f73913f,0x3e9d9e78,0x3f710908,0x3eac7cd4,0x3f6f20b0,0x3eb6ccc3
.long 0x3f6c835e,0x3ec3ef15,0x3f6a09a7,0x3ecf7bca,0x3f676bd8,0x3edae880,0x3f644fac,0x3ee79a5d
.long 0x3f61c598,0x3ef15aea,0x3f5df6be,0x3eff17b2,0x3f5b941a,0x3f039c3d,0x3f570348,0x3f0af1b7
.long 0x3f54db31,0x3f0e39da,0x3f4f7a1f,0x3f15f6d9,0x3f4d9f02,0x3f187fc0,0x3f476085,0x3f209393
.long 0x3f45e403,0x3f226799,0x3f3ebc1b,0x3f2ac082,0x3f3daef9,0x3f2beb4a,0x3f3592e7,0x3f34768f
.long 0x3f800000,0x0,0x3f800000,0x0,0x3f7f9c18,0x3d621469,0x3f7f90b1,0x3d6ea038
.long 0x3f7e70b0,0x3de1bc2e,0x3f7e4323,0x3dee3876,0x3f7c7eb0,0x3e28defc,0x3f7c187a,0x3e3228d4
.long 0x3f79c79d,0x3e605c13,0x3f791298,0x3e6c9a7f,0x3f764d97,0x3e8b9507,0x3f75341d,0x3e931f35
.long 0x3f721352,0x3ea68f12,0x3f708066,0x3eaf713a,0x3f6d1c1d,0x3ec1071e,0x3f6afb8c,0x3ecb2aae
.long 0x3f676bd8,0x3edae880,0x3f64aa59,0x3ee63375,0x3f6106f2,0x3ef41f07,0x3f5d924d,0x3f003a06
.long 0x3f59f26a,0x3f064b82,0x3f55b993,0x3f0cead0,0x3f5233c6,0x3f121eb0,0x3f4d26fd,0x3f19210f
.long 0x3f49d112,0x3f1d7fd1,0x3f43e200,0x3f24d225,0x3f40d0da,0x3f286605,0x3f39f2ac,0x3f2ff3e6
.long 0x3f373a23,0x3f32c8c9,0x3f2f61a5,0x3f3a7ca4,0x3f2d1469,0x3f3ca003,0x3f24381a,0x3f446337
.long 0x3f800000,0x0,0x3f800000,0x0,0x3f7f84ab,0x3d7b2b74,0x3f7f7808,0x3d83db0a
.long 0x3f7e1324,0x3dfab273,0x3f7de0b1,0x3e039502,0x3f7baccd,0x3e3b6ecf,0x3f7b3bab,0x3e44b0b9
.long 0x3f7853f8,0x3e78cfcc,0x3f778bc5,0x3e827dc0,0x3f740bdd,0x3e9aa086,0x3f72d4eb,0x3ea21887
.long 0x3f6ed89e,0x3eb8442a,0x3f6d1c1d,0x3ec1071e,0x3f68bf3c,0x3ed53641,0x3f666771,0x3edf28a9
.long 0x3f61c598,0x3ef15aea,0x3f5ebe05,0x3efc5d27,0x3f59f26a,0x3f064b82,0x3f5627fe,0x3f0c42c9
.long 0x3f514d3d,0x3f13682a,0x3f4cae79,0x3f19c200,0x3f47de65,0x3f1ff6cb,0x3f425b89,0x3f269de3
.long 0x3f3daef9,0x3f2beb4a,0x3f373a23,0x3f32c8c9,0x3f32c8c9,0x3f373a23,0x3f2b561b,0x3f3e35c5
.long 0x3f273656,0x3f41d870,0x3f1ebc12,0x3f48d8b3,0x3f1b02c6,0x3f4bbbf8,0x3f11796b,0x3f52a649
.long 0x3f800000,0x0,0x3f800000,0x0,0x3f7f6ac7,0x3d8a200a,0x3f7f5ce9,0x3d9064b4
.long 0x3f7dabcc,0x3e09cf86,0x3f7d7474,0x3e1008b7,0x3f7ac516,0x3e4dee60,0x3f7a4910,0x3e57278f
.long 0x3f76ba07,0x3e888e93,0x3f75dec6,0x3e8e9a22,0x3f718f57,0x3ea986c4,0x3f703b37,0x3eb0eacb
.long 0x3f6b4b0c,0x3ec9b953,0x3f696591,0x3ed25a09,0x3f63f473,0x3ee900b7,0x3f61668a,0x3ef2bd43
.long 0x3f5b941a,0x3f039c3d,0x3f584853,0x3f08f59b,0x3f5233c6,0x3f121eb0,0x3f4e1689,0x3f17de12
.long 0x3f47de65,0x3f1ff6cb,0x3f42de29,0x3f26050a,0x3f3ca003,0x3f2d1469,0x3f36ad7f,0x3f33587a
.long 0x3f3085bb,0x3f396842,0x3f299415,0x3f3fc767,0x3f239da9,0x3f44e3f5,0x3f1ba299,0x3f4b41fa
.long 0x3f15f6d9,0x3f4f7a1f,0x3f0cead0,0x3f55b993,0x3f07a136,0x3f591e6a,0x3efafef7,0x3f5f20db
.long 0x3f800000,0x0,0x3f800000,0x0,0x3f7f4e6d,0x3d96a905,0x3f7f3f54,0x3d9cecf9
.long 0x3f7d3aac,0x3e164083,0x3f7cfe73,0x3e1c76de,0x3f79c79d,0x3e605c13,0x3f7940c0,0x3e698bba
.long 0x3f74fa0b,0x3e94a031,0x3f740bdd,0x3e9aa086,0x3f6ed89e,0x3eb8442a,0x3f6d67a1,0x3ebf926f
.long 0x3f676bd8,0x3edae880,0x3f655e0b,0x3ee363fa,0x3f5ebe05,0x3efc5d27,0x3f5bfb34,0x3f02ef9f
.long 0x3f54db31,0x3f0e39da,0x3f514d3d,0x3f13682a,0x3f49d112,0x3f1d7fd1,0x3f456439,0x3f2302d3
.long 0x3f3daef9,0x3f2beb4a,0x3f385216,0x3f31a81d,0x3f3085bb,0x3f396842,0x3f2a2a80,0x3f3f41fc
.long 0x3f226799,0x3f45e403,0x3f1b02c6,0x3f4bbbf8,0x3f13682a,0x3f514d3d,0x3f0af1b7,0x3f570348
.long 0x3f039c3d,0x3f5b941a,0x3ef41f07,0x3f6106f2,0x3ee63375,0x3f64aa59,0x3ed0eb2a,0x3f69b7e4
.long 0x3f800000,0x0,0x3f800000,0x0,0x3f7f2f9d,0x3da3308c,0x3f7f1f49,0x3da973ba
.long 0x3f7cbfc9,0x3e22abb6,0x3f7c7eb0,0x3e28defc,0x3f78b47b,0x3e72b651,0x3f7822d1,0x3e7bdba4
.long 0x3f731447,0x3ea09ae5,0x3f721352,0x3ea68f12,0x3f6be858,0x3ec6d529,0x3f6a5ad9,0x3ece0bea
.long 0x3f633c5a,0x3eebcbbb,0x3f6106f2,0x3ef41f07,0x3f591e6a,0x3f07a136,0x3f5627fe,0x3f0c42c9
.long 0x3f4d9f02,0x3f187fc0,0x3f49d112,0x3f1d7fd1,0x3f40d0da,0x3f286605,0x3f3c17d9,0x3f2da859
.long 0x3f32c8c9,0x3f373a23,0x3f2d1469,0x3f3ca003,0x3f239da9,0x3f44e3f5,0x3f1ce11f,0x3f4a4c87
.long 0x3f13682a,0x3f514d3d,0x3f0b9a6b,0x3f5695e5,0x3f0242b1,0x3f5c61c7,0x3ef2bd43,0x3f61668a
.long 0x3ee0924f,0x3f660f88,0x3ecc9b8b,0x3f6aab7b,0x3ebb31a0,0x3f6e46be,0x3ea5129f,0x3f725470
.long 0x3f800000,0x0,0x3f800000,0x0,0x3f7f0e58,0x3dafb680,0x3f7efcc9,0x3db5f8da
.long 0x3f7c3b28,0x3e2f10a2,0x3f7bf531,0x3e354098,0x3f778bc5,0x3e827dc0,0x3f76ef5b,0x3e870ada
.long 0x3f710908,0x3eac7cd4,0x3f6ff573,0x3eb263ef,0x3f68bf3c,0x3ed53641,0x3f671599,0x3edc53c1
.long 0x3f5ebe05,0x3efc5d27,0x3f5c61c7,0x3f0242b1,0x3f531849,0x3f10d3cd,0x3f4fefa8,0x3f1553b7
.long 0x3f45e403,0x3f226799,0x3f41d870,0x3f273656,0x3f373a23,0x3f32c8c9,0x3f3238aa,0x3f37c655
.long 0x3f273656,0x3f41d870,0x3f212ff9,0x3f46e22a,0x3f15f6d9,0x3f4f7a1f,0x3f0ee0db,0x3f546b3b
.long 0x3f039c3d,0x3f5b941a,0x3ef6e0cb,0x3f604621,0x3ee0924f,0x3f660f88,0x3ece0bea,0x3f6a5ad9
.long 0x3eb8442a,0x3f6ed89e,0x3ea395c5,0x3f7294f8,0x3e8e9a22,0x3f75dec6,0x3e6fa8b2,0x3f78e3d6
.long 0x3f800000,0x0,0x3f800000,0x0,0x3f7eea9d,0x3dbc3ac3,0x3f7ed7d4,0x3dc27c39
.long 0x3f7baccd,0x3e3b6ecf,0x3f7b61fc,0x3e419b37,0x3f764d97,0x3e8b9507,0x3f75a67b,0x3e901c2c
.long 0x3f6ed89e,0x3eb8442a,0x3f6db293,0x3ebe1d4a,0x3f655e0b,0x3ee363fa,0x3f6398ac,0x3eea6681
.long 0x3f59f26a,0x3f064b82,0x3f577026,0x3f0a48ad,0x3f4cae79,0x3f19c200,0x3f495521,0x3f1e1e22
.long 0x3f3daef9,0x3f2beb4a,0x3f396842,0x3f3085bb,0x3f2d1469,0x3f3ca003,0x3f27ce61,0x3f4154e1
.long 0x3f1b02c6,0x3f4bbbf8,0x3f14b039,0x3f5064af,0x3f07a136,0x3f591e6a,0x3f003a06,0x3f5d924d
.long 0x3ee63375,0x3f64aa59,0x3ed53641,0x3f68bf3c,0x3ebb31a0,0x3f6e46be,0x3ea80b1f,0x3f71d19f
.long 0x3e8e9a22,0x3f75dec6,0x3e72b651,0x3f78b47b,0x3e419b37,0x3f7b61fc,0x3e1324ca,0x3f7d57de
.long 0x3f800000,0x0,0x3f7fb10f,0x3d48fb30,0x3f800000,0x0,0x3f7fa6e3,0x3d55880e
.long 0x3f800000,0x0,0x3f7f9c18,0x3d621469,0x3f800000,0x0,0x3f7f90b1,0x3d6ea038
.long 0x3f800000,0x0,0x3f7f84ab,0x3d7b2b74,0x3f800000,0x0,0x3f7f7808,0x3d83db0a
.long 0x3f800000,0x0,0x3f7f6ac7,0x3d8a200a,0x3f800000,0x0,0x3f7f5ce9,0x3d9064b4
.long 0x3f800000,0x0,0x3f7f4e6d,0x3d96a905,0x3f800000,0x0,0x3f7f3f54,0x3d9cecf9
.long 0x3f800000,0x0,0x3f7f2f9d,0x3da3308c,0x3f800000,0x0,0x3f7f1f49,0x3da973ba
.long 0x3f800000,0x0,0x3f7f0e58,0x3dafb680,0x3f800000,0x0,0x3f7efcc9,0x3db5f8da
.long 0x3f800000,0x0,0x3f7eea9d,0x3dbc3ac3,0x3f800000,0x0,0x3f7ed7d4,0x3dc27c39
.long 0x3f7ec46d,0x3dc8bd36,0x3f7d3aac,0x3e164083,0x3f7e9bc9,0x3dd53db9,0x3f7cdf6c,0x3e1f917b
.long 0x3f7e70b0,0x3de1bc2e,0x3f7c7eb0,0x3e28defc,0x3f7e4323,0x3dee3876,0x3f7c187a,0x3e3228d4
.long 0x3f7e1324,0x3dfab273,0x3f7baccd,0x3e3b6ecf,0x3f7de0b1,0x3e039502,0x3f7b3bab,0x3e44b0b9
.long 0x3f7dabcc,0x3e09cf86,0x3f7ac516,0x3e4dee60,0x3f7d7474,0x3e1008b7,0x3f7a4910,0x3e57278f
.long 0x3f7d3aac,0x3e164083,0x3f79c79d,0x3e605c13,0x3f7cfe73,0x3e1c76de,0x3f7940c0,0x3e698bba
.long 0x3f7cbfc9,0x3e22abb6,0x3f78b47b,0x3e72b651,0x3f7c7eb0,0x3e28defc,0x3f7822d1,0x3e7bdba4
.long 0x3f7c3b28,0x3e2f10a2,0x3f778bc5,0x3e827dc0,0x3f7bf531,0x3e354098,0x3f76ef5b,0x3e870ada
.long 0x3f7baccd,0x3e3b6ecf,0x3f764d97,0x3e8b9507,0x3f7b61fc,0x3e419b37,0x3f75a67b,0x3e901c2c
.long 0x3f7b14be,0x3e47c5c2,0x3f7853f8,0x3e78cfcc,0x3f7a7302,0x3e541501,0x3f77583a,0x3e840270
.long 0x3f79c79d,0x3e605c13,0x3f764d97,0x3e8b9507,0x3f791298,0x3e6c9a7f,0x3f75341d,0x3e931f35
.long 0x3f7853f8,0x3e78cfcc,0x3f740bdd,0x3e9aa086,0x3f778bc5,0x3e827dc0,0x3f72d4eb,0x3ea21887
.long 0x3f76ba07,0x3e888e93,0x3f718f57,0x3ea986c4,0x3f75dec6,0x3e8e9a22,0x3f703b37,0x3eb0eacb
.long 0x3f74fa0b,0x3e94a031,0x3f6ed89e,0x3eb8442a,0x3f740bdd,0x3e9aa086,0x3f6d67a1,0x3ebf926f
.long 0x3f731447,0x3ea09ae5,0x3f6be858,0x3ec6d529,0x3f721352,0x3ea68f12,0x3f6a5ad9,0x3ece0bea
.long 0x3f710908,0x3eac7cd4,0x3f68bf3c,0x3ed53641,0x3f6ff573,0x3eb263ef,0x3f671599,0x3edc53c1
.long 0x3f6ed89e,0x3eb8442a,0x3f655e0b,0x3ee363fa,0x3f6db293,0x3ebe1d4a,0x3f6398ac,0x3eea6681
.long 0x3f74fa0b,0x3e94a031,0x3f710908,0x3eac7cd4,0x3f73913f,0x3e9d9e78,0x3f6f20b0,0x3eb6ccc3
.long 0x3f721352,0x3ea68f12,0x3f6d1c1d,0x3ec1071e,0x3f708066,0x3eaf713a,0x3f6afb8c,0x3ecb2aae
.long 0x3f6ed89e,0x3eb8442a,0x3f68bf3c,0x3ed53641,0x3f6d1c1d,0x3ec1071e,0x3f666771,0x3edf28a9
.long 0x3f6b4b0c,0x3ec9b953,0x3f63f473,0x3ee900b7,0x3f696591,0x3ed25a09,0x3f61668a,0x3ef2bd43
.long 0x3f676bd8,0x3edae880,0x3f5ebe05,0x3efc5d27,0x3f655e0b,0x3ee363fa,0x3f5bfb34,0x3f02ef9f
.long 0x3f633c5a,0x3eebcbbb,0x3f591e6a,0x3f07a136,0x3f6106f2,0x3ef41f07,0x3f5627fe,0x3f0c42c9
.long 0x3f5ebe05,0x3efc5d27,0x3f531849,0x3f10d3cd,0x3f5c61c7,0x3f0242b1,0x3f4fefa8,0x3f1553b7
.long 0x3f59f26a,0x3f064b82,0x3f4cae79,0x3f19c200,0x3f577026,0x3f0a48ad,0x3f495521,0x3f1e1e22
.long 0x3f6c835e,0x3ec3ef15,0x3f676bd8,0x3edae880,0x3f6a09a7,0x3ecf7bca,0x3f644fac,0x3ee79a5d
.long 0x3f676bd8,0x3edae880,0x3f6106f2,0x3ef41f07,0x3f64aa59,0x3ee63375,0x3f5d924d,0x3f003a06
.long 0x3f61c598,0x3ef15aea,0x3f59f26a,0x3f064b82,0x3f5ebe05,0x3efc5d27,0x3f5627fe,0x3f0c42c9
.long 0x3f5b941a,0x3f039c3d,0x3f5233c6,0x3f121eb0,0x3f584853,0x3f08f59b,0x3f4e1689,0x3f17de12
.long 0x3f54db31,0x3f0e39da,0x3f49d112,0x3f1d7fd1,0x3f514d3d,0x3f13682a,0x3f456439,0x3f2302d3
.long 0x3f4d9f02,0x3f187fc0,0x3f40d0da,0x3f286605,0x3f49d112,0x3f1d7fd1,0x3f3c17d9,0x3f2da859
.long 0x3f45e403,0x3f226799,0x3f373a23,0x3f32c8c9,0x3f41d870,0x3f273656,0x3f3238aa,0x3f37c655
.long 0x3f3daef9,0x3f2beb4a,0x3f2d1469,0x3f3ca003,0x3f396842,0x3f3085bb,0x3f27ce61,0x3f4154e1
.long 0x3f61c598,0x3ef15aea,0x3f5b941a,0x3f039c3d,0x3f5df6be,0x3eff17b2,0x3f570348,0x3f0af1b7
.long 0x3f59f26a,0x3f064b82,0x3f5233c6,0x3f121eb0,0x3f55b993,0x3f0cead0,0x3f4d26fd,0x3f19210f
.long 0x3f514d3d,0x3f13682a,0x3f47de65,0x3f1ff6cb,0x3f4cae79,0x3f19c200,0x3f425b89,0x3f269de3
.long 0x3f47de65,0x3f1ff6cb,0x3f3ca003,0x3f2d1469,0x3f42de29,0x3f26050a,0x3f36ad7f,0x3f33587a
.long 0x3f3daef9,0x3f2beb4a,0x3f3085bb,0x3f396842,0x3f385216,0x3f31a81d,0x3f2a2a80,0x3f3f41fc
.long 0x3f32c8c9,0x3f373a23,0x3f239da9,0x3f44e3f5,0x3f2d1469,0x3f3ca003,0x3f1ce11f,0x3f4a4c87
.long 0x3f273656,0x3f41d870,0x3f15f6d9,0x3f4f7a1f,0x3f212ff9,0x3f46e22a,0x3f0ee0db,0x3f546b3b
.long 0x3f1b02c6,0x3f4bbbf8,0x3f07a136,0x3f591e6a,0x3f14b039,0x3f5064af,0x3f003a06,0x3f5d924d
.long 0x3f54db31,0x3f0e39da,0x3f4d9f02,0x3f187fc0,0x3f4f7a1f,0x3f15f6d9,0x3f476085,0x3f209393
.long 0x3f49d112,0x3f1d7fd1,0x3f40d0da,0x3f286605,0x3f43e200,0x3f24d225,0x3f39f2ac,0x3f2ff3e6
.long 0x3f3daef9,0x3f2beb4a,0x3f32c8c9,0x3f373a23,0x3f373a23,0x3f32c8c9,0x3f2b561b,0x3f3e35c5
.long 0x3f3085bb,0x3f396842,0x3f239da9,0x3f44e3f5,0x3f299415,0x3f3fc767,0x3f1ba299,0x3f4b41fa
.long 0x3f226799,0x3f45e403,0x3f13682a,0x3f514d3d,0x3f1b02c6,0x3f4bbbf8,0x3f0af1b7,0x3f570348
.long 0x3f13682a,0x3f514d3d,0x3f0242b1,0x3f5c61c7,0x3f0b9a6b,0x3f5695e5,0x3ef2bd43,0x3f61668a
.long 0x3f039c3d,0x3f5b941a,0x3ee0924f,0x3f660f88,0x3ef6e0cb,0x3f604621,0x3ece0bea,0x3f6a5ad9
.long 0x3ee63375,0x3f64aa59,0x3ebb31a0,0x3f6e46be,0x3ed53641,0x3f68bf3c,0x3ea80b1f,0x3f71d19f
.long 0x3f45e403,0x3f226799,0x3f3daef9,0x3f2beb4a,0x3f3ebc1b,0x3f2ac082,0x3f3592e7,0x3f34768f
.long 0x3f373a23,0x3f32c8c9,0x3f2d1469,0x3f3ca003,0x3f2f61a5,0x3f3a7ca4,0x3f24381a,0x3f446337
.long 0x3f273656,0x3f41d870,0x3f1b02c6,0x3f4bbbf8,0x3f1ebc12,0x3f48d8b3,0x3f11796b,0x3f52a649
.long 0x3f15f6d9,0x3f4f7a1f,0x3f07a136,0x3f591e6a,0x3f0cead0,0x3f55b993,0x3efafef7,0x3f5f20db
.long 0x3f039c3d,0x3f5b941a,0x3ee63375,0x3f64aa59,0x3ef41f07,0x3f6106f2,0x3ed0eb2a,0x3f69b7e4
.long 0x3ee0924f,0x3f660f88,0x3ebb31a0,0x3f6e46be,0x3ecc9b8b,0x3f6aab7b,0x3ea5129f,0x3f725470
.long 0x3eb8442a,0x3f6ed89e,0x3e8e9a22,0x3f75dec6,0x3ea395c5,0x3f7294f8,0x3e6fa8b2,0x3f78e3d6
.long 0x3e8e9a22,0x3f75dec6,0x3e419b37,0x3f7b61fc,0x3e72b651,0x3f78b47b,0x3e1324ca,0x3f7d57de
.long 0x3f3504f3,0x3f3504f3,0x3f2beb4a,0x3f3daef9,0x3f2beb4a,0x3f3daef9,0x3f21cbfb,0x3f466354
.long 0x3f226799,0x3f45e403,0x3f173c07,0x3f4e8d90,0x3f187fc0,0x3f4d9f02,0x3f0c42c9,0x3f5627fe
.long 0x3f0e39da,0x3f54db31,0x3f00e7e4,0x3f5d2d53,0x3f039c3d,0x3f5b941a,0x3eea6681,0x3f6398ac
.long 0x3ef15aea,0x3f61c598,0x3ed25a09,0x3f696591,0x3edae880,0x3f676bd8,0x3eb9bb1e,0x3f6e8ff8
.long 0x3ec3ef15,0x3f6c835e,0x3ea09ae5,0x3f731447,0x3eac7cd4,0x3f710908,0x3e870ada,0x3f76ef5b
.long 0x3e94a031,0x3f74fa0b,0x3e5a3997,0x3f7a1e84,0x3e78cfcc,0x3f7853f8,0x3e25c58c,0x3f7c9f8a
.long 0x3e47c5c2,0x3f7b14be,0x3de1bc2e,0x3f7e70b0,0x3e164083,0x3f7d3aac,0x3d6ea038,0x3f7f90b1
.long 0x3dc8bd36,0x3f7ec46d,0x3bc90f88,0x3f7ffec4,0x3d48fb30,0x3f7fb10f,0xbd3c6dd5,0x3f7fba9e
.long 0x3f226799,0x3f45e403,0x3f187fc0,0x3f4d9f02,0x3f173c07,0x3f4e8d90,0x3f0c42c9,0x3f5627fe
.long 0x3f0b9a6b,0x3f5695e5,0x3eff17b2,0x3f5df6be,0x3eff17b2,0x3f5df6be,0x3ee4cbfe,0x3f650479
.long 0x3ee63375,0x3f64aa59,0x3ec9b953,0x3f6b4b0c,0x3ecc9b8b,0x3f6aab7b,0x3eadf73c,0x3f70c501
.long 0x3eb263ef,0x3f6ff573,0x3e919ddd,0x3f756d97,0x3e97a117,0x3f748422,0x3e698bba,0x3f7940c0
.long 0x3e78cfcc,0x3f7853f8,0x3e2f10a2,0x3f7c3b28,0x3e419b37,0x3f7b61fc,0x3de7fa9a,0x3f7e5a38
.long 0x3e09cf86,0x3f7dabcc,0x3d621469,0x3f7f9c18,0x3da3308c,0x3f7f2f9d,0xbb490fc6,0x3f7fffb1
.long 0x3cc90ab0,0x3f7fec43,0xbd7b2b74,0x3f7f84ab,0xbcfb49ba,0x3f7fe129,0xbdf475c0,0x3f7e2b72
.long 0xbdafb680,0x3f7f0e58,0xbe354098,0x3f7bf531,0xbe1008b7,0x3f7d7474,0xbe6fa8b2,0x3f78e3d6
.long 0x3f0e39da,0x3f54db31,0x3f039c3d,0x3f5b941a,0x3f00e7e4,0x3f5d2d53,0x3eea6681,0x3f6398ac
.long 0x3ee63375,0x3f64aa59,0x3ecc9b8b,0x3f6aab7b,0x3ec9b953,0x3f6b4b0c,0x3eadf73c,0x3f70c501
.long 0x3eac7cd4,0x3f710908,0x3e8e9a22,0x3f75dec6,0x3e8e9a22,0x3f75dec6,0x3e5d4b1a,0x3f79f35e
.long 0x3e605c13,0x3f79c79d,0x3e1c76de,0x3f7cfe73,0x3e22abb6,0x3f7cbfc9,0x3db5f8da,0x3f7efcc9
.long 0x3dc8bd36,0x3f7ec46d,0x3cc90ab0,0x3f7fec43,0x3d16c32c,0x3f7fd397,0xbd2351cb,0x3f7fcbe2
.long 0xbcc90ab0,0x3f7fec43,0xbdd53db9,0x3f7e9bc9,0xbdafb680,0x3f7f0e58,0xbe2bf804,0x3f7c5d3a
.long 0xbe164083,0x3f7d3aac,0xbe6c9a7f,0x3f791298,0xbe541501,0x3f7a7302,0xbe9620d2,0x3f74bf62
.long 0xbe888e93,0x3f76ba07,0xbeb554ec,0x3f6f6830,0xbea68f12,0x3f721352,0xbed3c867,0x3f6912ae
.long 0x3ef15aea,0x3f61c598,0x3edae880,0x3f676bd8,0x3ed25a09,0x3f696591,0x3eb9bb1e,0x3f6e8ff8
.long 0x3eb263ef,0x3f6ff573,0x3e97a117,0x3f748422,0x3e919ddd,0x3f756d97,0x3e698bba,0x3f7940c0
.long 0x3e605c13,0x3f79c79d,0x3e22abb6,0x3f7cbfc9,0x3e1c76de,0x3f7cfe73,0x3db5f8da,0x3f7efcc9
.long 0x3dafb680,0x3f7f0e58,0x3c96c9b6,0x3f7ff4e6,0x3c96c9b6,0x3f7ff4e6,0xbd55880e,0x3f7fa6e3
.long 0xbd48fb30,0x3f7fb10f,0xbdfab273,0x3f7e1324,0xbdee3876,0x3f7e4323,0xbe44b0b9,0x3f7b3bab
.long 0xbe3b6ecf,0x3f7baccd,0xbe8586ce,0x3f772417,0xbe7ee6e1,0x3f77f110,0xbea80b1f,0x3f71d19f
.long 0xbea09ae5,0x3f731447,0xbec9b953,0x3f6b4b0c,0xbec1071e,0x3f6d1c1d,0xbeea6681,0x3f6398ac
.long 0xbee0924f,0x3f660f88,0xbf04f484,0x3f5ac450,0xbeff17b2,0x3f5df6be,0xbf140c5f,0x3f50d937
.long 0x3ec3ef15,0x3f6c835e,0x3eac7cd4,0x3f710908,0x3ea09ae5,0x3f731447,0x3e870ada,0x3f76ef5b
.long 0x3e78cfcc,0x3f7853f8,0x3e419b37,0x3f7b61fc,0x3e2f10a2,0x3f7c3b28,0x3de7fa9a,0x3f7e5a38
.long 0x3dc8bd36,0x3f7ec46d,0x3d16c32c,0x3f7fd397,0x3cc90ab0,0x3f7fec43,0xbd2351cb,0x3f7fcbe2
.long 0xbd48fb30,0x3f7fb10f,0xbdee3876,0x3f7e4323,0xbdfab273,0x3f7e1324,0xbe44b0b9,0x3f7b3bab
.long 0xbe47c5c2,0x3f7b14be,0xbe888e93,0x3f76ba07,0xbe888e93,0x3f76ba07,0xbeadf73c,0x3f70c501
.long 0xbeac7cd4,0x3f710908,0xbed25a09,0x3f696591,0xbecf7bca,0x3f6a09a7,0xbef58035,0x3f60a6cf
.long 0xbef15aea,0x3f61c598,0xbf0b9a6b,0x3f5695e5,0xbf08f59b,0x3f584853,0xbf1ba299,0x3f4b41fa
.long 0xbf187fc0,0x3f4d9f02,0xbf2ac082,0x3f3ebc1b,0xbf273656,0x3f41d870,0xbf38dd65,0x3f311722
.long 0x3e94a031,0x3f74fa0b,0x3e78cfcc,0x3f7853f8,0x3e5a3997,0x3f7a1e84,0x3e25c58c,0x3f7c9f8a
.long 0x3e09cf86,0x3f7dabcc,0x3da3308c,0x3f7f2f9d,0x3d621469,0x3f7f9c18,0xbb490fc6,0x3f7fffb1
.long 0xbcc90ab0,0x3f7fec43,0xbdafb680,0x3f7f0e58,0xbdd53db9,0x3f7e9bc9,0xbe2bf804,0x3f7c5d3a
.long 0xbe3b6ecf,0x3f7baccd,0xbe7ee6e1,0x3f77f110,0xbe8586ce,0x3f772417,0xbea80b1f,0x3f71d19f
.long 0xbeac7cd4,0x3f710908,0xbecf7bca,0x3f6a09a7,0xbed25a09,0x3f696591,0xbef58035,0x3f60a6cf
.long 0xbef6e0cb,0x3f604621,0xbf0cead0,0x3f55b993,0xbf0cead0,0x3f55b993,0xbf1e1e22,0x3f495521
.long 0xbf1d7fd1,0x3f49d112,0xbf2e3bde,0x3f3b8f3b,0xbf2d1469,0x3f3ca003,0xbf3d27b8,0x3f2c800f
.long 0xbf3b8f3b,0x3f2e3bde,0xbf4ac77f,0x3f1c420c,0xbf48d8b3,0x3f1ebc12,0xbf570348,0x3f0af1b7
.long 0x3e47c5c2,0x3f7b14be,0x3e164083,0x3f7d3aac,0x3de1bc2e,0x3f7e70b0,0x3d6ea038,0x3f7f90b1
.long 0x3cc90ab0,0x3f7fec43,0xbcfb49ba,0x3f7fe129,0xbd7b2b74,0x3f7f84ab,0xbdf475c0,0x3f7e2b72
.long 0xbe164083,0x3f7d3aac,0xbe541501,0x3f7a7302,0xbe6c9a7f,0x3f791298,0xbe9620d2,0x3f74bf62
.long 0xbea09ae5,0x3f731447,0xbec1071e,0x3f6d1c1d,0xbec9b953,0x3f6b4b0c,0xbeea6681,0x3f6398ac
.long 0xbef15aea,0x3f61c598,0xbf08f59b,0x3f584853,0xbf0b9a6b,0x3f5695e5,0xbf1ba299,0x3f4b41fa
.long 0xbf1d7fd1,0x3f49d112,0xbf2d1469,0x3f3ca003,0xbf2e3bde,0x3f3b8f3b,0xbf3d27b8,0x3f2c800f
.long 0xbf3daef9,0x3f2beb4a,0xbf4bbbf8,0x3f1b02c6,0xbf4bbbf8,0x3f1b02c6,0xbf58b3a1,0x3f084b92
.long 0xbf584853,0x3f08f59b,0xbf63f473,0x3ee900b7,0xbf633c5a,0x3eebcbbb,0xbf6d67a1,0x3ebf926f
.long 0x3dc8bd36,0x3f7ec46d,0x3d48fb30,0x3f7fb10f,0x3bc90f88,0x3f7ffec4,0xbd3c6dd5,0x3f7fba9e
.long 0xbdafb680,0x3f7f0e58,0xbe1008b7,0x3f7d7474,0xbe354098,0x3f7bf531,0xbe6fa8b2,0x3f78e3d6
.long 0xbe888e93,0x3f76ba07,0xbea68f12,0x3f721352,0xbeb554ec,0x3f6f6830,0xbed3c867,0x3f6912ae
.long 0xbee0924f,0x3f660f88,0xbeff17b2,0x3f5df6be,0xbf04f484,0x3f5ac450,0xbf140c5f,0x3f50d937
.long 0xbf187fc0,0x3f4d9f02,0xbf273656,0x3f41d870,0xbf2ac082,0x3f3ebc1b,0xbf38dd65,0x3f311722
.long 0xbf3b8f3b,0x3f2e3bde,0xbf48d8b3,0x3f1ebc12,0xbf4ac77f,0x3f1c420c,0xbf570348,0x3f0af1b7
.long 0xbf584853,0x3f08f59b,0xbf633c5a,0x3eebcbbb,0xbf63f473,0x3ee900b7,0xbf6d67a1,0x3ebf926f
.long 0xbf6db293,0x3ebe1d4a,0xbf756d97,0x3e919ddd,0xbf756d97,0x3e919ddd,0xbf7b3bab,0x3e44b0b9
.long 0x3f800000,0x0,0x3f7ec46d,0x3dc8bd36,0x3f800000,0x0,0x3f7eb069,0x3dcefdb7
.long 0x3f800000,0x0,0x3f7e9bc9,0x3dd53db9,0x3f800000,0x0,0x3f7e868b,0x3ddb7d37
.long 0x3f800000,0x0,0x3f7e70b0,0x3de1bc2e,0x3f800000,0x0,0x3f7e5a38,0x3de7fa9a
.long 0x3f800000,0x0,0x3f7e4323,0x3dee3876,0x3f800000,0x0,0x3f7e2b72,0x3df475c0
.long 0x3f800000,0x0,0x3f7e1324,0x3dfab273,0x3f800000,0x0,0x3f7dfa38,0x3e007745
.long 0x3f800000,0x0,0x3f7de0b1,0x3e039502,0x3f800000,0x0,0x3f7dc68c,0x3e06b26e
.long 0x3f800000,0x0,0x3f7dabcc,0x3e09cf86,0x3f800000,0x0,0x3f7d906e,0x3e0cec4a
.long 0x3f800000,0x0,0x3f7d7474,0x3e1008b7,0x3f800000,0x0,0x3f7d57de,0x3e1324ca
.long 0x3f7b14be,0x3e47c5c2,0x3f74fa0b,0x3e94a031,0x3f7ac516,0x3e4dee60,0x3f74484b,0x3e9920fe
.long 0x3f7a7302,0x3e541501,0x3f73913f,0x3e9d9e78,0x3f7a1e84,0x3e5a3997,0x3f72d4eb,0x3ea21887
.long 0x3f79c79d,0x3e605c13,0x3f721352,0x3ea68f12,0x3f796e4e,0x3e667c66,0x3f714c7a,0x3eab0201
.long 0x3f791298,0x3e6c9a7f,0x3f708066,0x3eaf713a,0x3f78b47b,0x3e72b651,0x3f6faf1b,0x3eb3dca5
.long 0x3f7853f8,0x3e78cfcc,0x3f6ed89e,0x3eb8442a,0x3f77f110,0x3e7ee6e1,0x3f6dfcf2,0x3ebca7af
.long 0x3f778bc5,0x3e827dc0,0x3f6d1c1d,0x3ec1071e,0x3f772417,0x3e8586ce,0x3f6c3624,0x3ec5625c
.long 0x3f76ba07,0x3e888e93,0x3f6b4b0c,0x3ec9b953,0x3f764d97,0x3e8b9507,0x3f6a5ad9,0x3ece0bea
.long 0x3f75dec6,0x3e8e9a22,0x3f696591,0x3ed25a09,0x3f756d97,0x3e919ddd,0x3f686b39,0x3ed6a399
.long 0x3f6c835e,0x3ec3ef15,0x3f61c598,0x3ef15aea,0x3f6b4b0c,0x3ec9b953,0x3f5fe4e9,0x3ef840c8
.long 0x3f6a09a7,0x3ecf7bca,0x3f5df6be,0x3eff17b2,0x3f68bf3c,0x3ed53641,0x3f5bfb34,0x3f02ef9f
.long 0x3f676bd8,0x3edae880,0x3f59f26a,0x3f064b82,0x3f660f88,0x3ee0924f,0x3f57dc7f,0x3f099f4e
.long 0x3f64aa59,0x3ee63375,0x3f55b993,0x3f0cead0,0x3f633c5a,0x3eebcbbb,0x3f5389c7,0x3f102dd5
.long 0x3f61c598,0x3ef15aea,0x3f514d3d,0x3f13682a,0x3f604621,0x3ef6e0cb,0x3f4f0417,0x3f16999f
.long 0x3f5ebe05,0x3efc5d27,0x3f4cae79,0x3f19c200,0x3f5d2d53,0x3f00e7e4,0x3f4a4c87,0x3f1ce11f
.long 0x3f5b941a,0x3f039c3d,0x3f47de65,0x3f1ff6cb,0x3f59f26a,0x3f064b82,0x3f456439,0x3f2302d3
.long 0x3f584853,0x3f08f59b,0x3f42de29,0x3f26050a,0x3f5695e5,0x3f0b9a6b,0x3f404c5c,0x3f28fd41
.long 0x3f54db31,0x3f0e39da,0x3f45e403,0x3f226799,0x3f5233c6,0x3f121eb0,0x3f425b89,0x3f269de3
.long 0x3f4f7a1f,0x3f15f6d9,0x3f3ebc1b,0x3f2ac082,0x3f4cae79,0x3f19c200,0x3f3b0629,0x3f2ecef7
.long 0x3f49d112,0x3f1d7fd1,0x3f373a23,0x3f32c8c9,0x3f46e22a,0x3f212ff9,0x3f33587a,0x3f36ad7f
.long 0x3f43e200,0x3f24d225,0x3f2f61a5,0x3f3a7ca4,0x3f40d0da,0x3f286605,0x3f2b561b,0x3f3e35c5
.long 0x3f3daef9,0x3f2beb4a,0x3f273656,0x3f41d870,0x3f3a7ca4,0x3f2f61a5,0x3f2302d3,0x3f456439
.long 0x3f373a23,0x3f32c8c9,0x3f1ebc12,0x3f48d8b3,0x3f33e7bc,0x3f36206c,0x3f1a6293,0x3f4c3578
.long 0x3f3085bb,0x3f396842,0x3f15f6d9,0x3f4f7a1f,0x3f2d1469,0x3f3ca003,0x3f11796b,0x3f52a649
.long 0x3f299415,0x3f3fc767,0x3f0cead0,0x3f55b993,0x3f26050a,0x3f42de29,0x3f084b92,0x3f58b3a1
.long 0x3f3504f3,0x3f3504f3,0x3f226799,0x3f45e403,0x3f3085bb,0x3f396842,0x3f1ce11f,0x3f4a4c87
.long 0x3f2beb4a,0x3f3daef9,0x3f173c07,0x3f4e8d90,0x3f273656,0x3f41d870,0x3f11796b,0x3f52a649
.long 0x3f226799,0x3f45e403,0x3f0b9a6b,0x3f5695e5,0x3f1d7fd1,0x3f49d112,0x3f05a02c,0x3f5a5ba0
.long 0x3f187fc0,0x3f4d9f02,0x3eff17b2,0x3f5df6be,0x3f13682a,0x3f514d3d,0x3ef2bd43,0x3f61668a
.long 0x3f0e39da,0x3f54db31,0x3ee63375,0x3f64aa59,0x3f08f59b,0x3f584853,0x3ed97cb9,0x3f67c187
.long 0x3f039c3d,0x3f5b941a,0x3ecc9b8b,0x3f6aab7b,0x3efc5d27,0x3f5ebe05,0x3ebf926f,0x3f6d67a1
.long 0x3ef15aea,0x3f61c598,0x3eb263ef,0x3f6ff573,0x3ee63375,0x3f64aa59,0x3ea5129f,0x3f725470
.long 0x3edae880,0x3f676bd8,0x3e97a117,0x3f748422,0x3ecf7bca,0x3f6a09a7,0x3e8a11f7,0x3f76841b
.long 0x3f0e39da,0x3f54db31,0x3ef15aea,0x3f61c598,0x3f07a136,0x3f591e6a,0x3ee1fb6a,0x3f65b710
.long 0x3f00e7e4,0x3f5d2d53,0x3ed25a09,0x3f696591,0x3ef41f07,0x3f6106f2,0x3ec27b55,0x3f6cd007
.long 0x3ee63375,0x3f64aa59,0x3eb263ef,0x3f6ff573,0x3ed8106b,0x3f6816a8,0x3ea21887,0x3f72d4eb
.long 0x3ec9b953,0x3f6b4b0c,0x3e919ddd,0x3f756d97,0x3ebb31a0,0x3f6e46be,0x3e80f8c0,0x3f77beb7
.long 0x3eac7cd4,0x3f710908,0x3e605c13,0x3f79c79d,0x3e9d9e78,0x3f73913f,0x3e3e853e,0x3f7b87b2
.long 0x3e8e9a22,0x3f75dec6,0x3e1c76de,0x3f7cfe73,0x3e7ee6e1,0x3f77f110,0x3df475c0,0x3f7e2b72
.long 0x3e605c13,0x3f79c79d,0x3dafb680,0x3f7f0e58,0x3e419b37,0x3f7b61fc,0x3d55880e,0x3f7fa6e3
.long 0x3e22abb6,0x3f7cbfc9,0x3c96c9b6,0x3f7ff4e6,0x3e039502,0x3f7de0b1,0xbc7b514b,0x3f7ff84a
.long 0x3ec3ef15,0x3f6c835e,0x3e94a031,0x3f74fa0b,0x3eb263ef,0x3f6ff573,0x3e80f8c0,0x3f77beb7
.long 0x3ea09ae5,0x3f731447,0x3e5a3997,0x3f7a1e84,0x3e8e9a22,0x3f75dec6,0x3e3228d4,0x3f7c187a
.long 0x3e78cfcc,0x3f7853f8,0x3e09cf86,0x3f7dabcc,0x3e541501,0x3f7a7302,0x3dc27c39,0x3f7ed7d4
.long 0x3e2f10a2,0x3f7c3b28,0x3d621469,0x3f7f9c18,0x3e09cf86,0x3f7dabcc,0x3c7b514b,0x3f7ff84a
.long 0x3dc8bd36,0x3f7ec46d,0xbcc90ab0,0x3f7fec43,0x3d7b2b74,0x3f7f84ab,0xbd83db0a,0x3f7f7808
.long 0x3cc90ab0,0x3f7fec43,0xbdd53db9,0x3f7e9bc9,0xbc490e90,0x3f7ffb11,0xbe1324ca,0x3f7d57de
.long 0xbd48fb30,0x3f7fb10f,0xbe3b6ecf,0x3f7baccd,0xbdafb680,0x3f7f0e58,0xbe636c83,0x3f799b43
.long 0xbdfab273,0x3f7e1324,0xbe8586ce,0x3f772417,0xbe22abb6,0x3f7cbfc9,0xbe9920fe,0x3f74484b
.long 0x3e47c5c2,0x3f7b14be,0x3dc8bd36,0x3f7ec46d,0x3e1c76de,0x3f7cfe73,0x3d55880e,0x3f7fa6e3
.long 0x3de1bc2e,0x3f7e70b0,0x3bc90f88,0x3f7ffec4,0x3d8a200a,0x3f7f6ac7,0xbd2351cb,0x3f7fcbe2
.long 0x3cc90ab0,0x3f7fec43,0xbdafb680,0x3f7f0e58,0xbc96c9b6,0x3f7ff4e6,0xbe06b26e,0x3f7dc68c
.long 0xbd7b2b74,0x3f7f84ab,0xbe354098,0x3f7bf531,0xbdd53db9,0x3f7e9bc9,0xbe636c83,0x3f799b43
.long 0xbe164083,0x3f7d3aac,0xbe888e93,0x3f76ba07,0xbe419b37,0x3f7b61fc,0xbe9f1cdf,0x3f73530e
.long 0xbe6c9a7f,0x3f791298,0xbeb554ec,0x3f6f6830,0xbe8b9507,0x3f764d97,0xbecb2aae,0x3f6afb8c
.long 0xbea09ae5,0x3f731447,0xbee0924f,0x3f660f88,0xbeb554ec,0x3f6f6830,0xbef58035,0x3f60a6cf
.long 0xbec9b953,0x3f6b4b0c,0xbf04f484,0x3f5ac450,0xbeddbe79,0x3f66becc,0xbf0ee0db,0x3f546b3b
.long 0x0,0x3f800000,0xbdc8bd36,0x3f7ec46d,0xbd48fb30,0x3f7fb10f,0xbe195be0,0x3f7d1cdd
.long 0xbdc8bd36,0x3f7ec46d,0xbe4dee60,0x3f7ac516,0xbe164083,0x3f7d3aac,0xbe80f8c0,0x3f77beb7
.long 0xbe47c5c2,0x3f7b14be,0xbe9aa086,0x3f740bdd,0xbe78cfcc,0x3f7853f8,0xbeb3dca5,0x3f6faf1b
.long 0xbe94a031,0x3f74fa0b,0xbecc9b8b,0x3f6aab7b,0xbeac7cd4,0x3f710908,0xbee4cbfe,0x3f650479
.long 0xbec3ef15,0x3f6c835e,0xbefc5d27,0x3f5ebe05,0xbedae880,0x3f676bd8,0xbf099f4e,0x3f57dc7f
.long 0xbef15aea,0x3f61c598,0xbf14b039,0x3f5064af,0xbf039c3d,0x3f5b941a,0xbf1f599f,0x3f485bca
.long 0xbf0e39da,0x3f54db31,0xbf299415,0x3f3fc767,0xbf187fc0,0x3f4d9f02,0xbf33587a,0x3f36ad7f
.long 0xbf226799,0x3f45e403,0xbf3ca003,0x3f2d1469,0xbf2beb4a,0x3f3daef9,0xbf456439,0x3f2302d3
.long 0xbe47c5c2,0x3f7b14be,0xbe94a031,0x3f74fa0b,0xbe7ee6e1,0x3f77f110,0xbeb0eacb,0x3f703b37
.long 0xbe9aa086,0x3f740bdd,0xbecc9b8b,0x3f6aab7b,0xbeb554ec,0x3f6f6830,0xbee79a5d,0x3f644fac
.long 0xbecf7bca,0x3f6a09a7,0xbf00e7e4,0x3f5d2d53,0xbee900b7,0x3f63f473,0xbf0d9281,0x3f554aa4
.long 0xbf00e7e4,0x3f5d2d53,0xbf19c200,0x3f4cae79,0xbf0cead0,0x3f55b993,0xbf256bcb,0x3f436051
.long 0xbf187fc0,0x3f4d9f02,0xbf3085bb,0x3f396842,0xbf239da9,0x3f44e3f5,0xbf3b0629,0x3f2ecef7
.long 0xbf2e3bde,0x3f3b8f3b,0xbf44e3f5,0x3f239da9,0xbf385216,0x3f31a81d,0xbf4e1689,0x3f17de12
.long 0xbf41d870,0x3f273656,0xbf5695e5,0x3f0b9a6b,0xbf4ac77f,0x3f1c420c,0xbf5e5aa6,0x3efdbabb
.long 0xbf531849,0x3f10d3cd,0xbf655e0b,0x3ee363fa,0xbf5ac450,0x3f04f484,0xbf6b99fb,0x3ec8477c
.long 0xbec3ef15,0x3f6c835e,0xbef15aea,0x3f61c598,0xbee0924f,0x3f660f88,0xbf06f686,0x3f5988ad
.long 0xbefc5d27,0x3f5ebe05,0xbf14b039,0x3f5064af,0xbf0b9a6b,0x3f5695e5,0xbf21cbfb,0x3f466354
.long 0xbf187fc0,0x3f4d9f02,0xbf2e3bde,0x3f3b8f3b,0xbf24d225,0x3f43e200,0xbf39f2ac,0x3f2ff3e6
.long 0xbf3085bb,0x3f396842,0xbf44e3f5,0x3f239da9,0xbf3b8f3b,0x3f2e3bde,0xbf4f0417,0x3f16999f
.long 0xbf45e403,0x3f226799,0xbf584853,0x3f08f59b,0xbf4f7a1f,0x3f15f6d9,0xbf60a6cf,0x3ef58035
.long 0xbf584853,0x3f08f59b,0xbf6816a8,0x3ed8106b,0xbf604621,0x3ef6e0cb,0xbf6e8ff8,0x3eb9bb1e
.long 0xbf676bd8,0x3edae880,0xbf740bdd,0x3e9aa086,0xbf6db293,0x3ebe1d4a,0xbf788486,0x3e75c35a
.long 0xbf731447,0x3ea09ae5,0xbf7bf531,0x3e354098,0xbf778bc5,0x3e827dc0,0xbf7e5a38,0x3de7fa9a
.long 0xbf0e39da,0x3f54db31,0xbf226799,0x3f45e403,0xbf1c420c,0x3f4ac77f,0xbf2ff3e6,0x3f39f2ac
.long 0xbf299415,0x3f3fc767,0xbf3ca003,0x3f2d1469,0xbf36206c,0x3f33e7bc,0xbf485bca,0x3f1f599f
.long 0xbf41d870,0x3f273656,0xbf531849,0x3f10d3cd,0xbf4cae79,0x3f19c200,0xbf5cc7d1,0x3f019573
.long 0xbf5695e5,0x3f0b9a6b,0xbf655e0b,0x3ee363fa,0xbf5f8327,0x3ef9a02d,0xbf6cd007,0x3ec27b55
.long 0xbf676bd8,0x3edae880,0xbf731447,0x3ea09ae5,0xbf6e46be,0x3ebb31a0,0xbf7822d1,0x3e7bdba4
.long 0xbf740bdd,0x3e9aa086,0xbf7bf531,0x3e354098,0xbf78b47b,0x3e72b651,0xbf7e868b,0x3ddb7d37
.long 0xbf7c3b28,0x3e2f10a2,0xbf7fd397,0x3d16c32c,0xbf7e9bc9,0x3dd53db9,0xbf7fdaaf,0xbd0a342f
.long 0xbf7fd397,0x3d16c32c,0xbf7e9bc9,0xbdd53db9,0xbf7fe129,0xbcfb49ba,0xbf7c187a,0xbe3228d4
.long 0xbf3504f3,0x3f3504f3,0xbf45e403,0x3f226799,0xbf41d870,0x3f273656,0xbf51c0c2,0x3f12c39a
.long 0xbf4d9f02,0x3f187fc0,0xbf5c61c7,0x3f0242b1,0xbf584853,0x3f08f59b,0xbf65b710,0x3ee1fb6a
.long 0xbf61c598,0x3ef15aea,0xbf6db293,0x3ebe1d4a,0xbf6a09a7,0x3ecf7bca,0xbf74484b,0x3e9920fe
.long 0xbf710908,0x3eac7cd4,0xbf796e4e,0x3e667c66,0xbf76ba07,0x3e888e93,0xbf7d1cdd,0x3e195be0
.long 0xbf7b14be,0x3e47c5c2,0xbf7f4e6d,0x3d96a905,0xbf7e1324,0x3dfab273,0xbf7fffb1,0xbb490fc6
.long 0xbf7fb10f,0x3d48fb30,0xbf7f2f9d,0xbda3308c,0xbf7fec43,0xbcc90ab0,0xbf7cdf6c,0xbe1f917b
.long 0xbf7ec46d,0xbdc8bd36,0xbf791298,0xbe6c9a7f,0xbf7c3b28,0xbe2f10a2,0xbf73ced9,0xbe9c1faf
.long 0xbf7853f8,0xbe78cfcc,0xbf6d1c1d,0xbec1071e,0xbf731447,0xbea09ae5,0xbf650479,0xbee4cbfe
.long 0xbf54db31,0x3f0e39da,0xbf61c598,0x3ef15aea,0xbf5f8327,0x3ef9a02d,0xbf6afb8c,0x3ecb2aae
.long 0xbf68bf3c,0x3ed53641,0xbf7294f8,0x3ea395c5,0xbf708066,0x3eaf713a,0xbf788486,0x3e75c35a
.long 0xbf76ba07,0x3e888e93,0xbf7cbfc9,0x3e22abb6,0xbf7b61fc,0x3e419b37,0xbf7f3f54,0x3d9cecf9
.long 0xbf7e70b0,0x3de1bc2e,0xbf7ffec4,0xbbc90f88,0xbf7fe129,0x3cfb49ba,0xbf7efcc9,0xbdb5f8da
.long 0xbf7fb10f,0xbd48fb30,0xbf7c3b28,0xbe2f10a2,0xbf7de0b1,0xbe039502,0xbf77beb7,0xbe80f8c0
.long 0xbf7a7302,0xbe541501,0xbf718f57,0xbea986c4,0xbf756d97,0xbe919ddd,0xbf69b7e4,0xbed0eb2a
.long 0xbf6ed89e,0xbeb8442a,0xbf604621,0xbef6e0cb,0xbf66becc,0xbeddbe79,0xbf554aa4,0xbf0d9281
.long 0xbf5d2d53,0xbf00e7e4,0xbf48d8b3,0xbf1ebc12,0xbf5233c6,0xbf121eb0,0xbf3b0629,0xbf2ecef7
.long 0xbf6c835e,0x3ec3ef15,0xbf74fa0b,0x3e94a031,0xbf740bdd,0x3e9aa086,0xbf7a9c59,0x3e5101f1
.long 0xbf79c79d,0x3e605c13,0xbf7e4323,0x3dee3876,0xbf7dabcc,0x3e09cf86,0xbf7fe705,0x3ce22a7a
.long 0xbf7fb10f,0x3d48fb30,0xbf7f84ab,0xbd7b2b74,0xbf7fd397,0xbd16c32c,0xbf7d1cdd,0xbe195be0
.long 0xbf7e1324,0xbdfab273,0xbf78b47b,0xbe72b651,0xbf7a7302,0xbe541501,0xbf725470,0xbea5129f
.long 0xbf74fa0b,0xbe94a031,0xbf6a09a7,0xbecf7bca,0xbf6db293,0xbebe1d4a,0xbf5fe4e9,0xbef840c8
.long 0xbf64aa59,0xbee63375,0xbf53fac3,0xbf0f8784,0xbf59f26a,0xbf064b82,0xbf466354,0xbf21cbfb
.long 0xbf4d9f02,0xbf187fc0,0xbf373a23,0xbf32c8c9,0xbf3fc767,0xbf299415,0xbf269de3,0xbf425b89
.long 0xbf3085bb,0xbf396842,0xbf14b039,0xbf5064af,0xbf1ff6cb,0xbf47de65,0xbf019573,0xbf5cc7d1
.long 0xbf7b14be,0x3e47c5c2,0xbf7ec46d,0x3dc8bd36,0xbf7e9bc9,0x3dd53db9,0xbf7fffb1,0x3b490fc6
.long 0xbf7ffb11,0x3c490e90,0xbf7eea9d,0xbdbc3ac3,0xbf7f2f9d,0xbda3308c,0xbf7b87b2,0xbe3e853e
.long 0xbf7c3b28,0xbe2f10a2,0xbf75dec6,0xbe8e9a22,0xbf772417,0xbe8586ce,0xbf6dfcf2,0xbebca7af
.long 0xbf6ff573,0xbeb263ef,0xbf63f473,0xbee900b7,0xbf66becc,0xbeddbe79,0xbf57dc7f,0xbf099f4e
.long 0xbf5b941a,0xbf039c3d,0xbf49d112,0xbf1d7fd1,0xbf4e8d90,0xbf173c07,0xbf39f2ac,0xbf2ff3e6
.long 0xbf3fc767,0xbf299415,0xbf286605,0xbf40d0da,0xbf2f61a5,0xbf3a7ca4,0xbf1553b7,0xbf4fefa8
.long 0xbf1d7fd1,0xbf49d112,0xbf00e7e4,0xbf5d2d53,0xbf0a48ad,0xbf577026,0xbed6a399,0xbf686b39
.long 0xbeebcbbb,0xbf633c5a,0xbea986c4,0xbf718f57,0xbec1071e,0xbf6d1c1d,0xbe75c35a,0xbf788486
.long 0x3f800000,0x0,0x3f7d3aac,0x3e164083,0x3f800000,0x0,0x3f7d1cdd,0x3e195be0
.long 0x3f800000,0x0,0x3f7cfe73,0x3e1c76de,0x3f800000,0x0,0x3f7cdf6c,0x3e1f917b
.long 0x3f800000,0x0,0x3f7cbfc9,0x3e22abb6,0x3f800000,0x0,0x3f7c9f8a,0x3e25c58c
.long 0x3f800000,0x0,0x3f7c7eb0,0x3e28defc,0x3f800000,0x0,0x3f7c5d3a,0x3e2bf804
.long 0x3f800000,0x0,0x3f7c3b28,0x3e2f10a2,0x3f800000,0x0,0x3f7c187a,0x3e3228d4
.long 0x3f800000,0x0,0x3f7bf531,0x3e354098,0x3f800000,0x0,0x3f7bd14d,0x3e3857ec
.long 0x3f800000,0x0,0x3f7baccd,0x3e3b6ecf,0x3f800000,0x0,0x3f7b87b2,0x3e3e853e
.long 0x3f800000,0x0,0x3f7b61fc,0x3e419b37,0x3f800000,0x0,0x3f7b3bab,0x3e44b0b9
.long 0x3f74fa0b,0x3e94a031,0x3f676bd8,0x3edae880,0x3f748422,0x3e97a117,0x3f666771,0x3edf28a9
.long 0x3f740bdd,0x3e9aa086,0x3f655e0b,0x3ee363fa,0x3f73913f,0x3e9d9e78,0x3f644fac,0x3ee79a5d
.long 0x3f731447,0x3ea09ae5,0x3f633c5a,0x3eebcbbb,0x3f7294f8,0x3ea395c5,0x3f62241a,0x3eeff7fb
.long 0x3f721352,0x3ea68f12,0x3f6106f2,0x3ef41f07,0x3f718f57,0x3ea986c4,0x3f5fe4e9,0x3ef840c8
.long 0x3f710908,0x3eac7cd4,0x3f5ebe05,0x3efc5d27,0x3f708066,0x3eaf713a,0x3f5d924d,0x3f003a06
.long 0x3f6ff573,0x3eb263ef,0x3f5c61c7,0x3f0242b1,0x3f6f6830,0x3eb554ec,0x3f5b2c79,0x3f044889
.long 0x3f6ed89e,0x3eb8442a,0x3f59f26a,0x3f064b82,0x3f6e46be,0x3ebb31a0,0x3f58b3a1,0x3f084b92
.long 0x3f6db293,0x3ebe1d4a,0x3f577026,0x3f0a48ad,0x3f6d1c1d,0x3ec1071e,0x3f5627fe,0x3f0c42c9
.long 0x3f54db31,0x3f0e39da,0x3f3daef9,0x3f2beb4a,0x3f531849,0x3f10d3cd,0x3f3b0629,0x3f2ecef7
.long 0x3f514d3d,0x3f13682a,0x3f385216,0x3f31a81d,0x3f4f7a1f,0x3f15f6d9,0x3f3592e7,0x3f34768f
.long 0x3f4d9f02,0x3f187fc0,0x3f32c8c9,0x3f373a23,0x3f4bbbf8,0x3f1b02c6,0x3f2ff3e6,0x3f39f2ac
.long 0x3f49d112,0x3f1d7fd1,0x3f2d1469,0x3f3ca003,0x3f47de65,0x3f1ff6cb,0x3f2a2a80,0x3f3f41fc
.long 0x3f45e403,0x3f226799,0x3f273656,0x3f41d870,0x3f43e200,0x3f24d225,0x3f24381a,0x3f446337
.long 0x3f41d870,0x3f273656,0x3f212ff9,0x3f46e22a,0x3f3fc767,0x3f299415,0x3f1e1e22,0x3f495521
.long 0x3f3daef9,0x3f2beb4a,0x3f1b02c6,0x3f4bbbf8,0x3f3b8f3b,0x3f2e3bde,0x3f17de12,0x3f4e1689
.long 0x3f396842,0x3f3085bb,0x3f14b039,0x3f5064af,0x3f373a23,0x3f32c8c9,0x3f11796b,0x3f52a649
.long 0x3f226799,0x3f45e403,0x3f039c3d,0x3f5b941a,0x3f1ebc12,0x3f48d8b3,0x3efdbabb,0x3f5e5aa6
.long 0x3f1b02c6,0x3f4bbbf8,0x3ef41f07,0x3f6106f2,0x3f173c07,0x3f4e8d90,0x3eea6681,0x3f6398ac
.long 0x3f13682a,0x3f514d3d,0x3ee0924f,0x3f660f88,0x3f0f8784,0x3f53fac3,0x3ed6a399,0x3f686b39
.long 0x3f0b9a6b,0x3f5695e5,0x3ecc9b8b,0x3f6aab7b,0x3f07a136,0x3f591e6a,0x3ec27b55,0x3f6cd007
.long 0x3f039c3d,0x3f5b941a,0x3eb8442a,0x3f6ed89e,0x3eff17b2,0x3f5df6be,0x3eadf73c,0x3f70c501
.long 0x3ef6e0cb,0x3f604621,0x3ea395c5,0x3f7294f8,0x3eee9479,0x3f628210,0x3e9920fe,0x3f74484b
.long 0x3ee63375,0x3f64aa59,0x3e8e9a22,0x3f75dec6,0x3eddbe79,0x3f66becc,0x3e840270,0x3f77583a
.long 0x3ed53641,0x3f68bf3c,0x3e72b651,0x3f78b47b,0x3ecc9b8b,0x3f6aab7b,0x3e5d4b1a,0x3f79f35e
.long 0x3ec3ef15,0x3f6c835e,0x3e78cfcc,0x3f7853f8,0x3eb8442a,0x3f6ed89e,0x3e5d4b1a,0x3f79f35e
.long 0x3eac7cd4,0x3f710908,0x3e419b37,0x3f7b61fc,0x3ea09ae5,0x3f731447,0x3e25c58c,0x3f7c9f8a
.long 0x3e94a031,0x3f74fa0b,0x3e09cf86,0x3f7dabcc,0x3e888e93,0x3f76ba07,0x3ddb7d37,0x3f7e868b
.long 0x3e78cfcc,0x3f7853f8,0x3da3308c,0x3f7f2f9d,0x3e605c13,0x3f79c79d,0x3d55880e,0x3f7fa6e3
.long 0x3e47c5c2,0x3f7b14be,0x3cc90ab0,0x3f7fec43,0x3e2f10a2,0x3f7c3b28,0xbb490fc6,0x3f7fffb1
.long 0x3e164083,0x3f7d3aac,0xbcfb49ba,0x3f7fe129,0x3dfab273,0x3f7e1324,0xbd6ea038,0x3f7f90b1
.long 0x3dc8bd36,0x3f7ec46d,0xbdafb680,0x3f7f0e58,0x3d96a905,0x3f7f4e6d,0xbde7fa9a,0x3f7e5a38
.long 0x3d48fb30,0x3f7fb10f,0xbe1008b7,0x3f7d7474,0x3cc90ab0,0x3f7fec43,0xbe2bf804,0x3f7c5d3a
.long 0x3dc8bd36,0x3f7ec46d,0xbd48fb30,0x3f7fb10f,0x3d8a200a,0x3f7f6ac7,0xbda973ba,0x3f7f1f49
.long 0x3d16c32c,0x3f7fd397,0xbdee3876,0x3f7e4323,0x3bc90f88,0x3f7ffec4,0xbe195be0,0x3f7d1cdd
.long 0xbcc90ab0,0x3f7fec43,0xbe3b6ecf,0x3f7baccd,0xbd621469,0x3f7f9c18,0xbe5d4b1a,0x3f79f35e
.long 0xbdafb680,0x3f7f0e58,0xbe7ee6e1,0x3f77f110,0xbdee3876,0x3f7e4323,0xbe901c2c,0x3f75a67b
.long 0xbe164083,0x3f7d3aac,0xbea09ae5,0x3f731447,0xbe354098,0x3f7bf531,0xbeb0eacb,0x3f703b37
.long 0xbe541501,0x3f7a7302,0xbec1071e,0x3f6d1c1d,0xbe72b651,0x3f78b47b,0xbed0eb2a,0x3f69b7e4
.long 0xbe888e93,0x3f76ba07,0xbee0924f,0x3f660f88,0xbe97a117,0x3f748422,0xbeeff7fb,0x3f62241a
.long 0xbea68f12,0x3f721352,0xbeff17b2,0x3f5df6be,0xbeb554ec,0x3f6f6830,0xbf06f686,0x3f5988ad
.long 0xbe47c5c2,0x3f7b14be,0xbeac7cd4,0x3f710908,0xbe6c9a7f,0x3f791298,0xbebf926f,0x3f6d67a1
.long 0xbe888e93,0x3f76ba07,0xbed25a09,0x3f696591,0xbe9aa086,0x3f740bdd,0xbee4cbfe,0x3f650479
.long 0xbeac7cd4,0x3f710908,0xbef6e0cb,0x3f604621,0xbebe1d4a,0x3f6db293,0xbf044889,0x3f5b2c79
.long 0xbecf7bca,0x3f6a09a7,0xbf0cead0,0x3f55b993,0xbee0924f,0x3f660f88,0xbf1553b7,0x3f4fefa8
.long 0xbef15aea,0x3f61c598,0xbf1d7fd1,0x3f49d112,0xbf00e7e4,0x3f5d2d53,0xbf256bcb,0x3f436051
.long 0xbf08f59b,0x3f584853,0xbf2d1469,0x3f3ca003,0xbf10d3cd,0x3f531849,0xbf34768f,0x3f3592e7
.long 0xbf187fc0,0x3f4d9f02,0xbf3b8f3b,0x3f2e3bde,0xbf1ff6cb,0x3f47de65,0xbf425b89,0x3f269de3
.long 0xbf273656,0x3f41d870,0xbf48d8b3,0x3f1ebc12,0xbf2e3bde,0x3f3b8f3b,0xbf4f0417,0x3f16999f
.long 0xbef15aea,0x3f61c598,0xbf187fc0,0x3f4d9f02,0xbf0242b1,0x3f5c61c7,0xbf21cbfb,0x3f466354
.long 0xbf0b9a6b,0x3f5695e5,0xbf2ac082,0x3f3ebc1b,0xbf14b039,0x3f5064af,0xbf33587a,0x3f36ad7f
.long 0xbf1d7fd1,0x3f49d112,0xbf3b8f3b,0x3f2e3bde,0xbf26050a,0x3f42de29,0xbf436051,0x3f256bcb
.long 0xbf2e3bde,0x3f3b8f3b,0xbf4ac77f,0x3f1c420c,0xbf36206c,0x3f33e7bc,0xbf51c0c2,0x3f12c39a
.long 0xbf3daef9,0x3f2beb4a,0xbf584853,0x3f08f59b,0xbf44e3f5,0x3f239da9,0xbf5e5aa6,0x3efdbabb
.long 0xbf4bbbf8,0x3f1b02c6,0xbf63f473,0x3ee900b7,0xbf5233c6,0x3f121eb0,0xbf6912ae,0x3ed3c867
.long 0xbf584853,0x3f08f59b,0xbf6db293,0x3ebe1d4a,0xbf5df6be,0x3eff17b2,0xbf71d19f,0x3ea80b1f
.long 0xbf633c5a,0x3eebcbbb,0xbf756d97,0x3e919ddd,0xbf6816a8,0x3ed8106b,0xbf788486,0x3e75c35a
.long 0xbf3504f3,0x3f3504f3,0xbf4d9f02,0x3f187fc0,0xbf3daef9,0x3f2beb4a,0xbf554aa4,0x3f0d9281
.long 0xbf45e403,0x3f226799,0xbf5c61c7,0x3f0242b1,0xbf4d9f02,0x3f187fc0,0xbf62df7b,0x3eed3063
.long 0xbf54db31,0x3f0e39da,0xbf68bf3c,0x3ed53641,0xbf5b941a,0x3f039c3d,0xbf6dfcf2,0x3ebca7af
.long 0xbf61c598,0x3ef15aea,0xbf7294f8,0x3ea395c5,0xbf676bd8,0x3edae880,0xbf76841b,0x3e8a11f7
.long 0xbf6c835e,0x3ec3ef15,0xbf79c79d,0x3e605c13,0xbf710908,0x3eac7cd4,0xbf7c5d3a,0x3e2bf804
.long 0xbf74fa0b,0x3e94a031,0xbf7e4323,0x3dee3876,0xbf7853f8,0x3e78cfcc,0xbf7f7808,0x3d83db0a
.long 0xbf7b14be,0x3e47c5c2,0xbf7ffb11,0x3c490e90,0xbf7d3aac,0x3e164083,0xbf7fcbe2,0xbd2351cb
.long 0xbf7ec46d,0x3dc8bd36,0xbf7eea9d,0xbdbc3ac3,0xbf7fb10f,0x3d48fb30,0xbf7d57de,0xbe1324ca
.long 0xbf61c598,0x3ef15aea,0xbf710908,0x3eac7cd4,0xbf6816a8,0x3ed8106b,0xbf75a67b,0x3e901c2c
.long 0xbf6db293,0x3ebe1d4a,0xbf796e4e,0x3e667c66,0xbf7294f8,0x3ea395c5,0xbf7c5d3a,0x3e2bf804
.long 0xbf76ba07,0x3e888e93,0xbf7e70b0,0x3de1bc2e,0xbf7a1e84,0x3e5a3997,0xbf7fa6e3,0x3d55880e
.long 0xbf7cbfc9,0x3e22abb6,0xbf7ffec4,0xbbc90f88,0xbf7e9bc9,0x3dd53db9,0xbf7f7808,0xbd83db0a
.long 0xbf7fb10f,0x3d48fb30,0xbf7e1324,0xbdfab273,0xbf7ffec4,0xbbc90f88,0xbf7bd14d,0xbe3857ec
.long 0xbf7f84ab,0xbd7b2b74,0xbf78b47b,0xbe72b651,0xbf7e4323,0xbdee3876,0xbf74bf62,0xbe9620d2
.long 0xbf7c3b28,0xbe2f10a2,0xbf6ff573,0xbeb263ef,0xbf796e4e,0xbe667c66,0xbf6a5ad9,0xbece0bea
.long 0xbf75dec6,0xbe8e9a22,0xbf63f473,0xbee900b7,0xbf718f57,0xbea986c4,0xbf5cc7d1,0xbf019573
.long 0xbf7b14be,0x3e47c5c2,0xbf7fb10f,0x3d48fb30,0xbf7dabcc,0x3e09cf86,0xbf7ff84a,0xbc7b514b
.long 0xbf7f4e6d,0x3d96a905,0xbf7f2f9d,0xbda3308c,0xbf7ffb11,0x3c490e90,0xbf7d57de,0xbe1324ca
.long 0xbf7fb10f,0xbd48fb30,0xbf7a7302,0xbe541501,0xbf7e70b0,0xbde1bc2e,0xbf76841b,0xbe8a11f7
.long 0xbf7c3b28,0xbe2f10a2,0xbf718f57,0xbea986c4,0xbf791298,0xbe6c9a7f,0xbf6b99fb,0xbec8477c
.long 0xbf74fa0b,0xbe94a031,0xbf64aa59,0xbee63375,0xbf6ff573,0xbeb263ef,0xbf5cc7d1,0xbf019573
.long 0xbf6a09a7,0xbecf7bca,0xbf53fac3,0xbf0f8784,0xbf633c5a,0xbeebcbbb,0xbf4a4c87,0xbf1ce11f
.long 0xbf5b941a,0xbf039c3d,0xbf3fc767,0xbf299415,0xbf531849,0xbf10d3cd,0xbf34768f,0xbf3592e7
.long 0xbf49d112,0xbf1d7fd1,0xbf286605,0xbf40d0da,0xbf3fc767,0xbf299415,0xbf1ba299,0xbf4b41fa
.long 0xbf7ec46d,0xbdc8bd36,0xbf7853f8,0xbe78cfcc,0xbf7c7eb0,0xbe28defc,0xbf73530e,0xbe9f1cdf
.long 0xbf791298,0xbe6c9a7f,0xbf6d1c1d,0xbec1071e,0xbf748422,0xbe97a117,0xbf65b710,0xbee1fb6a
.long 0xbf6ed89e,0xbeb8442a,0xbf5d2d53,0xbf00e7e4,0xbf6816a8,0xbed8106b,0xbf5389c7,0xbf102dd5
.long 0xbf604621,0xbef6e0cb,0xbf48d8b3,0xbf1ebc12,0xbf577026,0xbf0a48ad,0xbf3d27b8,0xbf2c800f
.long 0xbf4d9f02,0xbf187fc0,0xbf3085bb,0xbf396842,0xbf42de29,0xbf26050a,0xbf2302d3,0xbf456439
.long 0xbf373a23,0xbf32c8c9,0xbf14b039,0xbf5064af,0xbf2ac082,0xbf3ebc1b,0xbf05a02c,0xbf5a5ba0
.long 0xbf1d7fd1,0xbf49d112,0xbeebcbbb,0xbf633c5a,0xbf0f8784,0xbf53fac3,0xbecb2aae,0xbf6afb8c
.long 0xbf00e7e4,0xbf5d2d53,0xbea986c4,0xbf718f57,0xbee363fa,0xbf655e0b,0xbe870ada,0xbf76ef5b
.long 0xbf6c835e,0xbec3ef15,0xbf5b941a,0xbf039c3d,0xbf64aa59,0xbee63375,0xbf50d937,0xbf140c5f
.long 0xbf5b941a,0xbf039c3d,0xbf44e3f5,0xbf239da9,0xbf514d3d,0xbf13682a,0xbf37c655,0xbf3238aa
.long 0xbf45e403,0xbf226799,0xbf299415,0xbf3fc767,0xbf396842,0xbf3085bb,0xbf1a6293,0xbf4c3578
.long 0xbf2beb4a,0xbf3daef9,0xbf0a48ad,0xbf577026,0xbf1d7fd1,0xbf49d112,0xbef2bd43,0xbf61668a
.long 0xbf0e39da,0xbf54db31,0xbecf7bca,0xbf6a09a7,0xbefc5d27,0xbf5ebe05,0xbeab0201,0xbf714c7a
.long 0xbedae880,0xbf676bd8,0xbe8586ce,0xbf772417,0xbeb8442a,0xbf6ed89e,0xbe3e853e,0xbf7b87b2
.long 0xbe94a031,0xbf74fa0b,0xbde1bc2e,0xbf7e70b0,0xbe605c13,0xbf79c79d,0xbd0a342f,0xbf7fdaaf
.long 0xbe164083,0xbf7d3aac,0x3d2fe007,0xbf7fc38f,0xbd96a905,0xbf7f4e6d,0x3df475c0,0xbf7e2b72
.long 0xbf45e403,0xbf226799,0xbf2beb4a,0xbf3daef9,0xbf385216,0xbf31a81d,0xbf1ba299,0xbf4b41fa
.long 0xbf299415,0xbf3fc767,0xbf0a48ad,0xbf577026,0xbf19c200,0xbf4cae79,0xbeeff7fb,0xbf62241a
.long 0xbf08f59b,0xbf584853,0xbec9b953,0xbf6b4b0c,0xbeee9479,0xbf628210,0xbea21887,0xbf72d4eb
.long 0xbec9b953,0xbf6b4b0c,0xbe72b651,0xbf78b47b,0xbea395c5,0xbf7294f8,0xbe1f917b,0xbf7cdf6c
.long 0xbe78cfcc,0xbf7853f8,0xbd96a905,0xbf7f4e6d,0xbe28defc,0xbf7c7eb0,0x3c16cb58,0xbf7ffd39
.long 0xbdafb680,0xbf7f0e58,0x3dbc3ac3,0xbf7eea9d,0xbbc90f88,0xbf7ffec4,0x3e3228d4,0xbf7c187a
.long 0x3d96a905,0xbf7f4e6d,0x3e827dc0,0xbf778bc5,0x3e1c76de,0xbf7cfe73,0x3eab0201,0xbf714c7a
.long 0x3e6c9a7f,0xbf791298,0x3ed25a09,0xbf696591,0x3e9d9e78,0xbf73913f,0x3ef840c8,0xbf5fe4e9
.long 0xbf0e39da,0xbf54db31,0xbedae880,0xbf676bd8,0xbef6e0cb,0xbf604621,0xbeb0eacb,0xbf703b37
.long 0xbecf7bca,0xbf6a09a7,0xbe8586ce,0xbf772417,0xbea68f12,0xbf721352,0xbe3228d4,0xbf7c187a
.long 0xbe78cfcc,0xbf7853f8,0xbdafb680,0xbf7f0e58,0xbe22abb6,0xbf7cbfc9,0x3b490fc6,0xbf7fffb1
.long 0xbd96a905,0xbf7f4e6d,0x3dbc3ac3,0xbf7eea9d,0x3c490e90,0xbf7ffb11,0x3e3857ec,0xbf7bd14d
.long 0x3dc8bd36,0xbf7ec46d,0x3e888e93,0xbf76ba07,0x3e3b6ecf,0xbf7baccd,0x3eb3dca5,0xbf6faf1b
.long 0x3e888e93,0xbf76ba07,0x3eddbe79,0xbf66becc,0x3eb263ef,0xbf6ff573,0x3f02ef9f,0xbf5bfb34
.long 0x3edae880,0xbf676bd8,0x3f15f6d9,0xbf4f7a1f,0x3f00e7e4,0xbf5d2d53,0x3f27ce61,0xbf4154e1
.long 0x3f13682a,0xbf514d3d,0x3f385216,0xbf31a81d,0x3f24d225,0xbf43e200,0x3f476085,0xbf209393
.long 0xbe94a031,0xbf74fa0b,0xbe164083,0xbf7d3aac,0xbe4dee60,0xbf7ac516,0xbd55880e,0xbf7fa6e3
.long 0xbde1bc2e,0xbf7e70b0,0x3d2fe007,0xbf7fc38f,0xbc96c9b6,0xbf7ff4e6,0x3e0cec4a,0xbf7d906e
.long 0x3d96a905,0xbf7f4e6d,0x3e6c9a7f,0xbf791298,0x3e28defc,0xbf7c7eb0,0x3ea5129f,0xbf725470
.long 0x3e827dc0,0xbf778bc5,0x3ed25a09,0xbf696591,0x3eaf713a,0xbf708066,0x3efdbabb,0xbf5e5aa6
.long 0x3edae880,0xbf676bd8,0x3f13682a,0xbf514d3d,0x3f0242b1,0xbf5c61c7,0x3f269de3,0xbf425b89
.long 0x3f15f6d9,0xbf4f7a1f,0x3f385216,0xbf31a81d,0x3f286605,0xbf40d0da,0x3f485bca,0xbf1f599f
.long 0x3f396842,0xbf3085bb,0x3f5695e5,0xbf0b9a6b,0x3f48d8b3,0xbf1ebc12,0x3f62df7b,0xbeed3063
.long 0x3f5695e5,0xbf0b9a6b,0x3f6d1c1d,0xbec1071e,0x3f628210,0xbeee9479,0x3f75341d,0xbe931f35
.short 16256,16256,16256,16256,16256,16256,16256,16256,16256,16251,0,48712,49024,49019,0,15944
.short 16256,16237,49024,49005,16256,16237,49024,49005,16256,16213,0,16142,49024,48981,0,48910
.short 16256,16181,16256,16181,16256,16181,16256,16181,16256,16142,0,48981,49024,48910,0,16213
.short 16256,16068,49024,48836,16256,16068,49024,48836,16256,15944,0,16251,49024,48712,0,49019
.short 16256,0,16256,0,16256,0,16256,0,16256,48712,0,49019,49024,15944,0,16251
.short 16256,48836,49024,16068,16256,48836,49024,16068,16256,48910,0,16213,49024,16142,0,48981
.short 16256,48949,16256,48949,16256,48949,16256,48949,16256,48981,0,48910,49024,16213,0,16142
.short 16256,49005,49024,16237,16256,49005,49024,16237,16256,49019,0,15944,49024,16251,0,48712
.short 16256,16256,16256,16256,16256,16256,16256,16256,16237,16213,48836,48910,49005,48981,16068,16142
.short 16181,16068,48949,48836,16181,16068,48949,48836,16068,48712,16237,16251,48836,15944,49005,49019
.short 0,48949,0,48949,0,48949,0,48949,48836,49019,49005,48712,16068,16251,16237,15944
.short 48949,49005,16181,16237,48949,49005,16181,16237,49005,48910,16068,48981,16237,16142,48836,16213
.short 49024,0,49024,0,49024,0,49024,0,49005,16142,16068,16213,16237,48910,48836,48981
.short 48949,16237,16181,49005,48949,16237,16181,49005,48836,16251,49005,15944,16068,49019,16237,48712
.short 0,16181,0,16181,0,16181,0,16181,16068,15944,16237,49019,48836,48712,49005,16251
.short 16181,48836,48949,16068,16181,48836,48949,16068,16237,48981,48836,16142,49005,16213,16068,48910
.short 16256,16256,16256,16256,16256,16256,16256,16256,16181,16142,48949,48981,48949,48910,16181,16213
.short 0,48836,0,16068,0,48836,0,16068,48949,49019,16181,15944,16181,16251,48949,48712
.short 49024,48949,49024,48949,49024,48949,49024,48949,48949,15944,16181,16251,16181,48712,48949,49019
.short 0,16237,0,49005,0,16237,0,49005,16181,16213,48949,16142,48949,48981,16181,48910
.short 16256,0,16256,0,16256,0,16256,0,16181,48981,48949,48910,48949,16213,16181,16142
.short 0,49005,0,16237,0,49005,0,16237,48949,48712,16181,49019,16181,15944,48949,16251
.short 49024,16181,49024,16181,49024,16181,49024,16181,48949,16251,16181,48712,16181,49019,48949,15944
.short 0,16068,0,48836,0,16068,0,48836,16181,48910,48949,16213,48949,16142,16181,48981
.short 16256,16256,16256,16256,16256,16256,16256,16256,16068,15944,49005,49019,48836,48712,16237,16251
.short 48949,49005,16181,16237,48949,49005,16181,16237,49005,48910,48836,48981,16237,16142,16068,16213
.short 0,16181,0,16181,0,16181,0,16181,16237,16213,16068,48910,49005,48981,48836,16142
.short 16181,48836,48949,16068,16181,48836,48949,16068,48836,49019,16237,48712,16068,16251,49005,15944
.short 49024,0,49024,0,49024,0,49024,0,48836,16251,16237,15944,16068,49019,49005,48712
.short 16181,16068,48949,48836,16181,16068,48949,48836,16237,48981,16068,16142,49005,16213,48836,48910
.short 0,48949,0,48949,0,48949,0,48949,49005,16142,48836,16213,16237,48910,16068,48981
.short 48949,16237,16181,49005,48949,16237,16181,49005,16068,48712,49005,16251,48836,15944,16237,49019
.short 0,0,0,0,0,0,0,0,0,15944,16256,16251,0,48712,49024,49019
.short 0,16068,0,48836,0,16068,0,48836,0,16142,49024,48981,0,48910,16256,16213
.short 0,16181,0,16181,0,16181,0,16181,0,16213,16256,16142,0,48981,49024,48910
.short 0,16237,0,49005,0,16237,0,49005,0,16251,49024,48712,0,49019,16256,15944
.short 0,16256,0,16256,0,16256,0,16256,0,16251,16256,48712,0,49019,49024,15944
.short 0,16237,0,49005,0,16237,0,49005,0,16213,49024,16142,0,48981,16256,48910
.short 0,16181,0,16181,0,16181,0,16181,0,16142,16256,48981,0,48910,49024,16213
.short 0,16068,0,48836,0,16068,0,48836,0,15944,49024,16251,0,48712,16256,49019
.short 0,0,0,0,0,0,0,0,16068,16142,16237,16213,48836,48910,49005,48981
.short 16181,16237,48949,49005,16181,16237,48949,49005,16237,16251,48836,15944,49005,49019,16068,48712
.short 16256,16181,16256,16181,16256,16181,16256,16181,16237,15944,48836,49019,49005,48712,16068,16251
.short 16181,48836,48949,16068,16181,48836,48949,16068,16068,48981,16237,16142,48836,16213,49005,48910
.short 0,49024,0,49024,0,49024,0,49024,48836,48981,49005,16142,16068,16213,16237,48910
.short 48949,48836,16181,16068,48949,48836,16181,16068,49005,15944,16068,49019,16237,48712,48836,16251
.short 49024,16181,49024,16181,49024,16181,49024,16181,49005,16251,16068,15944,16237,49019,48836,48712
.short 48949,16237,16181,49005,48949,16237,16181,49005,48836,16142,49005,16213,16068,48910,16237,48981
.short 0,0,0,0,0,0,0,0,16181,16213,16181,16142,48949,48981,48949,48910
.short 16256,16237,49024,49005,16256,16237,49024,49005,16181,15944,16181,16251,48949,48712,48949,49019
.short 0,48949,0,48949,0,48949,0,48949,48949,49019,48949,15944,16181,16251,16181,48712
.short 49024,48836,16256,16068,49024,48836,16256,16068,48949,16142,48949,48981,16181,48910,16181,16213
.short 0,16256,0,16256,0,16256,0,16256,16181,16142,16181,48981,48949,48910,48949,16213
.short 16256,48836,49024,16068,16256,48836,49024,16068,16181,49019,16181,15944,48949,16251,48949,48712
.short 0,48949,0,48949,0,48949,0,48949,48949,15944,48949,16251,16181,48712,16181,49019
.short 49024,16237,16256,49005,49024,16237,16256,49005,48949,16213,48949,16142,16181,48981,16181,48910
.short 0,0,0,0,0,0,0,0,16237,16251,16068,15944,49005,49019,48836,48712
.short 16181,16068,48949,48836,16181,16068,48949,48836,48836,48981,16237,16142,16068,16213,49005,48910
.short 49024,48949,49024,48949,49024,48949,49024,48949,48836,16142,16237,16213,16068,48910,49005,48981
.short 16181,16237,48949,49005,16181,16237,48949,49005,16237,48712,16068,16251,49005,15944,48836,49019
.short 0,49024,0,49024,0,49024,0,49024,49005,48712,48836,16251,16237,15944,16068,49019
.short 48949,16237,16181,49005,48949,16237,16181,49005,16068,16142,49005,16213,48836,48910,16237,48981
.short 16256,48949,16256,48949,16256,48949,16256,48949,16068,48981,49005,16142,48836,16213,16237,48910
.short 48949,16068,16181,48836,48949,16068,16181,48836,49005,16251,48836,15944,16237,49019,16068,48712
.short 0,0,0,0,0,0,0,0,0,48712,49024,49019,0,15944,16256,16251
.short 0,48836,0,16068,0,48836,0,16068,0,48910,16256,16213,0,16142,49024,48981
.short 0,48949,0,48949,0,48949,0,48949,0,48981,49024,48910,0,16213,16256,16142
.short 0,49005,0,16237,0,49005,0,16237,0,49019,16256,15944,0,16251,49024,48712
.short 0,49024,0,49024,0,49024,0,49024,0,49019,49024,15944,0,16251,16256,48712
.short 0,49005,0,16237,0,49005,0,16237,0,48981,16256,48910,0,16213,49024,16142
.short 0,48949,0,48949,0,48949,0,48949,0,48910,49024,16213,0,16142,16256,48981
.short 0,48836,0,16068,0,48836,0,16068,0,48712,16256,49019,0,15944,49024,16251
.short 0,0,0,0,0,0,0,0,48836,48910,49005,48981,16068,16142,16237,16213
.short 48949,49005,16181,16237,48949,49005,16181,16237,49005,49019,16068,48712,16237,16251,48836,15944
.short 49024,48949,49024,48949,49024,48949,49024,48949,49005,48712,16068,16251,16237,15944,48836,49019
.short 48949,16068,16181,48836,48949,16068,16181,48836,48836,16213,49005,48910,16068,48981,16237,16142
.short 0,16256,0,16256,0,16256,0,16256,16068,16213,16237,48910,48836,48981,49005,16142
.short 16181,16068,48949,48836,16181,16068,48949,48836,16237,48712,48836,16251,49005,15944,16068,49019
.short 16256,48949,16256,48949,16256,48949,16256,48949,16237,49019,48836,48712,49005,16251,16068,15944
.short 16181,49005,48949,16237,16181,49005,48949,16237,16068,48910,16237,48981,48836,16142,49005,16213
.short 0,0,0,0,0,0,0,0,48949,48981,48949,48910,16181,16213,16181,16142
.short 49024,49005,16256,16237,49024,49005,16256,16237,48949,48712,48949,49019,16181,15944,16181,16251
.short 0,16181,0,16181,0,16181,0,16181,16181,16251,16181,48712,48949,49019,48949,15944
.short 16256,16068,49024,48836,16256,16068,49024,48836,16181,48910,16181,16213,48949,16142,48949,48981
.short 0,49024,0,49024,0,49024,0,49024,48949,48910,48949,16213,16181,16142,16181,48981
.short 49024,16068,16256,48836,49024,16068,16256,48836,48949,16251,48949,48712,16181,49019,16181,15944
.short 0,16181,0,16181,0,16181,0,16181,16181,48712,16181,49019,48949,15944,48949,16251
.short 16256,49005,49024,16237,16256,49005,49024,16237,16181,48981,16181,48910,48949,16213,48949,16142
.short 0,0,0,0,0,0,0,0,49005,49019,48836,48712,16237,16251,16068,15944
.short 48949,48836,16181,16068,48949,48836,16181,16068,16068,16213,49005,48910,48836,48981,16237,16142
.short 16256,16181,16256,16181,16256,16181,16256,16181,16068,48910,49005,48981,48836,16142,16237,16213
.short 48949,49005,16181,16237,48949,49005,16181,16237,49005,15944,48836,49019,16237,48712,16068,16251
.short 0,16256,0,16256,0,16256,0,16256,16237,15944,16068,49019,49005,48712,48836,16251
.short 16181,49005,48949,16237,16181,49005,48949,16237,48836,48910,16237,48981,16068,16142,49005,16213
.short 49024,16181,49024,16181,49024,16181,49024,16181,48836,16213,16237,48910,16068,48981,49005,16142
.short 16181,48836,48949,16068,16181,48836,48949,16068,16237,49019,16068,48712,49005,16251,48836,15944
.short 16256,49024,16256,49024,16256,49024,16256,49024,16256,49019,0,15944,49024,16251,0,48712
.short 16256,49005,49024,16237,16256,49005,49024,16237,16256,48981,0,48910,49024,16213,0,16142
.short 16256,48949,16256,48949,16256,48949,16256,48949,16256,48910,0,16213,49024,16142,0,48981
.short 16256,48836,49024,16068,16256,48836,49024,16068,16256,48712,0,49019,49024,15944,0,16251
.short 16256,0,16256,0,16256,0,16256,0,16256,15944,0,16251,49024,48712,0,49019
.short 16256,16068,49024,48836,16256,16068,49024,48836,16256,16142,0,48981,49024,48910,0,16213
.short 16256,16181,16256,16181,16256,16181,16256,16181,16256,16213,0,16142,49024,48981,0,48910
.short 16256,16237,49024,49005,16256,16237,49024,49005,16256,16251,0,48712,49024,49019,0,15944
.short 16256,49024,16256,49024,16256,49024,16256,49024,16237,48981,48836,16142,49005,16213,16068,48910
.short 16181,48836,48949,16068,16181,48836,48949,16068,16068,15944,16237,49019,48836,48712,49005,16251
.short 0,16181,0,16181,0,16181,0,16181,48836,16251,49005,15944,16068,49019,16237,48712
.short 48949,16237,16181,49005,48949,16237,16181,49005,49005,16142,16068,16213,16237,48910,48836,48981
.short 49024,0,49024,0,49024,0,49024,0,49005,48910,16068,48981,16237,16142,48836,16213
.short 48949,49005,16181,16237,48949,49005,16181,16237,48836,49019,49005,48712,16068,16251,16237,15944
.short 0,48949,0,48949,0,48949,0,48949,16068,48712,16237,16251,48836,15944,49005,49019
.short 16181,16068,48949,48836,16181,16068,48949,48836,16237,16213,48836,48910,49005,48981,16068,16142
.short 16256,49024,16256,49024,16256,49024,16256,49024,16181,48910,48949,16213,48949,16142,16181,48981
.short 0,16068,0,48836,0,16068,0,48836,48949,16251,16181,48712,16181,49019,48949,15944
.short 49024,16181,49024,16181,49024,16181,49024,16181,48949,48712,16181,49019,16181,15944,48949,16251
.short 0,49005,0,16237,0,49005,0,16237,16181,48981,48949,48910,48949,16213,16181,16142
.short 16256,0,16256,0,16256,0,16256,0,16181,16213,48949,16142,48949,48981,16181,48910
.short 0,16237,0,49005,0,16237,0,49005,48949,15944,16181,16251,16181,48712,48949,49019
.short 49024,48949,49024,48949,49024,48949,49024,48949,48949,49019,16181,15944,16181,16251,48949,48712
.short 0,48836,0,16068,0,48836,0,16068,16181,16142,48949,48981,48949,48910,16181,16213
.short 16256,49024,16256,49024,16256,49024,16256,49024,16068,48712,49005,16251,48836,15944,16237,49019
.short 48949,16237,16181,49005,48949,16237,16181,49005,49005,16142,48836,16213,16237,48910,16068,48981
.short 0,48949,0,48949,0,48949,0,48949,16237,48981,16068,16142,49005,16213,48836,48910
.short 16181,16068,48949,48836,16181,16068,48949,48836,48836,16251,16237,15944,16068,49019,49005,48712
.short 49024,0,49024,0,49024,0,49024,0,48836,49019,16237,48712,16068,16251,49005,15944
.short 16181,48836,48949,16068,16181,48836,48949,16068,16237,16213,16068,48910,49005,48981,48836,16142
.short 0,16181,0,16181,0,16181,0,16181,49005,48910,48836,48981,16237,16142,16068,16213
.short 48949,49005,16181,16237,48949,49005,16181,16237,16068,15944,49005,49019,48836,48712,16237,16251
.short 0,0,0,0,0,0,0,0,0,48712,16256,49019,0,15944,49024,16251
.short 0,48836,0,16068,0,48836,0,16068,0,48910,49024,16213,0,16142,16256,48981
.short 0,48949,0,48949,0,48949,0,48949,0,48981,16256,48910,0,16213,49024,16142
.short 0,49005,0,16237,0,49005,0,16237,0,49019,49024,15944,0,16251,16256,48712
.short 0,49024,0,49024,0,49024,0,49024,0,49019,16256,15944,0,16251,49024,48712
.short 0,49005,0,16237,0,49005,0,16237,0,48981,49024,48910,0,16213,16256,16142
.short 0,48949,0,48949,0,48949,0,48949,0,48910,16256,16213,0,16142,49024,48981
.short 0,48836,0,16068,0,48836,0,16068,0,48712,49024,49019,0,15944,16256,16251
.short 0,0,0,0,0,0,0,0,16068,48910,16237,48981,48836,16142,49005,16213
.short 16181,49005,48949,16237,16181,49005,48949,16237,16237,49019,48836,48712,49005,16251,16068,15944
.short 16256,48949,16256,48949,16256,48949,16256,48949,16237,48712,48836,16251,49005,15944,16068,49019
.short 16181,16068,48949,48836,16181,16068,48949,48836,16068,16213,16237,48910,48836,48981,49005,16142
.short 0,16256,0,16256,0,16256,0,16256,48836,16213,49005,48910,16068,48981,16237,16142
.short 48949,16068,16181,48836,48949,16068,16181,48836,49005,48712,16068,16251,16237,15944,48836,49019
.short 49024,48949,49024,48949,49024,48949,49024,48949,49005,49019,16068,48712,16237,16251,48836,15944
.short 48949,49005,16181,16237,48949,49005,16181,16237,48836,48910,49005,48981,16068,16142,16237,16213
.short 0,0,0,0,0,0,0,0,16181,48981,16181,48910,48949,16213,48949,16142
.short 16256,49005,49024,16237,16256,49005,49024,16237,16181,48712,16181,49019,48949,15944,48949,16251
.short 0,16181,0,16181,0,16181,0,16181,48949,16251,48949,48712,16181,49019,16181,15944
.short 49024,16068,16256,48836,49024,16068,16256,48836,48949,48910,48949,16213,16181,16142,16181,48981
.short 0,49024,0,49024,0,49024,0,49024,16181,48910,16181,16213,48949,16142,48949,48981
.short 16256,16068,49024,48836,16256,16068,49024,48836,16181,16251,16181,48712,48949,49019,48949,15944
.short 0,16181,0,16181,0,16181,0,16181,48949,48712,48949,49019,16181,15944,16181,16251
.short 49024,49005,16256,16237,49024,49005,16256,16237,48949,48981,48949,48910,16181,16213,16181,16142
.short 0,0,0,0,0,0,0,0,16237,49019,16068,48712,49005,16251,48836,15944
.short 16181,48836,48949,16068,16181,48836,48949,16068,48836,16213,16237,48910,16068,48981,49005,16142
.short 49024,16181,49024,16181,49024,16181,49024,16181,48836,48910,16237,48981,16068,16142,49005,16213
.short 16181,49005,48949,16237,16181,49005,48949,16237,16237,15944,16068,49019,49005,48712,48836,16251
.short 0,16256,0,16256,0,16256,0,16256,49005,15944,48836,49019,16237,48712,16068,16251
.short 48949,49005,16181,16237,48949,49005,16181,16237,16068,48910,49005,48981,48836,16142,16237,16213
.short 16256,16181,16256,16181,16256,16181,16256,16181,16068,16213,49005,48910,48836,48981,16237,16142
.short 48949,48836,16181,16068,48949,48836,16181,16068,49005,49019,48836,48712,16237,16251,16068,15944
.short 0,0,0,0,0,0,0,0,0,15944,49024,16251,0,48712,16256,49019
.short 0,16068,0,48836,0,16068,0,48836,0,16142,16256,48981,0,48910,49024,16213
.short 0,16181,0,16181,0,16181,0,16181,0,16213,49024,16142,0,48981,16256,48910
.short 0,16237,0,49005,0,16237,0,49005,0,16251,16256,48712,0,49019,49024,15944
.short 0,16256,0,16256,0,16256,0,16256,0,16251,49024,48712,0,49019,16256,15944
.short 0,16237,0,49005,0,16237,0,49005,0,16213,16256,16142,0,48981,49024,48910
.short 0,16181,0,16181,0,16181,0,16181,0,16142,49024,48981,0,48910,16256,16213
.short 0,16068,0,48836,0,16068,0,48836,0,15944,16256,16251,0,48712,49024,49019
.short 0,0,0,0,0,0,0,0,48836,16142,49005,16213,16068,48910,16237,48981
.short 48949,16237,16181,49005,48949,16237,16181,49005,49005,16251,16068,15944,16237,49019,48836,48712
.short 49024,16181,49024,16181,49024,16181,49024,16181,49005,15944,16068,49019,16237,48712,48836,16251
.short 48949,48836,16181,16068,48949,48836,16181,16068,48836,48981,49005,16142,16068,16213,16237,48910
.short 0,49024,0,49024,0,49024,0,49024,16068,48981,16237,16142,48836,16213,49005,48910
.short 16181,48836,48949,16068,16181,48836,48949,16068,16237,15944,48836,49019,49005,48712,16068,16251
.short 16256,16181,16256,16181,16256,16181,16256,16181,16237,16251,48836,15944,49005,49019,16068,48712
.short 16181,16237,48949,49005,16181,16237,48949,49005,16068,16142,16237,16213,48836,48910,49005,48981
.short 0,0,0,0,0,0,0,0,48949,16213,48949,16142,16181,48981,16181,48910
.short 49024,16237,16256,49005,49024,16237,16256,49005,48949,15944,48949,16251,16181,48712,16181,49019
.short 0,48949,0,48949,0,48949,0,48949,16181,49019,16181,15944,48949,16251,48949,48712
.short 16256,48836,49024,16068,16256,48836,49024,16068,16181,16142,16181,48981,48949,48910,48949,16213
.short 0,16256,0,16256,0,16256,0,16256,48949,16142,48949,48981,16181,48910,16181,16213
.short 49024,48836,16256,16068,49024,48836,16256,16068,48949,49019,48949,15944,16181,16251,16181,48712
.short 0,48949,0,48949,0,48949,0,48949,16181,15944,16181,16251,48949,48712,48949,49019
.short 16256,16237,49024,49005,16256,16237,49024,49005,16181,16213,16181,16142,48949,48981,48949,48910
.short 0,0,0,0,0,0,0,0,49005,16251,48836,15944,16237,49019,16068,48712
.short 48949,16068,16181,48836,48949,16068,16181,48836,16068,48981,49005,16142,48836,16213,16237,48910
.short 16256,48949,16256,48949,16256,48949,16256,48949,16068,16142,49005,16213,48836,48910,16237,48981
.short 48949,16237,16181,49005,48949,16237,16181,49005,49005,48712,48836,16251,16237,15944,16068,49019
.short 0,49024,0,49024,0,49024,0,49024,16237,48712,16068,16251,49005,15944,48836,49019
.short 16181,16237,48949,49005,16181,16237,48949,49005,48836,16142,16237,16213,16068,48910,49005,48981
.short 49024,48949,49024,48949,49024,48949,49024,48949,48836,48981,16237,16142,16068,16213,49005,48910
.short 16181,16068,48949,48836,16181,16068,48949,48836,16237,16251,16068,15944,49005,49019,48836,48712
.short 13668,13668,13668,13668,13668,13668,13668,13668,13668,13663,13650,13629,46436,46431,46418,46397
.short 13668,0,46436,0,13668,0,46436,0,13668,46130,46418,13565,46436,13362,13650,46333
.short 13668,13650,13601,13486,13668,13650,13601,13486,13668,13629,13486,46130,46436,46397,46254,13362
.short 13668,46254,46369,13650,13668,46254,46369,13650,13668,46333,46254,13663,46436,13565,13486,46431
.short 13668,13601,0,46369,13668,13601,0,46369,13668,13565,46254,46431,46436,46333,13486,13663
.short 13668,46369,0,13601,13668,46369,0,13601,13668,46397,13486,13362,46436,13629,46254,46130
.short 13668,13486,46369,46418,13668,13486,46369,46418,13668,13362,46418,46333,46436,46130,13650,13565
.short 13668,46418,13601,46254,13668,46418,13601,46254,13668,46431,13650,46397,46436,13663,46418,13629
.short 13668,13668,13668,13668,13668,13668,13668,13668,13601,13565,13486,13362,46369,46333,46254,46130
.short 13668,0,46436,0,13668,0,46436,0,13601,46397,46254,13663,46369,13629,13486,46431
.short 0,46254,46369,46418,0,46254,46369,46418,46369,46431,46418,46333,13601,13663,13650,13565
.short 0,46418,13601,13486,0,46418,13601,13486,46369,46130,13650,46397,13601,13362,46418,13629
.short 46436,46369,0,13601,46436,46369,0,13601,46369,13362,13650,13629,13601,46130,46418,46397
.short 46436,13601,0,46369,46436,13601,0,46369,46369,13663,46418,13565,13601,46431,13650,46333
.short 0,13650,13601,46254,0,13650,13601,46254,13601,13629,46254,46431,46369,46397,13486,13663
.short 0,13486,46369,13650,0,13486,46369,13650,13601,46333,13486,46130,46369,13565,46254,13362
.short 13668,13668,13668,13668,13668,13668,13668,13668,0,46130,46254,46333,0,13362,13486,13565
.short 13668,0,46436,0,13668,0,46436,0,0,46431,13486,13629,0,13663,46254,46397
.short 46436,46418,46369,46254,46436,46418,46369,46254,0,13565,13650,13663,0,46333,46418,46431
.short 46436,13486,13601,46418,46436,13486,13601,46418,0,13629,46418,13362,0,46397,13650,46130
.short 13668,13601,0,46369,13668,13601,0,46369,0,46397,46418,46130,0,13629,13650,13362
.short 13668,46369,0,13601,13668,46369,0,13601,0,46333,13650,46431,0,13565,46418,13663
.short 46436,46254,13601,13650,46436,46254,13601,13650,0,13663,13486,46397,0,46431,46254,13629
.short 46436,13650,46369,13486,46436,13650,46369,13486,0,13362,46254,13565,0,46130,13486,46333
.short 13668,13668,13668,13668,13668,13668,13668,13668,46369,46397,46418,46431,13601,13629,13650,13663
.short 13668,0,46436,0,13668,0,46436,0,46369,46333,13650,13362,13601,13565,46418,46130
.short 0,13486,13601,13650,0,13486,13601,13650,13601,13362,46254,46397,46369,46130,13486,13629
.short 0,13650,46369,46254,0,13650,46369,46254,13601,46431,13486,13565,46369,13663,46254,46333
.short 46436,46369,0,13601,46436,46369,0,13601,13601,13663,13486,46333,46369,46431,46254,13565
.short 46436,13601,0,46369,46436,13601,0,46369,13601,46130,46254,13629,46369,13362,13486,46397
.short 0,46418,46369,13486,0,46418,46369,13486,46369,13565,13650,46130,13601,46333,46418,13362
.short 0,46254,13601,46418,0,46254,13601,46418,46369,13629,46418,13663,13601,46397,13650,46431
.short 0,0,0,0,0,0,0,0,0,13362,13486,13565,0,46130,46254,46333
.short 0,13668,0,46436,0,13668,0,46436,0,13663,46254,46397,0,46431,13486,13629
.short 0,13486,13601,13650,0,13486,13601,13650,0,13565,13650,13663,0,46333,46418,46431
.short 0,13650,46369,46254,0,13650,46369,46254,0,13629,46418,13362,0,46397,13650,46130
.short 0,13601,13668,13601,0,13601,13668,13601,0,13629,13650,13362,0,46397,46418,46130
.short 0,13601,46436,13601,0,13601,46436,13601,0,13565,46418,13663,0,46333,13650,46431
.short 0,13650,13601,46254,0,13650,13601,46254,0,13663,13486,46397,0,46431,46254,13629
.short 0,13486,46369,13650,0,13486,46369,13650,0,13362,46254,13565,0,46130,13486,46333
.short 0,0,0,0,0,0,0,0,13601,13629,13650,13663,46369,46397,46418,46431
.short 0,13668,0,46436,0,13668,0,46436,13601,13565,46418,46130,46369,46333,13650,13362
.short 13668,13650,13601,13486,13668,13650,13601,13486,13601,13362,46254,46397,46369,46130,13486,13629
.short 13668,46254,46369,13650,13668,46254,46369,13650,13601,46431,13486,13565,46369,13663,46254,46333
.short 0,46369,46436,46369,0,46369,46436,46369,46369,46431,46254,13565,13601,13663,13486,46333
.short 0,46369,13668,46369,0,46369,13668,46369,46369,13362,13486,46397,13601,46130,46254,13629
.short 46436,46254,13601,13650,46436,46254,13601,13650,46369,13565,13650,46130,13601,46333,46418,13362
.short 46436,13650,46369,13486,46436,13650,46369,13486,46369,13629,46418,13663,13601,46397,13650,46431
.short 0,0,0,0,0,0,0,0,13668,13663,13650,13629,46436,46431,46418,46397
.short 0,13668,0,46436,0,13668,0,46436,13668,46130,46418,13565,46436,13362,13650,46333
.short 0,46254,46369,46418,0,46254,46369,46418,46436,46397,46254,13362,13668,13629,13486,46130
.short 0,46418,13601,13486,0,46418,13601,13486,46436,13565,13486,46431,13668,46333,46254,13663
.short 0,13601,13668,13601,0,13601,13668,13601,13668,13565,46254,46431,46436,46333,13486,13663
.short 0,13601,46436,13601,0,13601,46436,13601,13668,46397,13486,13362,46436,13629,46254,46130
.short 0,46418,46369,13486,0,46418,46369,13486,46436,46130,13650,13565,13668,13362,46418,46333
.short 0,46254,13601,46418,0,46254,13601,46418,46436,13663,46418,13629,13668,46431,13650,46397
.short 0,0,0,0,0,0,0,0,13601,13565,13486,13362,46369,46333,46254,46130
.short 0,13668,0,46436,0,13668,0,46436,13601,46397,46254,13663,46369,13629,13486,46431
.short 46436,46418,46369,46254,46436,46418,46369,46254,13601,13663,13650,13565,46369,46431,46418,46333
.short 46436,13486,13601,46418,46436,13486,13601,46418,13601,13362,46418,13629,46369,46130,13650,46397
.short 0,46369,46436,46369,0,46369,46436,46369,46369,13362,13650,13629,13601,46130,46418,46397
.short 0,46369,13668,46369,0,46369,13668,46369,46369,13663,46418,13565,13601,46431,13650,46333
.short 13668,13486,46369,46418,13668,13486,46369,46418,46369,46397,13486,13663,13601,13629,46254,46431
.short 13668,46418,13601,46254,13668,46418,13601,46254,46369,13565,46254,13362,13601,46333,13486,46130
.short 0,0,0,0,0,0,0,0,0,46130,46254,46333,0,13362,13486,13565
.short 0,46436,0,13668,0,46436,0,13668,0,46431,13486,13629,0,13663,46254,46397
.short 0,46254,46369,46418,0,46254,46369,46418,0,46333,46418,46431,0,13565,13650,13663
.short 0,46418,13601,13486,0,46418,13601,13486,0,46397,13650,46130,0,13629,46418,13362
.short 0,46369,46436,46369,0,46369,46436,46369,0,46397,46418,46130,0,13629,13650,13362
.short 0,46369,13668,46369,0,46369,13668,46369,0,46333,13650,46431,0,13565,46418,13663
.short 0,46418,46369,13486,0,46418,46369,13486,0,46431,46254,13629,0,13663,13486,46397
.short 0,46254,13601,46418,0,46254,13601,46418,0,46130,13486,46333,0,13362,46254,13565
.short 0,0,0,0,0,0,0,0,46369,46397,46418,46431,13601,13629,13650,13663
.short 0,46436,0,13668,0,46436,0,13668,46369,46333,13650,13362,13601,13565,46418,46130
.short 46436,46418,46369,46254,46436,46418,46369,46254,46369,46130,13486,13629,13601,13362,46254,46397
.short 46436,13486,13601,46418,46436,13486,13601,46418,46369,13663,46254,46333,13601,46431,13486,13565
.short 0,13601,13668,13601,0,13601,13668,13601,13601,13663,13486,46333,46369,46431,46254,13565
.short 0,13601,46436,13601,0,13601,46436,13601,13601,46130,46254,13629,46369,13362,13486,46397
.short 13668,13486,46369,46418,13668,13486,46369,46418,13601,46333,46418,13362,46369,13565,13650,46130
.short 13668,46418,13601,46254,13668,46418,13601,46254,13601,46397,13650,46431,46369,13629,46418,13663
.short 0,0,0,0,0,0,0,0,46436,46431,46418,46397,13668,13663,13650,13629
.short 0,46436,0,13668,0,46436,0,13668,46436,13362,13650,46333,13668,46130,46418,13565
.short 0,13486,13601,13650,0,13486,13601,13650,13668,13629,13486,46130,46436,46397,46254,13362
.short 0,13650,46369,46254,0,13650,46369,46254,13668,46333,46254,13663,46436,13565,13486,46431
.short 0,46369,46436,46369,0,46369,46436,46369,46436,46333,13486,13663,13668,13565,46254,46431
.short 0,46369,13668,46369,0,46369,13668,46369,46436,13629,46254,46130,13668,46397,13486,13362
.short 0,13650,13601,46254,0,13650,13601,46254,13668,13362,46418,46333,46436,46130,13650,13565
.short 0,13486,46369,13650,0,13486,46369,13650,13668,46431,13650,46397,46436,13663,46418,13629
.short 0,0,0,0,0,0,0,0,46369,46333,46254,46130,13601,13565,13486,13362
.short 0,46436,0,13668,0,46436,0,13668,46369,13629,13486,46431,13601,46397,46254,13663
.short 13668,13650,13601,13486,13668,13650,13601,13486,46369,46431,46418,46333,13601,13663,13650,13565
.short 13668,46254,46369,13650,13668,46254,46369,13650,46369,46130,13650,46397,13601,13362,46418,13629
.short 0,13601,13668,13601,0,13601,13668,13601,13601,46130,46418,46397,46369,13362,13650,13629
.short 0,13601,46436,13601,0,13601,46436,13601,13601,46431,13650,46333,46369,13663,46418,13565
.short 46436,46254,13601,13650,46436,46254,13601,13650,13601,13629,46254,46431,46369,46397,13486,13663
.short 46436,13650,46369,13486,46436,13650,46369,13486,13601,46333,13486,46130,46369,13565,46254,13362
.short 13668,46436,13668,46436,13668,46436,13668,46436,13668,46431,13650,46397,46436,13663,46418,13629
.short 13668,0,46436,0,13668,0,46436,0,13668,13362,46418,46333,46436,46130,13650,13565
.short 13668,46418,13601,46254,13668,46418,13601,46254,13668,46397,13486,13362,46436,13629,46254,46130
.short 13668,13486,46369,46418,13668,13486,46369,46418,13668,13565,46254,46431,46436,46333,13486,13663
.short 13668,46369,0,13601,13668,46369,0,13601,13668,46333,46254,13663,46436,13565,13486,46431
.short 13668,13601,0,46369,13668,13601,0,46369,13668,13629,13486,46130,46436,46397,46254,13362
.short 13668,46254,46369,13650,13668,46254,46369,13650,13668,46130,46418,13565,46436,13362,13650,46333
.short 13668,13650,13601,13486,13668,13650,13601,13486,13668,13663,13650,13629,46436,46431,46418,46397
.short 13668,46436,13668,46436,13668,46436,13668,46436,13601,46333,13486,46130,46369,13565,46254,13362
.short 13668,0,46436,0,13668,0,46436,0,13601,13629,46254,46431,46369,46397,13486,13663
.short 0,13486,46369,13650,0,13486,46369,13650,46369,13663,46418,13565,13601,46431,13650,46333
.short 0,13650,13601,46254,0,13650,13601,46254,46369,13362,13650,13629,13601,46130,46418,46397
.short 46436,13601,0,46369,46436,13601,0,46369,46369,46130,13650,46397,13601,13362,46418,13629
.short 46436,46369,0,13601,46436,46369,0,13601,46369,46431,46418,46333,13601,13663,13650,13565
.short 0,46418,13601,13486,0,46418,13601,13486,13601,46397,46254,13663,46369,13629,13486,46431
.short 0,46254,46369,46418,0,46254,46369,46418,13601,13565,13486,13362,46369,46333,46254,46130
.short 13668,46436,13668,46436,13668,46436,13668,46436,0,13362,46254,13565,0,46130,13486,46333
.short 13668,0,46436,0,13668,0,46436,0,0,13663,13486,46397,0,46431,46254,13629
.short 46436,13650,46369,13486,46436,13650,46369,13486,0,46333,13650,46431,0,13565,46418,13663
.short 46436,46254,13601,13650,46436,46254,13601,13650,0,46397,46418,46130,0,13629,13650,13362
.short 13668,46369,0,13601,13668,46369,0,13601,0,13629,46418,13362,0,46397,13650,46130
.short 13668,13601,0,46369,13668,13601,0,46369,0,13565,13650,13663,0,46333,46418,46431
.short 46436,13486,13601,46418,46436,13486,13601,46418,0,46431,13486,13629,0,13663,46254,46397
.short 46436,46418,46369,46254,46436,46418,46369,46254,0,46130,46254,46333,0,13362,13486,13565
.short 13668,46436,13668,46436,13668,46436,13668,46436,46369,13629,46418,13663,13601,46397,13650,46431
.short 13668,0,46436,0,13668,0,46436,0,46369,13565,13650,46130,13601,46333,46418,13362
.short 0,46254,13601,46418,0,46254,13601,46418,13601,46130,46254,13629,46369,13362,13486,46397
.short 0,46418,46369,13486,0,46418,46369,13486,13601,13663,13486,46333,46369,46431,46254,13565
.short 46436,13601,0,46369,46436,13601,0,46369,13601,46431,13486,13565,46369,13663,46254,46333
.short 46436,46369,0,13601,46436,46369,0,13601,13601,13362,46254,46397,46369,46130,13486,13629
.short 0,13650,46369,46254,0,13650,46369,46254,46369,46333,13650,13362,13601,13565,46418,46130
.short 0,13486,13601,13650,0,13486,13601,13650,46369,46397,46418,46431,13601,13629,13650,13663
.short 0,0,0,0,0,0,0,0,0,46130,13486,46333,0,13362,46254,13565
.short 0,46436,0,13668,0,46436,0,13668,0,46431,46254,13629,0,13663,13486,46397
.short 0,46254,13601,46418,0,46254,13601,46418,0,46333,13650,46431,0,13565,46418,13663
.short 0,46418,46369,13486,0,46418,46369,13486,0,46397,46418,46130,0,13629,13650,13362
.short 0,46369,13668,46369,0,46369,13668,46369,0,46397,13650,46130,0,13629,46418,13362
.short 0,46369,46436,46369,0,46369,46436,46369,0,46333,46418,46431,0,13565,13650,13663
.short 0,46418,13601,13486,0,46418,13601,13486,0,46431,13486,13629,0,13663,46254,46397
.short 0,46254,46369,46418,0,46254,46369,46418,0,46130,46254,46333,0,13362,13486,13565
.short 0,0,0,0,0,0,0,0,13601,46397,13650,46431,46369,13629,46418,13663
.short 0,46436,0,13668,0,46436,0,13668,13601,46333,46418,13362,46369,13565,13650,46130
.short 13668,46418,13601,46254,13668,46418,13601,46254,13601,46130,46254,13629,46369,13362,13486,46397
.short 13668,13486,46369,46418,13668,13486,46369,46418,13601,13663,13486,46333,46369,46431,46254,13565
.short 0,13601,46436,13601,0,13601,46436,13601,46369,13663,46254,46333,13601,46431,13486,13565
.short 0,13601,13668,13601,0,13601,13668,13601,46369,46130,13486,13629,13601,13362,46254,46397
.short 46436,13486,13601,46418,46436,13486,13601,46418,46369,46333,13650,13362,13601,13565,46418,46130
.short 46436,46418,46369,46254,46436,46418,46369,46254,46369,46397,46418,46431,13601,13629,13650,13663
.short 0,0,0,0,0,0,0,0,13668,46431,13650,46397,46436,13663,46418,13629
.short 0,46436,0,13668,0,46436,0,13668,13668,13362,46418,46333,46436,46130,13650,13565
.short 0,13486,46369,13650,0,13486,46369,13650,46436,13629,46254,46130,13668,46397,13486,13362
.short 0,13650,13601,46254,0,13650,13601,46254,46436,46333,13486,13663,13668,13565,46254,46431
.short 0,46369,13668,46369,0,46369,13668,46369,13668,46333,46254,13663,46436,13565,13486,46431
.short 0,46369,46436,46369,0,46369,46436,46369,13668,13629,13486,46130,46436,46397,46254,13362
.short 0,13650,46369,46254,0,13650,46369,46254,46436,13362,13650,46333,13668,46130,46418,13565
.short 0,13486,13601,13650,0,13486,13601,13650,46436,46431,46418,46397,13668,13663,13650,13629
.short 0,0,0,0,0,0,0,0,13601,46333,13486,46130,46369,13565,46254,13362
.short 0,46436,0,13668,0,46436,0,13668,13601,13629,46254,46431,46369,46397,13486,13663
.short 46436,13650,46369,13486,46436,13650,46369,13486,13601,46431,13650,46333,46369,13663,46418,13565
.short 46436,46254,13601,13650,46436,46254,13601,13650,13601,46130,46418,46397,46369,13362,13650,13629
.short 0,13601,46436,13601,0,13601,46436,13601,46369,46130,13650,46397,13601,13362,46418,13629
.short 0,13601,13668,13601,0,13601,13668,13601,46369,46431,46418,46333,13601,13663,13650,13565
.short 13668,46254,46369,13650,13668,46254,46369,13650,46369,13629,13486,46431,13601,46397,46254,13663
.short 13668,13650,13601,13486,13668,13650,13601,13486,46369,46333,46254,46130,13601,13565,13486,13362
.short 0,0,0,0,0,0,0,0,0,13362,46254,13565,0,46130,13486,46333
.short 0,13668,0,46436,0,13668,0,46436,0,13663,13486,46397,0,46431,46254,13629
.short 0,13486,46369,13650,0,13486,46369,13650,0,13565,46418,13663,0,46333,13650,46431
.short 0,13650,13601,46254,0,13650,13601,46254,0,13629,13650,13362,0,46397,46418,46130
.short 0,13601,46436,13601,0,13601,46436,13601,0,13629,46418,13362,0,46397,13650,46130
.short 0,13601,13668,13601,0,13601,13668,13601,0,13565,13650,13663,0,46333,46418,46431
.short 0,13650,46369,46254,0,13650,46369,46254,0,13663,46254,46397,0,46431,13486,13629
.short 0,13486,13601,13650,0,13486,13601,13650,0,13362,13486,13565,0,46130,46254,46333
.short 0,0,0,0,0,0,0,0,46369,13629,46418,13663,13601,46397,13650,46431
.short 0,13668,0,46436,0,13668,0,46436,46369,13565,13650,46130,13601,46333,46418,13362
.short 46436,13650,46369,13486,46436,13650,46369,13486,46369,13362,13486,46397,13601,46130,46254,13629
.short 46436,46254,13601,13650,46436,46254,13601,13650,46369,46431,46254,13565,13601,13663,13486,46333
.short 0,46369,13668,46369,0,46369,13668,46369,13601,46431,13486,13565,46369,13663,46254,46333
.short 0,46369,46436,46369,0,46369,46436,46369,13601,13362,46254,46397,46369,46130,13486,13629
.short 13668,46254,46369,13650,13668,46254,46369,13650,13601,13565,46418,46130,46369,46333,13650,13362
.short 13668,13650,13601,13486,13668,13650,13601,13486,13601,13629,13650,13663,46369,46397,46418,46431
.short 0,0,0,0,0,0,0,0,46436,13663,46418,13629,13668,46431,13650,46397
.short 0,13668,0,46436,0,13668,0,46436,46436,46130,13650,13565,13668,13362,46418,46333
.short 0,46254,13601,46418,0,46254,13601,46418,13668,46397,13486,13362,46436,13629,46254,46130
.short 0,46418,46369,13486,0,46418,46369,13486,13668,13565,46254,46431,46436,46333,13486,13663
.short 0,13601,46436,13601,0,13601,46436,13601,46436,13565,13486,46431,13668,46333,46254,13663
.short 0,13601,13668,13601,0,13601,13668,13601,46436,46397,46254,13362,13668,13629,13486,46130
.short 0,46418,13601,13486,0,46418,13601,13486,13668,46130,46418,13565,46436,13362,13650,46333
.short 0,46254,46369,46418,0,46254,46369,46418,13668,13663,13650,13629,46436,46431,46418,46397
.short 0,0,0,0,0,0,0,0,46369,13565,46254,13362,13601,46333,13486,46130
.short 0,13668,0,46436,0,13668,0,46436,46369,46397,13486,13663,13601,13629,46254,46431
.short 13668,46418,13601,46254,13668,46418,13601,46254,46369,13663,46418,13565,13601,46431,13650,46333
.short 13668,13486,46369,46418,13668,13486,46369,46418,46369,13362,13650,13629,13601,46130,46418,46397
.short 0,46369,13668,46369,0,46369,13668,46369,13601,13362,46418,13629,46369,46130,13650,46397
.short 0,46369,46436,46369,0,46369,46436,46369,13601,13663,13650,13565,46369,46431,46418,46333
.short 46436,13486,13601,46418,46436,13486,13601,46418,13601,46397,46254,13663,46369,13629,13486,46431
.short 46436,46418,46369,46254,46436,46418,46369,46254,13601,13565,13486,13362,46369,46333,46254,46130
.size spec_S2, 45056
.noaltmacro
.popsection
)TBL");
}

__global__ __launch_bounds__(256) void k_mega(const u16* x, const u16* wr, const u16* wi, const u16* wg, const u16* wu, const u16* wd, const u16* gw, char* ws, u16* out) {
  unsigned tid = threadIdx.x;
  asm volatile(R"ASM(
; ==== prologue (1 iteration(s))
s_getpc_b64 s[0:1]
L_pf:
s_mov_b32 s2, L_end-L_pf-4
v_lshlrev_b32 v7, 7, v0
v_min_u32 v8, s2, v7
global_load_dword v191, v8, s[0:1]
v_add_u32 v7, 32768, v7
v_min_u32 v8, s2, v7
global_load_dword v191, v8, s[0:1]
v_add_u32 v7, 32768, v7
v_lshrrev_b32 v1, 6, v0
v_and_b32 v178, 63, v0
v_readfirstlane_b32 s58, v1
s_and_b32 s66, s22, 7
s_lshr_b32 s67, s22, 3
s_and_b32 s82, s66, 3
s_and_b32 s83, s67, 3
s_lshl_b32 s57, s82, 2
s_add_u32 s57, s57, s83
s_lshr_b32 s56, s66, 2
s_lshl_b32 s56, s56, 3
s_lshr_b32 s67, s67, 2
s_add_u32 s56, s56, s67
s_mov_b32 s24, s4
s_and_b32 s25, s5, 65535
s_mov_b32 s26, 2359296
s_mov_b32 s27, 131072
s_mov_b32 s32, s10
s_and_b32 s33, s11, 65535
s_mov_b32 s34, 5308416
s_mov_b32 s35, 131072
s_sub_u32 s36, s12, 1024
s_subb_u32 s37, s13, 0
s_and_b32 s37, s37, 65535
s_mov_b32 s38, 5309440
s_mov_b32 s39, 131072
v_lshrrev_b32 v1, 3, v178
v_and_b32 v2, 7, v178
v_lshrrev_b32 v4, 1, v1
v_xor_b32 v4, v4, v2
s_mov_b32 s66, 1152
v_lshlrev_b32 v5, 4, v4
v_mad_u32_u24 v168, v1, s66, v5
v_xor_b32 v5, 64, v5
v_mad_u32_u24 v169, v1, s66, v5
v_lshlrev_b32 v3, 1, v1
v_and_b32 v3, 4, v3
v_xor_b32 v3, v3, v2
v_lshlrev_b32 v3, 4, v3
s_mov_b32 s66, 9216
v_mad_u32_u24 v181, v1, s66, v3
v_lshl_add_u32 v190, v1, 7, v3
s_lshl_b32 s70, s58, 11
s_lshl_b32 s71, s58, 10
s_lshl_b32 s72, s58, 12
s_lshl_b32 s66, s56, 7
s_lshl_b32 s67, s58, 5
s_add_u32 s66, s66, s67
s_mul_i32 s73, s66, 1152
s_mul_i32 s74, s57, 576
s_and_b32 s66, s57, 1
s_lshl_b32 s67, s66, 6
s_add_u32 s68, s74, s67
s_mov_b32 s95, s68
s_xor_b32 s66, s66, 1
s_lshl_b32 s66, s66, 9
s_add_u32 s84, s74, s66
s_mul_i32 s66, s58, 147456
s_add_u32 s75, s68, s66
s_add_u32 s84, s84, s66
s_add_u32 m0, s72, 98304
s_add_u32 s79, s73, 0
buffer_load_dwordx4 v168, s[24:27], s79 offen lds
s_add_u32 m0, s72, 99328
s_add_u32 s79, s73, 9216
buffer_load_dwordx4 v169, s[24:27], s79 offen lds
s_add_u32 m0, s72, 100352
s_add_u32 s79, s73, 18432
buffer_load_dwordx4 v168, s[24:27], s79 offen lds
s_add_u32 m0, s72, 101376
s_add_u32 s79, s73, 27648
buffer_load_dwordx4 v169, s[24:27], s79 offen lds
s_add_u32 s64, s75, 0
s_add_u32 m0, s72, 0
s_add_u32 s65, s64, 73728
buffer_load_dwordx4 v181, s[32:35], s64 offen lds
buffer_load_dwordx4 v181, s[36:39], s64 offen offset:1024 lds
s_add_u32 m0, s72, 2048
s_nop 0
buffer_load_dwordx4 v181, s[32:35], s65 offen lds
buffer_load_dwordx4 v181, s[36:39], s65 offen offset:1024 lds
s_add_u32 m0, s72, 114688
s_add_u32 s79, s73, 128
buffer_load_dwordx4 v168, s[24:27], s79 offen lds
s_add_u32 m0, s72, 115712
s_add_u32 s79, s73, 9344
buffer_load_dwordx4 v169, s[24:27], s79 offen lds
s_add_u32 m0, s72, 116736
s_add_u32 s79, s73, 18560
buffer_load_dwordx4 v168, s[24:27], s79 offen lds
s_add_u32 m0, s72, 117760
s_add_u32 s79, s73, 27776
buffer_load_dwordx4 v169, s[24:27], s79 offen lds
s_add_u32 s64, s75, 589824
s_add_u32 m0, s72, 16384
s_add_u32 s65, s64, 73728
buffer_load_dwordx4 v181, s[32:35], s64 offen lds
buffer_load_dwordx4 v181, s[36:39], s64 offen offset:1024 lds
s_add_u32 m0, s72, 18432
s_nop 0
buffer_load_dwordx4 v181, s[32:35], s65 offen lds
buffer_load_dwordx4 v181, s[36:39], s65 offen offset:1024 lds
s_mov_b32 s52, s16
s_and_b32 s53, s17, 65535
s_mov_b32 s54, 1152
s_mov_b32 s55, 131072
v_lshlrev_b32 v6, 4, v178
s_mov_b32 m0, 147456
s_mov_b32 s66, 0
buffer_load_dwordx4 v6, s[52:55], s66 offen lds
s_mov_b32 m0, 148480
s_mov_b32 s66, 1024
buffer_load_dwordx4 v6, s[52:55], s66 offen lds
s_and_b32 s59, s58, 1
s_lshr_b32 s60, s58, 1
s_mov_b32 s40, s18
s_and_b32 s41, s19, 65535
s_mov_b32 s42, 35450880
s_mov_b32 s43, 131072
s_mov_b32 s44, s6
s_and_b32 s45, s7, 65535
s_mov_b32 s46, 1183744
s_mov_b32 s47, 131072
s_mov_b32 s48, s8
s_and_b32 s49, s9, 65535
s_mov_b32 s50, 1183744
s_mov_b32 s51, 131072
s_getpc_b64 s[0:1]
s_add_u32 s0, s0, spec_T1@rel32@lo+4
s_addc_u32 s1, s1, spec_T1@rel32@hi+12
s_mul_i32 s66, s57, 73728
s_add_u32 s0, s0, s66
s_addc_u32 s1, s1, 0
s_mov_b32 s28, s0
s_and_b32 s29, s1, 65535
s_mov_b32 s30, 73728
s_mov_b32 s31, 131072
v_lshrrev_b32 v1, 4, v178
v_and_b32 v2, 15, v178
v_lshrrev_b32 v3, 2, v2
v_and_b32 v4, 3, v2
v_lshrrev_b32 v5, 1, v1
v_and_b32 v6, 1, v1
v_lshlrev_b32 v6, 1, v6
v_lshrrev_b32 v7, 1, v4
v_add_u32 v6, v6, v7
s_lshl_b32 s66, s60, 2
v_add_u32 v7, s66, v6
v_lshrrev_b32 v8, 1, v3
v_lshlrev_b32 v8, 2, v8
v_xor_b32 v7, v7, v8
v_lshlrev_b32 v7, 4, v7
v_and_b32 v8, 1, v4
v_lshl_add_u32 v7, v8, 3, v7
v_lshl_add_u32 v7, v3, 7, v7
v_lshl_add_u32 v179, v5, 11, v7
v_add_u32 v180, 32768, v179
v_lshl_add_u32 v8, v5, 3, v3
v_lshlrev_b32 v8, 6, v8
v_lshl_add_u32 v8, v4, 3, v8
v_and_b32 v6, 1, v1
v_xor_b32 v7, s60, v6
v_lshl_add_u32 v8, v7, 5, v8
v_lshl_add_u32 v184, v6, 10, v8
v_lshrrev_b32 v6, 2, v178
v_and_b32 v7, 3, v178
v_lshlrev_b32 v7, 4, v7
s_mov_b32 s66, 9216
v_mad_u32_u24 v185, v6, s66, v7
v_and_b32 v1, 31, v178
v_lshrrev_b32 v2, 5, v178
v_lshrrev_b32 v3, 1, v1
v_and_b32 v3, 7, v3
v_xor_b32 v3, v3, v2
v_lshlrev_b32 v3, 4, v3
v_lshl_add_u32 v3, v1, 7, v3
s_lshl_b32 s66, s59, 13
s_add_u32 s66, s66, 81920
s_lshl_b32 s67, s60, 12
s_add_u32 s68, s66, s67
s_xor_b32 s67, s67, 4096
s_add_u32 s69, s66, s67
v_xor_b32 v4, 0, v3
v_add_u32 v96, s68, v4
v_add_u32 v97, s69, v4
v_xor_b32 v4, 32, v3
v_add_u32 v98, s68, v4
v_add_u32 v99, s69, v4
v_xor_b32 v4, 64, v3
v_add_u32 v100, s68, v4
v_add_u32 v101, s69, v4
v_xor_b32 v4, 96, v3
v_add_u32 v102, s68, v4
v_add_u32 v103, s69, v4
s_cmp_eq_u32 s60, 1
s_cselect_b32 s80, 64, 0
s_cselect_b32 s81, 64, 0
s_lshl_b32 s66, s56, 4
s_add_u32 s66, s66, s57
s_mul_i32 s66, s66, 73728
s_add_u32 s66, s66, 7139328
s_lshl_b32 s67, s59, 12
s_add_u32 s66, s66, s67
s_lshl_b32 s67, s60, 13
s_add_u32 s68, s66, s67
s_lshl_b32 s67, s60, 11
s_add_u32 s69, s68, s67
v_lshlrev_b32 v3, 4, v178
v_add_u32 v182, s69, v3
s_xor_b32 s67, s67, 2048
s_add_u32 s69, s68, s67
v_add_u32 v183, s69, v3
s_mov_b32 s76, 3216550459
s_mov_b32 s77, 84148480
s_mov_b32 s78, 117834498
; ==== first (1 iteration(s))
s_waitcnt vmcnt(10)
s_barrier
ds_read_b128 a[0:3], v96 offset:16384
ds_read_b128 a[4:7], v97 offset:16384
ds_read_b64_tr_b16 v[128:129], v179
ds_read_b64_tr_b16 v[130:131], v179 offset:512
ds_read_b64_tr_b16 v[132:133], v179 offset:1024
ds_read_b64_tr_b16 v[134:135], v179 offset:1536
s_waitcnt lgkmcnt(0)
v_mfma_f32_32x32x16_bf16 v[0:15], v[128:131], a[0:3], 0
ds_read_b128 a[8:11], v98 offset:16384
ds_read_b128 a[12:15], v99 offset:16384
ds_read_b64_tr_b16 v[136:137], v179 offset:4096
ds_read_b64_tr_b16 v[138:139], v179 offset:4608
s_add_u32 m0, s72, 131072
s_add_u32 s79, s73, 256
buffer_load_dwordx4 v168, s[24:27], s79 offen lds
v_mfma_f32_32x32x16_bf16 v[16:31], v[128:131], a[4:7], 0
ds_read_b64_tr_b16 v[140:141], v179 offset:5120
ds_read_b64_tr_b16 v[142:143], v179 offset:5632
ds_read_b128 a[16:19], v100 offset:16384
ds_read_b128 a[20:23], v101 offset:16384
s_add_u32 m0, s72, 132096
s_add_u32 s79, s73, 9472
buffer_load_dwordx4 v169, s[24:27], s79 offen lds
v_mfma_f32_32x32x16_bf16 v[48:63], v[132:135], a[4:7], 0
ds_read_b64_tr_b16 v[144:145], v179 offset:8192
ds_read_b64_tr_b16 v[146:147], v179 offset:8704
ds_read_b64_tr_b16 v[148:149], v179 offset:9216
ds_read_b64_tr_b16 v[150:151], v179 offset:9728
s_add_u32 m0, s72, 133120
s_add_u32 s79, s73, 18688
buffer_load_dwordx4 v168, s[24:27], s79 offen lds
v_mfma_f32_32x32x16_bf16 v[32:47], v[132:135], a[0:3], 0
s_add_u32 m0, s72, 134144
s_add_u32 s79, s73, 27904
buffer_load_dwordx4 v169, s[24:27], s79 offen lds
s_waitcnt lgkmcnt(6)
v_mfma_f32_32x32x16_bf16 v[0:15], v[136:139], a[8:11], v[0:15]
ds_read_b128 a[24:27], v102 offset:16384
ds_read_b128 a[28:31], v103 offset:16384
ds_read_b64_tr_b16 v[128:129], v179 offset:12288
ds_read_b64_tr_b16 v[130:131], v179 offset:12800
s_add_u32 s64, s75, 1179648
s_add_u32 m0, s72, 32768
s_add_u32 s65, s64, 73728
v_mfma_f32_32x32x16_bf16 v[16:31], v[136:139], a[12:15], v[16:31]
ds_read_b64_tr_b16 v[132:133], v179 offset:13312
ds_read_b64_tr_b16 v[134:135], v179 offset:13824
buffer_load_dwordx4 v181, s[32:35], s64 offen lds
v_mfma_f32_32x32x16_bf16 v[48:63], v[140:143], a[12:15], v[48:63]
buffer_load_dwordx4 v181, s[36:39], s64 offen offset:1024 lds
s_add_u32 m0, s72, 34816
v_mfma_f32_32x32x16_bf16 v[32:47], v[140:143], a[8:11], v[32:47]
buffer_load_dwordx4 v181, s[32:35], s65 offen lds
s_waitcnt lgkmcnt(6)
v_mfma_f32_32x32x16_bf16 v[0:15], v[144:147], a[16:19], v[0:15]
s_waitcnt vmcnt(9)
s_barrier
ds_read_b128 a[32:35], v96 offset:32768
ds_read_b128 a[36:39], v97 offset:32768
ds_read_b64_tr_b16 v[136:137], v179 offset:16384
ds_read_b64_tr_b16 v[138:139], v179 offset:16896
buffer_load_dwordx4 v181, s[36:39], s65 offen offset:1024 lds
v_mfma_f32_32x32x16_bf16 v[16:31], v[144:147], a[20:23], v[16:31]
ds_read_b64_tr_b16 v[140:141], v179 offset:17408
ds_read_b64_tr_b16 v[142:143], v179 offset:17920
s_add_u32 m0, s72, 81920
s_add_u32 s79, s73, 384
buffer_load_dwordx4 v168, s[24:27], s79 offen lds
v_mfma_f32_32x32x16_bf16 v[48:63], v[148:151], a[20:23], v[48:63]
s_add_u32 m0, s72, 82944
s_add_u32 s79, s73, 9600
buffer_load_dwordx4 v169, s[24:27], s79 offen lds
v_mfma_f32_32x32x16_bf16 v[32:47], v[148:151], a[16:19], v[32:47]
s_add_u32 m0, s72, 83968
s_add_u32 s79, s73, 18816
buffer_load_dwordx4 v168, s[24:27], s79 offen lds
s_waitcnt lgkmcnt(6)
v_mfma_f32_32x32x16_bf16 v[0:15], v[128:131], a[24:27], v[0:15]
ds_read_b128 a[40:43], v98 offset:32768
ds_read_b128 a[44:47], v99 offset:32768
ds_read_b64_tr_b16 v[144:145], v179 offset:20480
ds_read_b64_tr_b16 v[146:147], v179 offset:20992
s_add_u32 m0, s72, 84992
s_add_u32 s79, s73, 28032
buffer_load_dwordx4 v169, s[24:27], s79 offen lds
v_mfma_f32_32x32x16_bf16 v[16:31], v[128:131], a[28:31], v[16:31]
ds_read_b64_tr_b16 v[148:149], v179 offset:21504
ds_read_b64_tr_b16 v[150:151], v179 offset:22016
s_add_u32 s64, s75, 1769472
s_add_u32 m0, s72, 49152
s_add_u32 s65, s64, 73728
v_mfma_f32_32x32x16_bf16 v[48:63], v[132:135], a[28:31], v[48:63]
buffer_load_dwordx4 v181, s[32:35], s64 offen lds
v_mov_b32 v152, 0
v_mfma_f32_32x32x16_bf16 v[32:47], v[132:135], a[24:27], v[32:47]
buffer_load_dwordx4 v181, s[36:39], s64 offen offset:1024 lds
s_add_u32 m0, s72, 51200
s_waitcnt lgkmcnt(6)
v_mfma_f32_32x32x16_bf16 v[0:15], v[136:139], a[32:35], v[0:15]
ds_read_b128 a[48:51], v100 offset:32768
ds_read_b128 a[52:55], v101 offset:32768
ds_read_b64_tr_b16 v[128:129], v179 offset:24576
ds_read_b64_tr_b16 v[130:131], v179 offset:25088
buffer_load_dwordx4 v181, s[32:35], s65 offen lds
v_mfma_f32_32x32x16_bf16 v[16:31], v[136:139], a[36:39], v[16:31]
ds_read_b64_tr_b16 v[132:133], v179 offset:25600
ds_read_b64_tr_b16 v[134:135], v179 offset:26112
buffer_load_dwordx4 v181, s[36:39], s65 offen offset:1024 lds
v_mfma_f32_32x32x16_bf16 v[48:63], v[140:143], a[36:39], v[48:63]
s_add_u32 s64, s75, 2359296
s_add_u32 m0, s72, 65536
s_add_u32 s65, s64, 73728
v_mfma_f32_32x32x16_bf16 v[32:47], v[140:143], a[32:35], v[32:47]
buffer_load_dwordx4 v181, s[32:35], s64 offen lds
s_waitcnt lgkmcnt(6)
v_mfma_f32_32x32x16_bf16 v[0:15], v[144:147], a[40:43], v[0:15]
ds_read_b128 a[56:59], v102 offset:32768
ds_read_b128 a[60:63], v103 offset:32768
ds_read_b64_tr_b16 v[136:137], v179 offset:28672
ds_read_b64_tr_b16 v[138:139], v179 offset:29184
buffer_load_dwordx4 v181, s[36:39], s64 offen offset:1024 lds
s_add_u32 m0, s72, 67584
v_mfma_f32_32x32x16_bf16 v[16:31], v[144:147], a[44:47], v[16:31]
ds_read_b64_tr_b16 v[140:141], v179 offset:29696
ds_read_b64_tr_b16 v[142:143], v179 offset:30208
buffer_load_dwordx4 v181, s[32:35], s65 offen lds
v_mfma_f32_32x32x16_bf16 v[48:63], v[148:151], a[44:47], v[48:63]
buffer_load_dwordx4 v181, s[36:39], s65 offen offset:1024 lds
buffer_load_dwordx2 v[188:189], v152, s[40:43], 0 offen sc1
v_mfma_f32_32x32x16_bf16 v[32:47], v[148:151], a[40:43], v[32:47]
s_waitcnt lgkmcnt(6)
v_mfma_f32_32x32x16_bf16 v[0:15], v[128:131], a[48:51], v[0:15]
s_waitcnt vmcnt(13)
s_barrier
ds_read_b128 a[64:67], v96 offset:49152
ds_read_b128 a[68:71], v97 offset:49152
ds_read_b64_tr_b16 v[144:145], v179 offset:32768
ds_read_b64_tr_b16 v[146:147], v179 offset:33280
s_add_u32 m0, s72, 98304
s_add_u32 s79, s73, 512
buffer_load_dwordx4 v168, s[24:27], s79 offen lds
v_mfma_f32_32x32x16_bf16 v[16:31], v[128:131], a[52:55], v[16:31]
ds_read_b64_tr_b16 v[148:149], v179 offset:33792
ds_read_b64_tr_b16 v[150:151], v179 offset:34304
s_add_u32 m0, s72, 99328
s_add_u32 s79, s73, 9728
buffer_load_dwordx4 v169, s[24:27], s79 offen lds
v_mfma_f32_32x32x16_bf16 v[48:63], v[132:135], a[52:55], v[48:63]
s_add_u32 m0, s72, 100352
s_add_u32 s79, s73, 18944
buffer_load_dwordx4 v168, s[24:27], s79 offen lds
v_mfma_f32_32x32x16_bf16 v[32:47], v[132:135], a[48:51], v[32:47]
s_add_u32 m0, s72, 101376
s_add_u32 s79, s73, 28160
buffer_load_dwordx4 v169, s[24:27], s79 offen lds
s_waitcnt lgkmcnt(6)
v_mfma_f32_32x32x16_bf16 v[0:15], v[136:139], a[56:59], v[0:15]
ds_read_b128 a[72:75], v98 offset:49152
ds_read_b128 a[76:79], v99 offset:49152
ds_read_b64_tr_b16 v[128:129], v179 offset:36864
ds_read_b64_tr_b16 v[130:131], v179 offset:37376
s_add_u32 s64, s75, 2949120
s_add_u32 m0, s72, 0
s_add_u32 s65, s64, 73728
v_mfma_f32_32x32x16_bf16 v[16:31], v[136:139], a[60:63], v[16:31]
ds_read_b64_tr_b16 v[132:133], v179 offset:37888
ds_read_b64_tr_b16 v[134:135], v179 offset:38400
buffer_load_dwordx4 v181, s[32:35], s64 offen lds
v_mfma_f32_32x32x16_bf16 v[48:63], v[140:143], a[60:63], v[48:63]
buffer_load_dwordx4 v181, s[36:39], s64 offen offset:1024 lds
s_add_u32 m0, s72, 2048
v_mfma_f32_32x32x16_bf16 v[32:47], v[140:143], a[56:59], v[32:47]
buffer_load_dwordx4 v181, s[32:35], s65 offen lds
s_waitcnt lgkmcnt(6)
v_mfma_f32_32x32x16_bf16 v[0:15], v[144:147], a[64:67], v[0:15]
ds_read_b128 a[80:83], v100 offset:49152
ds_read_b128 a[84:87], v101 offset:49152
ds_read_b64_tr_b16 v[136:137], v179 offset:40960
ds_read_b64_tr_b16 v[138:139], v179 offset:41472
buffer_load_dwordx4 v181, s[36:39], s65 offen offset:1024 lds
v_mfma_f32_32x32x16_bf16 v[16:31], v[144:147], a[68:71], v[16:31]
ds_read_b64_tr_b16 v[140:141], v179 offset:41984
ds_read_b64_tr_b16 v[142:143], v179 offset:42496
v_mfma_f32_32x32x16_bf16 v[48:63], v[148:151], a[68:71], v[48:63]
v_mfma_f32_32x32x16_bf16 v[32:47], v[148:151], a[64:67], v[32:47]
s_waitcnt lgkmcnt(6)
v_mfma_f32_32x32x16_bf16 v[0:15], v[128:131], a[72:75], v[0:15]
ds_read_b128 a[88:91], v102 offset:49152
ds_read_b128 a[92:95], v103 offset:49152
ds_read_b64_tr_b16 v[144:145], v179 offset:45056
ds_read_b64_tr_b16 v[146:147], v179 offset:45568
v_mfma_f32_32x32x16_bf16 v[16:31], v[128:131], a[76:79], v[16:31]
ds_read_b64_tr_b16 v[148:149], v179 offset:46080
ds_read_b64_tr_b16 v[150:151], v179 offset:46592
s_mul_i32 s86, s58, 9
v_mfma_f32_32x32x16_bf16 v[48:63], v[132:135], a[76:79], v[48:63]
v_add_u32 v152, s86, v178
v_mfma_f32_32x32x16_bf16 v[32:47], v[132:135], a[72:75], v[32:47]
s_waitcnt lgkmcnt(6)
v_mfma_f32_32x32x16_bf16 v[0:15], v[136:139], a[80:83], v[0:15]
s_waitcnt vmcnt(13)
s_barrier
ds_read_b128 a[96:99], v96
ds_read_b128 a[100:103], v97
ds_read_b64_tr_b16 v[128:129], v179 offset:49152
ds_read_b64_tr_b16 v[130:131], v179 offset:49664
s_add_u32 m0, s72, 114688
s_add_u32 s79, s73, 640
buffer_load_dwordx4 v168, s[24:27], s79 offen lds
v_mfma_f32_32x32x16_bf16 v[16:31], v[136:139], a[84:87], v[16:31]
ds_read_b64_tr_b16 v[132:133], v179 offset:50176
ds_read_b64_tr_b16 v[134:135], v179 offset:50688
s_add_u32 m0, s72, 115712
s_add_u32 s79, s73, 9856
buffer_load_dwordx4 v169, s[24:27], s79 offen lds
v_mfma_f32_32x32x16_bf16 v[48:63], v[140:143], a[84:87], v[48:63]
s_add_u32 m0, s72, 116736
s_add_u32 s79, s73, 19072
buffer_load_dwordx4 v168, s[24:27], s79 offen lds
v_mfma_f32_32x32x16_bf16 v[32:47], v[140:143], a[80:83], v[32:47]
s_add_u32 m0, s72, 117760
s_add_u32 s79, s73, 28288
buffer_load_dwordx4 v169, s[24:27], s79 offen lds
s_mul_i32 s87, s56, 36
s_waitcnt lgkmcnt(6)
v_mfma_f32_32x32x16_bf16 v[0:15], v[144:147], a[88:91], v[0:15]
ds_read_b128 a[104:107], v98
ds_read_b128 a[108:111], v99
ds_read_b64_tr_b16 v[136:137], v179 offset:53248
ds_read_b64_tr_b16 v[138:139], v179 offset:53760
s_add_u32 s64, s75, 3538944
s_add_u32 m0, s72, 16384
s_add_u32 s65, s64, 73728
v_mfma_f32_32x32x16_bf16 v[16:31], v[144:147], a[92:95], v[16:31]
ds_read_b64_tr_b16 v[140:141], v179 offset:54272
ds_read_b64_tr_b16 v[142:143], v179 offset:54784
buffer_load_dwordx4 v181, s[32:35], s64 offen lds
v_mfma_f32_32x32x16_bf16 v[48:63], v[148:151], a[92:95], v[48:63]
buffer_load_dwordx4 v181, s[36:39], s64 offen offset:1024 lds
s_add_u32 m0, s72, 18432
v_mfma_f32_32x32x16_bf16 v[32:47], v[148:151], a[88:91], v[32:47]
buffer_load_dwordx4 v181, s[32:35], s65 offen lds
s_waitcnt lgkmcnt(6)
v_mfma_f32_32x32x16_bf16 v[0:15], v[128:131], a[96:99], v[0:15]
ds_read_b128 a[112:115], v100
ds_read_b128 a[116:119], v101
ds_read_b64_tr_b16 v[144:145], v179 offset:57344
ds_read_b64_tr_b16 v[146:147], v179 offset:57856
buffer_load_dwordx4 v181, s[36:39], s65 offen offset:1024 lds
v_mfma_f32_32x32x16_bf16 v[16:31], v[128:131], a[100:103], v[16:31]
ds_read_b64_tr_b16 v[148:149], v179 offset:58368
ds_read_b64_tr_b16 v[150:151], v179 offset:58880
v_add_u32 v153, s87, v152
v_mfma_f32_32x32x16_bf16 v[48:63], v[132:135], a[100:103], v[48:63]
v_lshlrev_b32 v154, 7, v153
v_mfma_f32_32x32x16_bf16 v[32:47], v[132:135], a[96:99], v[32:47]
s_waitcnt lgkmcnt(6)
v_mfma_f32_32x32x16_bf16 v[0:15], v[136:139], a[104:107], v[0:15]
ds_read_b128 a[120:123], v102
ds_read_b128 a[124:127], v103
ds_read_b64_tr_b16 v[128:129], v179 offset:61440
ds_read_b64_tr_b16 v[130:131], v179 offset:61952
v_mfma_f32_32x32x16_bf16 v[16:31], v[136:139], a[108:111], v[16:31]
ds_read_b64_tr_b16 v[132:133], v179 offset:62464
ds_read_b64_tr_b16 v[134:135], v179 offset:62976
v_mfma_f32_32x32x16_bf16 v[48:63], v[140:143], a[108:111], v[48:63]
v_cmp_gt_u32 vcc, 9, v178
v_mov_b32 v155, 2113929216
s_nop 0
v_cndmask_b32 v154, v155, v154, vcc
v_mfma_f32_32x32x16_bf16 v[32:47], v[140:143], a[104:107], v[32:47]
s_waitcnt lgkmcnt(6)
v_mfma_f32_32x32x16_bf16 v[0:15], v[144:147], a[112:115], v[0:15]
s_waitcnt vmcnt(12)
s_barrier
ds_read_b128 a[128:131], v96 offset:16384
ds_read_b128 a[132:135], v97 offset:16384
ds_read_b64_tr_b16 v[136:137], v180 offset:32768
ds_read_b64_tr_b16 v[138:139], v180 offset:33280
s_add_u32 m0, s72, 131072
s_add_u32 s79, s73, 768
buffer_load_dwordx4 v168, s[24:27], s79 offen lds
v_mfma_f32_32x32x16_bf16 v[16:31], v[144:147], a[116:119], v[16:31]
ds_read_b64_tr_b16 v[140:141], v180 offset:33792
ds_read_b64_tr_b16 v[142:143], v180 offset:34304
s_add_u32 m0, s72, 132096
s_add_u32 s79, s73, 9984
buffer_load_dwordx4 v169, s[24:27], s79 offen lds
v_mfma_f32_32x32x16_bf16 v[48:63], v[148:151], a[116:119], v[48:63]
s_add_u32 m0, s72, 133120
s_add_u32 s79, s73, 19200
buffer_load_dwordx4 v168, s[24:27], s79 offen lds
v_mfma_f32_32x32x16_bf16 v[32:47], v[148:151], a[112:115], v[32:47]
s_add_u32 m0, s72, 134144
s_add_u32 s79, s73, 28416
buffer_load_dwordx4 v169, s[24:27], s79 offen lds
s_waitcnt lgkmcnt(6)
v_mfma_f32_32x32x16_bf16 v[0:15], v[128:131], a[120:123], v[0:15]
ds_read_b128 a[136:139], v98 offset:16384
ds_read_b128 a[140:143], v99 offset:16384
ds_read_b64_tr_b16 v[144:145], v180 offset:36864
ds_read_b64_tr_b16 v[146:147], v180 offset:37376
s_add_u32 s64, s75, 4128768
s_add_u32 m0, s72, 32768
s_add_u32 s65, s64, 73728
v_mfma_f32_32x32x16_bf16 v[16:31], v[128:131], a[124:127], v[16:31]
ds_read_b64_tr_b16 v[148:149], v180 offset:37888
ds_read_b64_tr_b16 v[150:151], v180 offset:38400
buffer_load_dwordx4 v181, s[32:35], s64 offen lds
v_mfma_f32_32x32x16_bf16 v[48:63], v[132:135], a[124:127], v[48:63]
buffer_load_dwordx4 v181, s[36:39], s64 offen offset:1024 lds
s_add_u32 m0, s72, 34816
v_mfma_f32_32x32x16_bf16 v[32:47], v[132:135], a[120:123], v[32:47]
buffer_load_dwordx4 v181, s[32:35], s65 offen lds
s_waitcnt lgkmcnt(6)
v_mfma_f32_32x32x16_bf16 v[0:15], v[136:139], a[128:131], v[0:15]
ds_read_b128 a[144:147], v100 offset:16384
ds_read_b128 a[148:151], v101 offset:16384
ds_read_b64_tr_b16 v[128:129], v180 offset:40960
ds_read_b64_tr_b16 v[130:131], v180 offset:41472
buffer_load_dwordx4 v181, s[36:39], s65 offen offset:1024 lds
v_mfma_f32_32x32x16_bf16 v[16:31], v[136:139], a[132:135], v[16:31]
ds_read_b64_tr_b16 v[132:133], v180 offset:41984
ds_read_b64_tr_b16 v[134:135], v180 offset:42496
buffer_load_dword v191, v154, s[28:31], 0 offen
v_mfma_f32_32x32x16_bf16 v[48:63], v[140:143], a[132:135], v[48:63]
v_mfma_f32_32x32x16_bf16 v[32:47], v[140:143], a[128:131], v[32:47]
s_mov_b32 s87, 9216
s_waitcnt lgkmcnt(6)
v_mfma_f32_32x32x16_bf16 v[0:15], v[144:147], a[136:139], v[0:15]
ds_read_b128 a[152:155], v102 offset:16384
ds_read_b128 a[156:159], v103 offset:16384
ds_read_b64_tr_b16 v[136:137], v180 offset:45056
ds_read_b64_tr_b16 v[138:139], v180 offset:45568
v_mfma_f32_32x32x16_bf16 v[16:31], v[144:147], a[140:143], v[16:31]
ds_read_b64_tr_b16 v[140:141], v180 offset:46080
ds_read_b64_tr_b16 v[142:143], v180 offset:46592
v_mfma_f32_32x32x16_bf16 v[48:63], v[148:151], a[140:143], v[48:63]
v_mul_lo_u32 v154, v153, s87
v_mfma_f32_32x32x16_bf16 v[32:47], v[148:151], a[136:139], v[32:47]
s_waitcnt lgkmcnt(6)
v_mfma_f32_32x32x16_bf16 v[0:15], v[128:131], a[144:147], v[0:15]
s_waitcnt vmcnt(13)
s_barrier
ds_read_b128 a[160:163], v96 offset:32768
ds_read_b128 a[164:167], v97 offset:32768
ds_read_b64_tr_b16 v[144:145], v179
ds_read_b64_tr_b16 v[146:147], v179 offset:512
s_add_u32 m0, s72, 81920
s_add_u32 s79, s73, 896
buffer_load_dwordx4 v168, s[24:27], s79 offen lds
v_mfma_f32_32x32x16_bf16 v[16:31], v[128:131], a[148:151], v[16:31]
ds_read_b64_tr_b16 v[148:149], v179 offset:1024
ds_read_b64_tr_b16 v[150:151], v179 offset:1536
s_add_u32 m0, s72, 82944
s_add_u32 s79, s73, 10112
buffer_load_dwordx4 v169, s[24:27], s79 offen lds
v_mfma_f32_32x32x16_bf16 v[48:63], v[132:135], a[148:151], v[48:63]
s_add_u32 m0, s72, 83968
s_add_u32 s79, s73, 19328
buffer_load_dwordx4 v168, s[24:27], s79 offen lds
v_mfma_f32_32x32x16_bf16 v[32:47], v[132:135], a[144:147], v[32:47]
s_add_u32 m0, s72, 84992
s_add_u32 s79, s73, 28544
buffer_load_dwordx4 v169, s[24:27], s79 offen lds
s_waitcnt lgkmcnt(6)
v_mfma_f32_32x32x16_bf16 v[0:15], v[136:139], a[152:155], v[0:15]
ds_read_b128 a[168:171], v98 offset:32768
ds_read_b128 a[172:175], v99 offset:32768
ds_read_b64_tr_b16 v[128:129], v179 offset:4096
ds_read_b64_tr_b16 v[130:131], v179 offset:4608
s_add_u32 s64, s75, 4718592
s_add_u32 m0, s72, 49152
s_add_u32 s65, s64, 73728
v_mfma_f32_32x32x16_bf16 v[16:31], v[136:139], a[156:159], v[16:31]
ds_read_b64_tr_b16 v[132:133], v179 offset:5120
ds_read_b64_tr_b16 v[134:135], v179 offset:5632
buffer_load_dwordx4 v181, s[32:35], s64 offen lds
v_mfma_f32_32x32x16_bf16 v[48:63], v[140:143], a[156:159], v[48:63]
buffer_load_dwordx4 v181, s[36:39], s64 offen offset:1024 lds
s_add_u32 m0, s72, 51200
v_cmp_gt_u32 vcc, 9, v178
v_mov_b32 v155, 2113929216
s_nop 0
v_cndmask_b32 v187, v155, v154, vcc
v_mfma_f32_32x32x16_bf16 v[32:47], v[140:143], a[152:155], v[32:47]
buffer_load_dwordx4 v181, s[32:35], s65 offen lds
s_waitcnt lgkmcnt(6)
v_mfma_f32_32x32x16_bf16 v[0:15], v[144:147], a[160:163], v[0:15]
ds_read_b128 a[176:179], v100 offset:32768
ds_read_b128 a[180:183], v101 offset:32768
ds_read_b64_tr_b16 v[136:137], v179 offset:8192
ds_read_b64_tr_b16 v[138:139], v179 offset:8704
buffer_load_dwordx4 v181, s[36:39], s65 offen offset:1024 lds
v_mfma_f32_32x32x16_bf16 v[16:31], v[144:147], a[164:167], v[16:31]
ds_read_b64_tr_b16 v[140:141], v179 offset:9216
ds_read_b64_tr_b16 v[142:143], v179 offset:9728
v_mfma_f32_32x32x16_bf16 v[48:63], v[148:151], a[164:167], v[48:63]
s_add_u32 s94, s95, 128
v_mfma_f32_32x32x16_bf16 v[32:47], v[148:151], a[160:163], v[32:47]
s_waitcnt lgkmcnt(6)
v_mfma_f32_32x32x16_bf16 v[0:15], v[128:131], a[168:171], v[0:15]
ds_read_b128 a[184:187], v102 offset:32768
ds_read_b128 a[188:191], v103 offset:32768
ds_read_b64_tr_b16 v[144:145], v179 offset:12288
ds_read_b64_tr_b16 v[146:147], v179 offset:12800
v_mfma_f32_32x32x16_bf16 v[16:31], v[128:131], a[172:175], v[16:31]
ds_read_b64_tr_b16 v[148:149], v179 offset:13312
ds_read_b64_tr_b16 v[150:151], v179 offset:13824
v_mfma_f32_32x32x16_bf16 v[48:63], v[132:135], a[172:175], v[48:63]
buffer_load_dword v191, v187, s[32:35], s94 offen
v_mfma_f32_32x32x16_bf16 v[32:47], v[132:135], a[168:171], v[32:47]
s_waitcnt lgkmcnt(6)
v_mfma_f32_32x32x16_bf16 v[0:15], v[136:139], a[176:179], v[0:15]
s_waitcnt vmcnt(14)
s_barrier
ds_read_b128 a[192:195], v96 offset:49152
ds_read_b128 a[196:199], v97 offset:49152
ds_read_b64_tr_b16 v[128:129], v179 offset:16384
ds_read_b64_tr_b16 v[130:131], v179 offset:16896
s_add_u32 m0, s72, 98304
s_add_u32 s79, s73, 1024
buffer_load_dwordx4 v168, s[24:27], s79 offen lds
v_mfma_f32_32x32x16_bf16 v[16:31], v[136:139], a[180:183], v[16:31]
ds_read_b64_tr_b16 v[132:133], v179 offset:17408
ds_read_b64_tr_b16 v[134:135], v179 offset:17920
s_add_u32 m0, s72, 99328
s_add_u32 s79, s73, 10240
buffer_load_dwordx4 v169, s[24:27], s79 offen lds
v_mfma_f32_32x32x16_bf16 v[48:63], v[140:143], a[180:183], v[48:63]
s_add_u32 m0, s72, 100352
s_add_u32 s79, s73, 19456
buffer_load_dwordx4 v168, s[24:27], s79 offen lds
v_mfma_f32_32x32x16_bf16 v[32:47], v[140:143], a[176:179], v[32:47]
s_add_u32 m0, s72, 101376
s_add_u32 s79, s73, 28672
buffer_load_dwordx4 v169, s[24:27], s79 offen lds
s_waitcnt lgkmcnt(6)
v_mfma_f32_32x32x16_bf16 v[0:15], v[144:147], a[184:187], v[0:15]
ds_read_b128 a[200:203], v98 offset:49152
ds_read_b128 a[204:207], v99 offset:49152
ds_read_b64_tr_b16 v[136:137], v179 offset:20480
ds_read_b64_tr_b16 v[138:139], v179 offset:20992
s_add_u32 s64, s75, 128
s_add_u32 m0, s72, 65536
s_add_u32 s65, s64, 73728
v_mfma_f32_32x32x16_bf16 v[16:31], v[144:147], a[188:191], v[16:31]
ds_read_b64_tr_b16 v[140:141], v179 offset:21504
ds_read_b64_tr_b16 v[142:143], v179 offset:22016
buffer_load_dwordx4 v181, s[32:35], s64 offen lds
s_add_u32 s94, s94, 1024
v_mfma_f32_32x32x16_bf16 v[48:63], v[148:151], a[188:191], v[48:63]
buffer_load_dwordx4 v181, s[36:39], s64 offen offset:1024 lds
s_add_u32 m0, s72, 67584
v_mfma_f32_32x32x16_bf16 v[32:47], v[148:151], a[184:187], v[32:47]
buffer_load_dwordx4 v181, s[32:35], s65 offen lds
s_waitcnt lgkmcnt(6)
v_mfma_f32_32x32x16_bf16 v[0:15], v[128:131], a[192:195], v[0:15]
ds_read_b128 a[208:211], v100 offset:49152
ds_read_b128 a[212:215], v101 offset:49152
ds_read_b64_tr_b16 v[144:145], v179 offset:24576
ds_read_b64_tr_b16 v[146:147], v179 offset:25088
buffer_load_dwordx4 v181, s[36:39], s65 offen offset:1024 lds
v_mfma_f32_32x32x16_bf16 v[16:31], v[128:131], a[196:199], v[16:31]
ds_read_b64_tr_b16 v[148:149], v179 offset:25600
ds_read_b64_tr_b16 v[150:151], v179 offset:26112
v_mfma_f32_32x32x16_bf16 v[48:63], v[132:135], a[196:199], v[48:63]
buffer_load_dword v191, v187, s[36:39], s94 offen
v_mfma_f32_32x32x16_bf16 v[32:47], v[132:135], a[192:195], v[32:47]
s_waitcnt lgkmcnt(6)
v_mfma_f32_32x32x16_bf16 v[0:15], v[136:139], a[200:203], v[0:15]
ds_read_b128 a[216:219], v102 offset:49152
ds_read_b128 a[220:223], v103 offset:49152
ds_read_b64_tr_b16 v[128:129], v179 offset:28672
ds_read_b64_tr_b16 v[130:131], v179 offset:29184
v_mfma_f32_32x32x16_bf16 v[16:31], v[136:139], a[204:207], v[16:31]
ds_read_b64_tr_b16 v[132:133], v179 offset:29696
ds_read_b64_tr_b16 v[134:135], v179 offset:30208
v_mfma_f32_32x32x16_bf16 v[48:63], v[140:143], a[204:207], v[48:63]
v_mfma_f32_32x32x16_bf16 v[32:47], v[140:143], a[200:203], v[32:47]
s_waitcnt lgkmcnt(6)
v_mfma_f32_32x32x16_bf16 v[0:15], v[144:147], a[208:211], v[0:15]
s_waitcnt vmcnt(14)
s_barrier
ds_read_b128 a[224:227], v96
ds_read_b128 a[228:231], v97
ds_read_b64_tr_b16 v[136:137], v179 offset:32768
ds_read_b64_tr_b16 v[138:139], v179 offset:33280
s_add_u32 m0, s70, 150016
s_add_u32 s79, s70, 0
v_mfma_f32_32x32x16_bf16 v[16:31], v[144:147], a[212:215], v[16:31]
ds_read_b64_tr_b16 v[140:141], v179 offset:33792
ds_read_b64_tr_b16 v[142:143], v179 offset:34304
buffer_load_dwordx4 v190, s[28:31], s79 offen lds
v_mfma_f32_32x32x16_bf16 v[48:63], v[148:151], a[212:215], v[48:63]
buffer_load_dwordx4 v190, s[28:31], s79 offen offset:1024 lds
v_mfma_f32_32x32x16_bf16 v[32:47], v[148:151], a[208:211], v[32:47]
s_add_u32 m0, s70, 114688
s_add_u32 s79, s70, 8192
s_waitcnt lgkmcnt(6)
v_mfma_f32_32x32x16_bf16 v[0:15], v[128:131], a[216:219], v[0:15]
ds_read_b128 a[232:235], v98
ds_read_b128 a[236:239], v99
ds_read_b64_tr_b16 v[144:145], v179 offset:36864
ds_read_b64_tr_b16 v[146:147], v179 offset:37376
buffer_load_dwordx4 v190, s[28:31], s79 offen lds
v_mfma_f32_32x32x16_bf16 v[16:31], v[128:131], a[220:223], v[16:31]
ds_read_b64_tr_b16 v[148:149], v179 offset:37888
ds_read_b64_tr_b16 v[150:151], v179 offset:38400
buffer_load_dwordx4 v190, s[28:31], s79 offen offset:1024 lds
v_mfma_f32_32x32x16_bf16 v[48:63], v[132:135], a[220:223], v[48:63]
s_add_u32 m0, s70, 122880
s_add_u32 s79, s70, 16384
v_mfma_f32_32x32x16_bf16 v[32:47], v[132:135], a[216:219], v[32:47]
buffer_load_dwordx4 v190, s[28:31], s79 offen lds
s_waitcnt lgkmcnt(6)
v_mfma_f32_32x32x16_bf16 v[0:15], v[136:139], a[224:227], v[0:15]
ds_read_b128 a[240:243], v100
ds_read_b128 a[244:247], v101
ds_read_b64_tr_b16 v[128:129], v179 offset:40960
ds_read_b64_tr_b16 v[130:131], v179 offset:41472
buffer_load_dwordx4 v190, s[28:31], s79 offen offset:1024 lds
v_mfma_f32_32x32x16_bf16 v[16:31], v[136:139], a[228:231], v[16:31]
ds_read_b64_tr_b16 v[132:133], v179 offset:41984
ds_read_b64_tr_b16 v[134:135], v179 offset:42496
s_add_u32 s64, s75, 589952
s_add_u32 m0, s72, 0
s_add_u32 s65, s64, 73728
v_mfma_f32_32x32x16_bf16 v[48:63], v[140:143], a[228:231], v[48:63]
buffer_load_dwordx4 v181, s[32:35], s64 offen lds
v_mfma_f32_32x32x16_bf16 v[32:47], v[140:143], a[224:227], v[32:47]
buffer_load_dwordx4 v181, s[36:39], s64 offen offset:1024 lds
s_add_u32 m0, s72, 2048
s_waitcnt lgkmcnt(6)
v_mfma_f32_32x32x16_bf16 v[0:15], v[144:147], a[232:235], v[0:15]
ds_read_b128 a[248:251], v102
ds_read_b128 a[252:255], v103
ds_read_b64_tr_b16 v[136:137], v179 offset:45056
ds_read_b64_tr_b16 v[138:139], v179 offset:45568
buffer_load_dwordx4 v181, s[32:35], s65 offen lds
v_mfma_f32_32x32x16_bf16 v[16:31], v[144:147], a[236:239], v[16:31]
ds_read_b64_tr_b16 v[140:141], v179 offset:46080
ds_read_b64_tr_b16 v[142:143], v179 offset:46592
buffer_load_dwordx4 v181, s[36:39], s65 offen offset:1024 lds
v_mfma_f32_32x32x16_bf16 v[48:63], v[148:151], a[236:239], v[48:63]
v_mfma_f32_32x32x16_bf16 v[32:47], v[148:151], a[232:235], v[32:47]
s_waitcnt lgkmcnt(6)
v_mfma_f32_32x32x16_bf16 v[0:15], v[128:131], a[240:243], v[0:15]
s_waitcnt vmcnt(15)
s_barrier
ds_read_b128 v[224:227], v96 offset:16384
ds_read_b128 v[228:231], v97 offset:16384
ds_read_b64_tr_b16 v[144:145], v179 offset:49152
ds_read_b64_tr_b16 v[146:147], v179 offset:49664
s_add_u32 m0, s70, 131072
s_add_u32 s79, s70, 24576
v_mfma_f32_32x32x16_bf16 v[16:31], v[128:131], a[244:247], v[16:31]
ds_read_b64_tr_b16 v[148:149], v179 offset:50176
ds_read_b64_tr_b16 v[150:151], v179 offset:50688
buffer_load_dwordx4 v190, s[28:31], s79 offen lds
v_mfma_f32_32x32x16_bf16 v[48:63], v[132:135], a[244:247], v[48:63]
buffer_load_dwordx4 v190, s[28:31], s79 offen offset:1024 lds
v_mfma_f32_32x32x16_bf16 v[32:47], v[132:135], a[240:243], v[32:47]
s_add_u32 s64, s75, 1179776
s_add_u32 m0, s72, 16384
s_add_u32 s65, s64, 73728
s_waitcnt lgkmcnt(6)
v_mfma_f32_32x32x16_bf16 v[0:15], v[136:139], a[248:251], v[0:15]
ds_read_b128 v[232:235], v98 offset:16384
ds_read_b128 v[236:239], v99 offset:16384
ds_read_b64_tr_b16 v[128:129], v179 offset:53248
ds_read_b64_tr_b16 v[130:131], v179 offset:53760
buffer_load_dwordx4 v181, s[32:35], s64 offen lds
v_mfma_f32_32x32x16_bf16 v[16:31], v[136:139], a[252:255], v[16:31]
ds_read_b64_tr_b16 v[132:133], v179 offset:54272
ds_read_b64_tr_b16 v[134:135], v179 offset:54784
buffer_load_dwordx4 v181, s[36:39], s64 offen offset:1024 lds
s_add_u32 m0, s72, 18432
v_mfma_f32_32x32x16_bf16 v[48:63], v[140:143], a[252:255], v[48:63]
buffer_load_dwordx4 v181, s[32:35], s65 offen lds
v_mfma_f32_32x32x16_bf16 v[32:47], v[140:143], a[248:251], v[32:47]
buffer_load_dwordx4 v181, s[36:39], s65 offen offset:1024 lds
s_waitcnt lgkmcnt(6)
v_mfma_f32_32x32x16_bf16 v[0:15], v[144:147], v[224:227], v[0:15]
ds_read_b128 v[240:243], v100 offset:16384
ds_read_b128 v[244:247], v101 offset:16384
ds_read_b64_tr_b16 v[136:137], v179 offset:57344
ds_read_b64_tr_b16 v[138:139], v179 offset:57856
v_mfma_f32_32x32x16_bf16 v[16:31], v[144:147], v[228:231], v[16:31]
ds_read_b64_tr_b16 v[140:141], v179 offset:58368
ds_read_b64_tr_b16 v[142:143], v179 offset:58880
v_mfma_f32_32x32x16_bf16 v[48:63], v[148:151], v[228:231], v[48:63]
v_mfma_f32_32x32x16_bf16 v[32:47], v[148:151], v[224:227], v[32:47]
s_waitcnt lgkmcnt(6)
v_mfma_f32_32x32x16_bf16 v[0:15], v[128:131], v[232:235], v[0:15]
ds_read_b128 v[248:251], v102 offset:16384
ds_read_b128 v[252:255], v103 offset:16384
ds_read_b64_tr_b16 v[144:145], v179 offset:61440
ds_read_b64_tr_b16 v[146:147], v179 offset:61952
v_mfma_f32_32x32x16_bf16 v[16:31], v[128:131], v[236:239], v[16:31]
ds_read_b64_tr_b16 v[148:149], v179 offset:62464
ds_read_b64_tr_b16 v[150:151], v179 offset:62976
v_mfma_f32_32x32x16_bf16 v[48:63], v[132:135], v[236:239], v[48:63]
v_mfma_f32_32x32x16_bf16 v[32:47], v[132:135], v[232:235], v[32:47]
s_waitcnt lgkmcnt(6)
v_mfma_f32_32x32x16_bf16 v[0:15], v[136:139], v[240:243], v[0:15]
s_waitcnt vmcnt(14)
s_barrier
v_lshrrev_b32 v168, 5, v178
v_lshlrev_b32 v168, 10, v168
v_sub_u32 v168, v179, v168
v_add_u32 v168, 114688, v168
v_lshrrev_b32 v169, 5, v178
v_lshlrev_b32 v169, 4, v169
v_add_u32 v169, 147456, v169
ds_read_b64_tr_b16 v[128:129], v180 offset:32768
ds_read_b64_tr_b16 v[130:131], v180 offset:33280
ds_read_b64_tr_b16 v[132:133], v180 offset:33792
ds_read_b64_tr_b16 v[134:135], v180 offset:34304
s_add_u32 s64, s75, 1769600
s_add_u32 m0, s72, 32768
s_add_u32 s65, s64, 73728
v_mfma_f32_32x32x16_bf16 v[16:31], v[136:139], v[244:247], v[16:31]
ds_read_b64_tr_b16 v[136:137], v168 offset:35328
ds_read_b64_tr_b16 v[138:139], v168 offset:35840
s_mov_b64 exec, s[80:81]
ds_read_b128 v[136:139], v169
s_mov_b64 exec, -1
buffer_load_dwordx4 v181, s[32:35], s64 offen lds
v_mfma_f32_32x32x16_bf16 v[48:63], v[140:143], v[244:247], v[48:63]
buffer_load_dwordx4 v181, s[36:39], s64 offen offset:1024 lds
s_add_u32 m0, s72, 34816
v_mfma_f32_32x32x16_bf16 v[32:47], v[140:143], v[240:243], v[32:47]
buffer_load_dwordx4 v181, s[32:35], s65 offen lds
s_waitcnt lgkmcnt(6)
v_mfma_f32_32x32x16_bf16 v[0:15], v[144:147], v[248:251], v[0:15]
ds_read_b64_tr_b16 v[140:141], v180 offset:36864
ds_read_b64_tr_b16 v[142:143], v180 offset:37376
buffer_load_dwordx4 v181, s[36:39], s65 offen offset:1024 lds
v_mfma_f32_32x32x16_bf16 v[16:31], v[144:147], v[252:255], v[16:31]
ds_read_b64_tr_b16 v[144:145], v180 offset:37888
ds_read_b64_tr_b16 v[146:147], v180 offset:38400
s_add_u32 s64, s75, 2359424
s_add_u32 m0, s72, 81920
s_add_u32 s65, s64, 73728
v_mfma_f32_32x32x16_bf16 v[48:63], v[148:151], v[252:255], v[48:63]
buffer_load_dwordx4 v181, s[32:35], s64 offen lds
v_mfma_f32_32x32x16_bf16 v[32:47], v[148:151], v[248:251], v[32:47]
ds_read_b64_tr_b16 v[148:149], v168 offset:37376
ds_read_b64_tr_b16 v[150:151], v168 offset:37888
s_mov_b64 exec, s[80:81]
ds_read_b128 v[148:151], v169 offset:32
s_mov_b64 exec, -1
buffer_load_dwordx4 v181, s[36:39], s64 offen offset:1024 lds
s_add_u32 m0, s72, 83968
; ==== p1 (1 iteration(s))
s_waitcnt lgkmcnt(6)
v_mfma_f32_32x32x16_bf16 v[64:79], v[128:131], a[0:3], 0
buffer_load_dwordx4 v181, s[32:35], s65 offen lds
v_readfirstlane_b32 s62, v188
v_mfma_f32_32x32x16_bf16 v[80:95], v[128:131], a[4:7], 0
ds_read_b64_tr_b16 v[128:129], v180 offset:40960
ds_read_b64_tr_b16 v[130:131], v180 offset:41472
v_readfirstlane_b32 s63, v189
v_mfma_f32_32x32x16_bf16 v[112:127], v[132:135], a[4:7], 0
buffer_load_dwordx4 v181, s[36:39], s65 offen offset:1024 lds
s_add_u32 s62, s62, 1
s_nop 0
s_addc_u32 s63, s63, 0
v_mfma_f32_32x32x16_bf16 v[96:111], v[132:135], a[0:3], 0
ds_read_b64_tr_b16 v[132:133], v180 offset:41984
ds_read_b64_tr_b16 v[134:135], v180 offset:42496
v_mul_f32 v152, s76, v0
v_mul_f32 v153, s76, v1
v_mfma_f32_32x32x16_bf16 v[192:207], v[136:139], a[0:3], 0
v_exp_f32 v152, v152
v_mfma_f32_32x32x16_bf16 v[208:223], v[136:139], a[4:7], 0
ds_read_b64_tr_b16 v[136:137], v168 offset:39424
ds_read_b64_tr_b16 v[138:139], v168 offset:39936
s_mov_b64 exec, s[80:81]
ds_read_b128 v[136:139], v169 offset:64
s_mov_b64 exec, -1
s_waitcnt lgkmcnt(6)
v_mfma_f32_32x32x16_bf16 v[64:79], v[140:143], a[8:11], v[64:79]
v_exp_f32 v153, v153
v_mul_f32 v154, v0, v32
v_mfma_f32_32x32x16_bf16 v[80:95], v[140:143], a[12:15], v[80:95]
ds_read_b64_tr_b16 v[140:141], v180 offset:45056
ds_read_b64_tr_b16 v[142:143], v180 offset:45568
v_mul_f32 v155, v1, v33
v_mfma_f32_32x32x16_bf16 v[112:127], v[144:147], a[12:15], v[112:127]
v_add_f32 v152, 1.0, v152
v_add_f32 v153, 1.0, v153
v_mfma_f32_32x32x16_bf16 v[96:111], v[144:147], a[8:11], v[96:111]
ds_read_b64_tr_b16 v[144:145], v180 offset:46080
ds_read_b64_tr_b16 v[146:147], v180 offset:46592
v_rcp_f32 v152, v152
v_mfma_f32_32x32x16_bf16 v[192:207], v[148:151], a[8:11], v[192:207]
v_rcp_f32 v153, v153
v_mfma_f32_32x32x16_bf16 v[208:223], v[148:151], a[12:15], v[208:223]
ds_read_b64_tr_b16 v[148:149], v168 offset:41472
ds_read_b64_tr_b16 v[150:151], v168 offset:41984
s_mov_b64 exec, s[80:81]
ds_read_b128 v[148:151], v169 offset:96
s_mov_b64 exec, -1
s_waitcnt lgkmcnt(6)
v_mfma_f32_32x32x16_bf16 v[64:79], v[128:131], a[16:19], v[64:79]
v_mul_f32 v154, v154, v152
v_mul_f32 v155, v155, v153
v_cvt_pk_bf16_f32 v170, v154, v155
v_mfma_f32_32x32x16_bf16 v[80:95], v[128:131], a[20:23], v[80:95]
s_waitcnt vmcnt(14)
s_barrier
ds_read_b64_tr_b16 v[128:129], v179
ds_read_b64_tr_b16 v[130:131], v179 offset:512
v_mfma_f32_32x32x16_bf16 v[112:127], v[132:135], a[20:23], v[112:127]
s_add_u32 s64, s75, 2949248
s_add_u32 m0, s72, 49152
s_add_u32 s65, s64, 73728
v_mul_f32 v156, s76, v2
v_mul_f32 v157, s76, v3
v_mfma_f32_32x32x16_bf16 v[96:111], v[132:135], a[16:19], v[96:111]
ds_read_b64_tr_b16 v[132:133], v179 offset:1024
ds_read_b64_tr_b16 v[134:135], v179 offset:1536
v_exp_f32 v156, v156
v_mfma_f32_32x32x16_bf16 v[192:207], v[136:139], a[16:19], v[192:207]
buffer_load_dwordx4 v181, s[32:35], s64 offen lds
v_mfma_f32_32x32x16_bf16 v[208:223], v[136:139], a[20:23], v[208:223]
ds_read_b64_tr_b16 v[136:137], v168
ds_read_b64_tr_b16 v[138:139], v168 offset:512
s_mov_b64 exec, s[80:81]
ds_read_b128 v[136:139], v169 offset:128
s_mov_b64 exec, -1
s_waitcnt lgkmcnt(6)
v_mfma_f32_32x32x16_bf16 v[64:79], v[140:143], a[24:27], v[64:79]
buffer_load_dwordx4 v181, s[36:39], s64 offen offset:1024 lds
s_add_u32 m0, s72, 51200
v_exp_f32 v157, v157
v_mfma_f32_32x32x16_bf16 v[80:95], v[140:143], a[28:31], v[80:95]
ds_read_b64_tr_b16 v[140:141], v179 offset:4096
ds_read_b64_tr_b16 v[142:143], v179 offset:4608
v_mul_f32 v158, v2, v34
v_mfma_f32_32x32x16_bf16 v[112:127], v[144:147], a[28:31], v[112:127]
buffer_load_dwordx4 v181, s[32:35], s65 offen lds
v_mul_f32 v159, v3, v35
v_mfma_f32_32x32x16_bf16 v[96:111], v[144:147], a[24:27], v[96:111]
ds_read_b64_tr_b16 v[144:145], v179 offset:5120
ds_read_b64_tr_b16 v[146:147], v179 offset:5632
v_add_f32 v156, 1.0, v156
v_add_f32 v157, 1.0, v157
v_mfma_f32_32x32x16_bf16 v[192:207], v[148:151], a[24:27], v[192:207]
buffer_load_dwordx4 v181, s[36:39], s65 offen offset:1024 lds
v_rcp_f32 v156, v156
v_mfma_f32_32x32x16_bf16 v[208:223], v[148:151], a[28:31], v[208:223]
ds_read_b64_tr_b16 v[148:149], v168 offset:2048
ds_read_b64_tr_b16 v[150:151], v168 offset:2560
s_mov_b64 exec, s[80:81]
ds_read_b128 v[148:151], v169 offset:160
s_mov_b64 exec, -1
s_waitcnt lgkmcnt(6)
v_mfma_f32_32x32x16_bf16 v[64:79], v[128:131], a[32:35], v[64:79]
v_rcp_f32 v157, v157
v_mfma_f32_32x32x16_bf16 v[80:95], v[128:131], a[36:39], v[80:95]
ds_read_b64_tr_b16 v[128:129], v179 offset:8192
ds_read_b64_tr_b16 v[130:131], v179 offset:8704
v_mul_f32 v158, v158, v156
v_mfma_f32_32x32x16_bf16 v[112:127], v[132:135], a[36:39], v[112:127]
v_mul_f32 v159, v159, v157
v_cvt_pk_bf16_f32 v171, v158, v159
v_mul_f32 v160, s76, v4
v_mfma_f32_32x32x16_bf16 v[96:111], v[132:135], a[32:35], v[96:111]
ds_read_b64_tr_b16 v[132:133], v179 offset:9216
ds_read_b64_tr_b16 v[134:135], v179 offset:9728
v_mul_f32 v161, s76, v5
v_mfma_f32_32x32x16_bf16 v[192:207], v[136:139], a[32:35], v[192:207]
v_exp_f32 v160, v160
v_mfma_f32_32x32x16_bf16 v[208:223], v[136:139], a[36:39], v[208:223]
ds_read_b64_tr_b16 v[136:137], v168 offset:4096
ds_read_b64_tr_b16 v[138:139], v168 offset:4608
s_mov_b64 exec, s[80:81]
ds_read_b128 v[136:139], v169 offset:192
s_mov_b64 exec, -1
s_waitcnt lgkmcnt(6)
v_mfma_f32_32x32x16_bf16 v[64:79], v[140:143], a[40:43], v[64:79]
v_exp_f32 v161, v161
v_mul_f32 v162, v4, v36
v_mfma_f32_32x32x16_bf16 v[80:95], v[140:143], a[44:47], v[80:95]
ds_read_b64_tr_b16 v[140:141], v179 offset:12288
ds_read_b64_tr_b16 v[142:143], v179 offset:12800
v_mul_f32 v163, v5, v37
v_mfma_f32_32x32x16_bf16 v[112:127], v[144:147], a[44:47], v[112:127]
v_add_f32 v160, 1.0, v160
v_add_f32 v161, 1.0, v161
v_mfma_f32_32x32x16_bf16 v[96:111], v[144:147], a[40:43], v[96:111]
ds_read_b64_tr_b16 v[144:145], v179 offset:13312
ds_read_b64_tr_b16 v[146:147], v179 offset:13824
v_rcp_f32 v160, v160
v_mfma_f32_32x32x16_bf16 v[192:207], v[148:151], a[40:43], v[192:207]
v_rcp_f32 v161, v161
v_mul_f32 v162, v162, v160
v_mfma_f32_32x32x16_bf16 v[208:223], v[148:151], a[44:47], v[208:223]
ds_read_b64_tr_b16 v[148:149], v168 offset:6144
ds_read_b64_tr_b16 v[150:151], v168 offset:6656
s_mov_b64 exec, s[80:81]
ds_read_b128 v[148:151], v169 offset:224
s_mov_b64 exec, -1
s_waitcnt lgkmcnt(6)
v_mfma_f32_32x32x16_bf16 v[64:79], v[128:131], a[48:51], v[64:79]
v_mul_f32 v163, v163, v161
v_cvt_pk_bf16_f32 v172, v162, v163
v_mfma_f32_32x32x16_bf16 v[80:95], v[128:131], a[52:55], v[80:95]
s_waitcnt vmcnt(12)
s_barrier
ds_read_b64_tr_b16 v[128:129], v179 offset:16384
ds_read_b64_tr_b16 v[130:131], v179 offset:16896
s_add_u32 m0, s70, 150016
s_add_u32 s79, s70, 32768
v_mfma_f32_32x32x16_bf16 v[112:127], v[132:135], a[52:55], v[112:127]
buffer_load_dwordx4 v190, s[28:31], s79 offen lds
v_mul_f32 v164, s76, v6
v_mfma_f32_32x32x16_bf16 v[96:111], v[132:135], a[48:51], v[96:111]
ds_read_b64_tr_b16 v[132:133], v179 offset:17408
ds_read_b64_tr_b16 v[134:135], v179 offset:17920
v_mul_f32 v165, s76, v7
v_mfma_f32_32x32x16_bf16 v[192:207], v[136:139], a[48:51], v[192:207]
buffer_load_dwordx4 v190, s[28:31], s79 offen offset:1024 lds
v_exp_f32 v164, v164
v_mfma_f32_32x32x16_bf16 v[208:223], v[136:139], a[52:55], v[208:223]
ds_read_b64_tr_b16 v[136:137], v168 offset:8192
ds_read_b64_tr_b16 v[138:139], v168 offset:8704
s_mov_b64 exec, s[80:81]
ds_read_b128 v[136:139], v169 offset:256
s_mov_b64 exec, -1
s_waitcnt lgkmcnt(6)
v_mfma_f32_32x32x16_bf16 v[64:79], v[140:143], a[56:59], v[64:79]
s_add_u32 s64, s75, 3539072
s_add_u32 m0, s72, 65536
s_add_u32 s65, s64, 73728
v_exp_f32 v165, v165
v_mfma_f32_32x32x16_bf16 v[80:95], v[140:143], a[60:63], v[80:95]
ds_read_b64_tr_b16 v[140:141], v179 offset:20480
ds_read_b64_tr_b16 v[142:143], v179 offset:20992
v_mul_f32 v166, v6, v38
v_mfma_f32_32x32x16_bf16 v[112:127], v[144:147], a[60:63], v[112:127]
buffer_load_dwordx4 v181, s[32:35], s64 offen lds
v_mul_f32 v167, v7, v39
v_mfma_f32_32x32x16_bf16 v[96:111], v[144:147], a[56:59], v[96:111]
ds_read_b64_tr_b16 v[144:145], v179 offset:21504
ds_read_b64_tr_b16 v[146:147], v179 offset:22016
v_add_f32 v164, 1.0, v164
v_add_f32 v165, 1.0, v165
v_mfma_f32_32x32x16_bf16 v[192:207], v[148:151], a[56:59], v[192:207]
buffer_load_dwordx4 v181, s[36:39], s64 offen offset:1024 lds
s_add_u32 m0, s72, 67584
v_mfma_f32_32x32x16_bf16 v[208:223], v[148:151], a[60:63], v[208:223]
ds_read_b64_tr_b16 v[148:149], v168 offset:10240
ds_read_b64_tr_b16 v[150:151], v168 offset:10752
s_mov_b64 exec, s[80:81]
ds_read_b128 v[148:151], v169 offset:288
s_mov_b64 exec, -1
s_waitcnt lgkmcnt(6)
v_mfma_f32_32x32x16_bf16 v[64:79], v[128:131], a[64:67], v[64:79]
buffer_load_dwordx4 v181, s[32:35], s65 offen lds
v_rcp_f32 v164, v164
v_mfma_f32_32x32x16_bf16 v[80:95], v[128:131], a[68:71], v[80:95]
ds_read_b64_tr_b16 v[128:129], v179 offset:24576
ds_read_b64_tr_b16 v[130:131], v179 offset:25088
v_rcp_f32 v165, v165
v_mfma_f32_32x32x16_bf16 v[112:127], v[132:135], a[68:71], v[112:127]
buffer_load_dwordx4 v181, s[36:39], s65 offen offset:1024 lds
v_mul_f32 v166, v166, v164
v_mfma_f32_32x32x16_bf16 v[96:111], v[132:135], a[64:67], v[96:111]
ds_read_b64_tr_b16 v[132:133], v179 offset:25600
ds_read_b64_tr_b16 v[134:135], v179 offset:26112
v_mul_f32 v167, v167, v165
v_mfma_f32_32x32x16_bf16 v[192:207], v[136:139], a[64:67], v[192:207]
v_cvt_pk_bf16_f32 v173, v166, v167
s_mov_b32 s85, 0
v_mfma_f32_32x32x16_bf16 v[208:223], v[136:139], a[68:71], v[208:223]
ds_read_b64_tr_b16 v[136:137], v168 offset:12288
ds_read_b64_tr_b16 v[138:139], v168 offset:12800
s_mov_b64 exec, s[80:81]
ds_read_b128 v[136:139], v169 offset:320
s_mov_b64 exec, -1
s_waitcnt lgkmcnt(6)
v_mfma_f32_32x32x16_bf16 v[64:79], v[140:143], a[72:75], v[64:79]
buffer_store_dwordx4 v[170:173], v182, s[40:43], s85 offen sc1
v_mul_f32 v152, s76, v8
v_mfma_f32_32x32x16_bf16 v[80:95], v[140:143], a[76:79], v[80:95]
ds_read_b64_tr_b16 v[140:141], v179 offset:28672
ds_read_b64_tr_b16 v[142:143], v179 offset:29184
v_mul_f32 v153, s76, v9
v_mfma_f32_32x32x16_bf16 v[112:127], v[144:147], a[76:79], v[112:127]
v_exp_f32 v152, v152
v_mfma_f32_32x32x16_bf16 v[96:111], v[144:147], a[72:75], v[96:111]
ds_read_b64_tr_b16 v[144:145], v179 offset:29696
ds_read_b64_tr_b16 v[146:147], v179 offset:30208
v_exp_f32 v153, v153
v_mfma_f32_32x32x16_bf16 v[192:207], v[148:151], a[72:75], v[192:207]
v_mul_f32 v154, v8, v40
v_mul_f32 v155, v9, v41
v_add_f32 v152, 1.0, v152
v_mfma_f32_32x32x16_bf16 v[208:223], v[148:151], a[76:79], v[208:223]
ds_read_b64_tr_b16 v[148:149], v168 offset:14336
ds_read_b64_tr_b16 v[150:151], v168 offset:14848
s_mov_b64 exec, s[80:81]
ds_read_b128 v[148:151], v169 offset:352
s_mov_b64 exec, -1
s_waitcnt lgkmcnt(6)
v_mfma_f32_32x32x16_bf16 v[64:79], v[128:131], a[80:83], v[64:79]
v_add_f32 v153, 1.0, v153
v_mfma_f32_32x32x16_bf16 v[80:95], v[128:131], a[84:87], v[80:95]
s_waitcnt vmcnt(15)
s_barrier
ds_read_b64_tr_b16 v[128:129], v179 offset:32768
ds_read_b64_tr_b16 v[130:131], v179 offset:33280
s_add_u32 m0, s70, 114688
s_add_u32 s79, s70, 40960
v_mfma_f32_32x32x16_bf16 v[112:127], v[132:135], a[84:87], v[112:127]
buffer_load_dwordx4 v190, s[28:31], s79 offen lds
v_rcp_f32 v152, v152
v_mfma_f32_32x32x16_bf16 v[96:111], v[132:135], a[80:83], v[96:111]
ds_read_b64_tr_b16 v[132:133], v179 offset:33792
ds_read_b64_tr_b16 v[134:135], v179 offset:34304
v_rcp_f32 v153, v153
v_mfma_f32_32x32x16_bf16 v[192:207], v[136:139], a[80:83], v[192:207]
buffer_load_dwordx4 v190, s[28:31], s79 offen offset:1024 lds
v_mul_f32 v154, v154, v152
v_mfma_f32_32x32x16_bf16 v[208:223], v[136:139], a[84:87], v[208:223]
ds_read_b64_tr_b16 v[136:137], v168 offset:16384
ds_read_b64_tr_b16 v[138:139], v168 offset:16896
s_mov_b64 exec, s[80:81]
ds_read_b128 v[136:139], v169 offset:384
s_mov_b64 exec, -1
s_waitcnt lgkmcnt(6)
v_mfma_f32_32x32x16_bf16 v[64:79], v[140:143], a[88:91], v[64:79]
s_add_u32 s64, s75, 4128896
s_add_u32 m0, s72, 0
s_add_u32 s65, s64, 73728
v_mul_f32 v155, v155, v153
v_mfma_f32_32x32x16_bf16 v[80:95], v[140:143], a[92:95], v[80:95]
ds_read_b64_tr_b16 v[140:141], v179 offset:36864
ds_read_b64_tr_b16 v[142:143], v179 offset:37376
v_cvt_pk_bf16_f32 v174, v154, v155
v_mul_f32 v156, s76, v10
v_mfma_f32_32x32x16_bf16 v[112:127], v[144:147], a[92:95], v[112:127]
buffer_load_dwordx4 v181, s[32:35], s64 offen lds
v_mul_f32 v157, s76, v11
v_mfma_f32_32x32x16_bf16 v[96:111], v[144:147], a[88:91], v[96:111]
ds_read_b64_tr_b16 v[144:145], v179 offset:37888
ds_read_b64_tr_b16 v[146:147], v179 offset:38400
v_exp_f32 v156, v156
v_mfma_f32_32x32x16_bf16 v[192:207], v[148:151], a[88:91], v[192:207]
buffer_load_dwordx4 v181, s[36:39], s64 offen offset:1024 lds
s_add_u32 m0, s72, 2048
v_mfma_f32_32x32x16_bf16 v[208:223], v[148:151], a[92:95], v[208:223]
ds_read_b64_tr_b16 v[148:149], v168 offset:18432
ds_read_b64_tr_b16 v[150:151], v168 offset:18944
s_mov_b64 exec, s[80:81]
ds_read_b128 v[148:151], v169 offset:416
s_mov_b64 exec, -1
s_waitcnt lgkmcnt(6)
v_mfma_f32_32x32x16_bf16 v[64:79], v[128:131], a[96:99], v[64:79]
buffer_load_dwordx4 v181, s[32:35], s65 offen lds
v_exp_f32 v157, v157
v_mfma_f32_32x32x16_bf16 v[80:95], v[128:131], a[100:103], v[80:95]
ds_read_b64_tr_b16 v[128:129], v179 offset:40960
ds_read_b64_tr_b16 v[130:131], v179 offset:41472
v_mul_f32 v158, v10, v42
v_mul_f32 v159, v11, v43
v_mfma_f32_32x32x16_bf16 v[112:127], v[132:135], a[100:103], v[112:127]
buffer_load_dwordx4 v181, s[36:39], s65 offen offset:1024 lds
v_add_f32 v156, 1.0, v156
v_mfma_f32_32x32x16_bf16 v[96:111], v[132:135], a[96:99], v[96:111]
ds_read_b64_tr_b16 v[132:133], v179 offset:41984
ds_read_b64_tr_b16 v[134:135], v179 offset:42496
v_add_f32 v157, 1.0, v157
v_mfma_f32_32x32x16_bf16 v[192:207], v[136:139], a[96:99], v[192:207]
v_rcp_f32 v156, v156
v_mfma_f32_32x32x16_bf16 v[208:223], v[136:139], a[100:103], v[208:223]
ds_read_b64_tr_b16 v[136:137], v168 offset:20480
ds_read_b64_tr_b16 v[138:139], v168 offset:20992
s_mov_b64 exec, s[80:81]
ds_read_b128 v[136:139], v169 offset:448
s_mov_b64 exec, -1
s_waitcnt lgkmcnt(6)
v_mfma_f32_32x32x16_bf16 v[64:79], v[140:143], a[104:107], v[64:79]
v_rcp_f32 v157, v157
v_mul_f32 v158, v158, v156
v_mfma_f32_32x32x16_bf16 v[80:95], v[140:143], a[108:111], v[80:95]
ds_read_b64_tr_b16 v[140:141], v179 offset:45056
ds_read_b64_tr_b16 v[142:143], v179 offset:45568
v_mul_f32 v159, v159, v157
v_cvt_pk_bf16_f32 v175, v158, v159
v_mfma_f32_32x32x16_bf16 v[112:127], v[144:147], a[108:111], v[112:127]
v_mul_f32 v160, s76, v12
v_mul_f32 v161, s76, v13
v_mfma_f32_32x32x16_bf16 v[96:111], v[144:147], a[104:107], v[96:111]
ds_read_b64_tr_b16 v[144:145], v179 offset:46080
ds_read_b64_tr_b16 v[146:147], v179 offset:46592
v_exp_f32 v160, v160
v_mfma_f32_32x32x16_bf16 v[192:207], v[148:151], a[104:107], v[192:207]
v_exp_f32 v161, v161
v_mfma_f32_32x32x16_bf16 v[208:223], v[148:151], a[108:111], v[208:223]
ds_read_b64_tr_b16 v[148:149], v168 offset:22528
ds_read_b64_tr_b16 v[150:151], v168 offset:23040
s_mov_b64 exec, s[80:81]
ds_read_b128 v[148:151], v169 offset:480
s_mov_b64 exec, -1
s_waitcnt lgkmcnt(6)
v_mfma_f32_32x32x16_bf16 v[64:79], v[128:131], a[112:115], v[64:79]
v_mul_f32 v162, v12, v44
s_add_u32 s94, s95, 256
v_mfma_f32_32x32x16_bf16 v[80:95], v[128:131], a[116:119], v[80:95]
s_waitcnt vmcnt(11)
s_barrier
ds_read_b64_tr_b16 v[128:129], v180 offset:49152
ds_read_b64_tr_b16 v[130:131], v180 offset:49664
s_add_u32 m0, s70, 122880
s_add_u32 s79, s70, 49152
v_mfma_f32_32x32x16_bf16 v[112:127], v[132:135], a[116:119], v[112:127]
buffer_load_dwordx4 v190, s[28:31], s79 offen lds
buffer_load_dword v191, v187, s[32:35], s94 offen
v_mfma_f32_32x32x16_bf16 v[96:111], v[132:135], a[112:115], v[96:111]
ds_read_b64_tr_b16 v[132:133], v180 offset:50176
ds_read_b64_tr_b16 v[134:135], v180 offset:50688
s_add_u32 s94, s94, 1024
v_mfma_f32_32x32x16_bf16 v[192:207], v[136:139], a[112:115], v[192:207]
buffer_load_dwordx4 v190, s[28:31], s79 offen offset:1024 lds
v_mfma_f32_32x32x16_bf16 v[208:223], v[136:139], a[116:119], v[208:223]
ds_read_b64_tr_b16 v[136:137], v168 offset:35328
ds_read_b64_tr_b16 v[138:139], v168 offset:35840
s_mov_b64 exec, s[80:81]
ds_read_b128 v[136:139], v169 offset:512
s_mov_b64 exec, -1
s_waitcnt lgkmcnt(6)
v_mfma_f32_32x32x16_bf16 v[64:79], v[140:143], a[120:123], v[64:79]
s_add_u32 s64, s75, 4718720
s_add_u32 m0, s72, 16384
s_add_u32 s65, s64, 73728
buffer_load_dword v191, v187, s[36:39], s94 offen
v_mfma_f32_32x32x16_bf16 v[80:95], v[140:143], a[124:127], v[80:95]
ds_read_b64_tr_b16 v[140:141], v180 offset:53248
ds_read_b64_tr_b16 v[142:143], v180 offset:53760
v_mul_f32 v163, v13, v45
v_mfma_f32_32x32x16_bf16 v[112:127], v[144:147], a[124:127], v[112:127]
buffer_load_dwordx4 v181, s[32:35], s64 offen lds
v_add_f32 v160, 1.0, v160
v_add_f32 v161, 1.0, v161
v_mfma_f32_32x32x16_bf16 v[96:111], v[144:147], a[120:123], v[96:111]
ds_read_b64_tr_b16 v[144:145], v180 offset:54272
ds_read_b64_tr_b16 v[146:147], v180 offset:54784
v_rcp_f32 v160, v160
v_mfma_f32_32x32x16_bf16 v[192:207], v[148:151], a[120:123], v[192:207]
buffer_load_dwordx4 v181, s[36:39], s64 offen offset:1024 lds
s_add_u32 m0, s72, 18432
v_mfma_f32_32x32x16_bf16 v[208:223], v[148:151], a[124:127], v[208:223]
ds_read_b64_tr_b16 v[148:149], v168 offset:37376
ds_read_b64_tr_b16 v[150:151], v168 offset:37888
s_mov_b64 exec, s[80:81]
ds_read_b128 v[148:151], v169 offset:544
s_mov_b64 exec, -1
s_waitcnt lgkmcnt(6)
v_mfma_f32_32x32x16_bf16 v[64:79], v[128:131], a[128:131], v[64:79]
buffer_load_dwordx4 v181, s[32:35], s65 offen lds
v_rcp_f32 v161, v161
v_mfma_f32_32x32x16_bf16 v[80:95], v[128:131], a[132:135], v[80:95]
ds_read_b64_tr_b16 v[128:129], v180 offset:57344
ds_read_b64_tr_b16 v[130:131], v180 offset:57856
v_mul_f32 v162, v162, v160
v_mfma_f32_32x32x16_bf16 v[112:127], v[132:135], a[132:135], v[112:127]
buffer_load_dwordx4 v181, s[36:39], s65 offen offset:1024 lds
v_mul_f32 v163, v163, v161
v_cvt_pk_bf16_f32 v176, v162, v163
v_mfma_f32_32x32x16_bf16 v[96:111], v[132:135], a[128:131], v[96:111]
ds_read_b64_tr_b16 v[132:133], v180 offset:58368
ds_read_b64_tr_b16 v[134:135], v180 offset:58880
v_mul_f32 v164, s76, v14
v_mfma_f32_32x32x16_bf16 v[192:207], v[136:139], a[128:131], v[192:207]
v_mul_f32 v165, s76, v15
v_exp_f32 v164, v164
v_mfma_f32_32x32x16_bf16 v[208:223], v[136:139], a[132:135], v[208:223]
ds_read_b64_tr_b16 v[136:137], v168 offset:39424
ds_read_b64_tr_b16 v[138:139], v168 offset:39936
s_mov_b64 exec, s[80:81]
ds_read_b128 v[136:139], v169 offset:576
s_mov_b64 exec, -1
s_waitcnt lgkmcnt(6)
v_mfma_f32_32x32x16_bf16 v[64:79], v[140:143], a[136:139], v[64:79]
v_exp_f32 v165, v165
v_mfma_f32_32x32x16_bf16 v[80:95], v[140:143], a[140:143], v[80:95]
ds_read_b64_tr_b16 v[140:141], v180 offset:61440
ds_read_b64_tr_b16 v[142:143], v180 offset:61952
v_mul_f32 v166, v14, v46
v_mfma_f32_32x32x16_bf16 v[112:127], v[144:147], a[140:143], v[112:127]
v_mul_f32 v167, v15, v47
v_add_f32 v164, 1.0, v164
v_add_f32 v165, 1.0, v165
v_mfma_f32_32x32x16_bf16 v[96:111], v[144:147], a[136:139], v[96:111]
ds_read_b64_tr_b16 v[144:145], v180 offset:62464
ds_read_b64_tr_b16 v[146:147], v180 offset:62976
v_rcp_f32 v164, v164
v_mfma_f32_32x32x16_bf16 v[192:207], v[148:151], a[136:139], v[192:207]
v_rcp_f32 v165, v165
v_mfma_f32_32x32x16_bf16 v[208:223], v[148:151], a[140:143], v[208:223]
ds_read_b64_tr_b16 v[148:149], v168 offset:41472
ds_read_b64_tr_b16 v[150:151], v168 offset:41984
s_mov_b64 exec, s[80:81]
ds_read_b128 v[148:151], v169 offset:608
s_mov_b64 exec, -1
s_waitcnt lgkmcnt(6)
v_mfma_f32_32x32x16_bf16 v[64:79], v[128:131], a[144:147], v[64:79]
v_mul_f32 v166, v166, v164
v_mul_f32 v167, v167, v165
v_mfma_f32_32x32x16_bf16 v[80:95], v[128:131], a[148:151], v[80:95]
s_waitcnt vmcnt(12)
s_barrier
ds_read_b64_tr_b16 v[128:129], v179 offset:49152
ds_read_b64_tr_b16 v[130:131], v179 offset:49664
s_add_u32 m0, s70, 131072
s_add_u32 s79, s70, 57344
v_mfma_f32_32x32x16_bf16 v[112:127], v[132:135], a[148:151], v[112:127]
buffer_load_dwordx4 v190, s[28:31], s79 offen lds
v_cvt_pk_bf16_f32 v177, v166, v167
v_mfma_f32_32x32x16_bf16 v[96:111], v[132:135], a[144:147], v[96:111]
ds_read_b64_tr_b16 v[132:133], v179 offset:50176
ds_read_b64_tr_b16 v[134:135], v179 offset:50688
s_mov_b32 s85, 0
v_mfma_f32_32x32x16_bf16 v[192:207], v[136:139], a[144:147], v[192:207]
buffer_load_dwordx4 v190, s[28:31], s79 offen offset:1024 lds
buffer_store_dwordx4 v[174:177], v182, s[40:43], s85 offen offset:1024 sc1
v_mfma_f32_32x32x16_bf16 v[208:223], v[136:139], a[148:151], v[208:223]
ds_read_b64_tr_b16 v[136:137], v168
ds_read_b64_tr_b16 v[138:139], v168 offset:512
s_mov_b64 exec, s[80:81]
ds_read_b128 v[136:139], v169 offset:640
s_mov_b64 exec, -1
s_waitcnt lgkmcnt(6)
v_mfma_f32_32x32x16_bf16 v[64:79], v[140:143], a[152:155], v[64:79]
s_add_u32 s64, s75, 256
s_add_u32 m0, s72, 32768
s_add_u32 s65, s64, 73728
v_mul_f32 v152, s76, v16
v_mfma_f32_32x32x16_bf16 v[80:95], v[140:143], a[156:159], v[80:95]
ds_read_b64_tr_b16 v[140:141], v179 offset:53248
ds_read_b64_tr_b16 v[142:143], v179 offset:53760
v_mul_f32 v153, s76, v17
v_mfma_f32_32x32x16_bf16 v[112:127], v[144:147], a[156:159], v[112:127]
buffer_load_dwordx4 v181, s[32:35], s64 offen lds
v_exp_f32 v152, v152
v_mfma_f32_32x32x16_bf16 v[96:111], v[144:147], a[152:155], v[96:111]
ds_read_b64_tr_b16 v[144:145], v179 offset:54272
ds_read_b64_tr_b16 v[146:147], v179 offset:54784
v_exp_f32 v153, v153
v_mfma_f32_32x32x16_bf16 v[192:207], v[148:151], a[152:155], v[192:207]
buffer_load_dwordx4 v181, s[36:39], s64 offen offset:1024 lds
s_add_u32 m0, s72, 34816
v_mfma_f32_32x32x16_bf16 v[208:223], v[148:151], a[156:159], v[208:223]
ds_read_b64_tr_b16 v[148:149], v168 offset:2048
ds_read_b64_tr_b16 v[150:151], v168 offset:2560
s_mov_b64 exec, s[80:81]
ds_read_b128 v[148:151], v169 offset:672
s_mov_b64 exec, -1
s_waitcnt lgkmcnt(6)
v_mfma_f32_32x32x16_bf16 v[64:79], v[128:131], a[160:163], v[64:79]
buffer_load_dwordx4 v181, s[32:35], s65 offen lds
v_mul_f32 v154, v16, v48
v_mul_f32 v155, v17, v49
v_mfma_f32_32x32x16_bf16 v[80:95], v[128:131], a[164:167], v[80:95]
ds_read_b64_tr_b16 v[128:129], v179 offset:57344
ds_read_b64_tr_b16 v[130:131], v179 offset:57856
v_add_f32 v152, 1.0, v152
v_mfma_f32_32x32x16_bf16 v[112:127], v[132:135], a[164:167], v[112:127]
buffer_load_dwordx4 v181, s[36:39], s65 offen offset:1024 lds
v_add_f32 v153, 1.0, v153
v_mfma_f32_32x32x16_bf16 v[96:111], v[132:135], a[160:163], v[96:111]
ds_read_b64_tr_b16 v[132:133], v179 offset:58368
ds_read_b64_tr_b16 v[134:135], v179 offset:58880
v_rcp_f32 v152, v152
v_mfma_f32_32x32x16_bf16 v[192:207], v[136:139], a[160:163], v[192:207]
v_rcp_f32 v153, v153
v_mfma_f32_32x32x16_bf16 v[208:223], v[136:139], a[164:167], v[208:223]
ds_read_b64_tr_b16 v[136:137], v168 offset:4096
ds_read_b64_tr_b16 v[138:139], v168 offset:4608
s_mov_b64 exec, s[80:81]
ds_read_b128 v[136:139], v169 offset:704
s_mov_b64 exec, -1
s_waitcnt lgkmcnt(6)
v_mfma_f32_32x32x16_bf16 v[64:79], v[140:143], a[168:171], v[64:79]
v_mul_f32 v154, v154, v152
v_mul_f32 v155, v155, v153
v_cvt_pk_bf16_f32 v170, v154, v155
v_mfma_f32_32x32x16_bf16 v[80:95], v[140:143], a[172:175], v[80:95]
ds_read_b64_tr_b16 v[140:141], v179 offset:61440
ds_read_b64_tr_b16 v[142:143], v179 offset:61952
v_mul_f32 v156, s76, v18
v_mfma_f32_32x32x16_bf16 v[112:127], v[144:147], a[172:175], v[112:127]
v_mul_f32 v157, s76, v19
v_exp_f32 v156, v156
v_mfma_f32_32x32x16_bf16 v[96:111], v[144:147], a[168:171], v[96:111]
ds_read_b64_tr_b16 v[144:145], v179 offset:62464
ds_read_b64_tr_b16 v[146:147], v179 offset:62976
v_exp_f32 v157, v157
v_mfma_f32_32x32x16_bf16 v[192:207], v[148:151], a[168:171], v[192:207]
v_mul_f32 v158, v18, v50
v_mul_f32 v159, v19, v51
v_mfma_f32_32x32x16_bf16 v[208:223], v[148:151], a[172:175], v[208:223]
ds_read_b64_tr_b16 v[148:149], v168 offset:6144
ds_read_b64_tr_b16 v[150:151], v168 offset:6656
s_mov_b64 exec, s[80:81]
ds_read_b128 v[148:151], v169 offset:736
s_mov_b64 exec, -1
s_waitcnt lgkmcnt(6)
v_mfma_f32_32x32x16_bf16 v[64:79], v[128:131], a[176:179], v[64:79]
v_add_f32 v156, 1.0, v156
v_add_f32 v157, 1.0, v157
v_mfma_f32_32x32x16_bf16 v[80:95], v[128:131], a[180:183], v[80:95]
s_waitcnt vmcnt(12)
s_barrier
ds_read_b64_tr_b16 v[128:129], v180 offset:32768
ds_read_b64_tr_b16 v[130:131], v180 offset:33280
s_add_u32 m0, s70, 150016
s_add_u32 s79, s70, 65536
v_mfma_f32_32x32x16_bf16 v[112:127], v[132:135], a[180:183], v[112:127]
buffer_load_dwordx4 v190, s[28:31], s79 offen lds
v_rcp_f32 v156, v156
v_mfma_f32_32x32x16_bf16 v[96:111], v[132:135], a[176:179], v[96:111]
ds_read_b64_tr_b16 v[132:133], v180 offset:33792
ds_read_b64_tr_b16 v[134:135], v180 offset:34304
v_mfma_f32_32x32x16_bf16 v[192:207], v[136:139], a[176:179], v[192:207]
buffer_load_dwordx4 v190, s[28:31], s79 offen offset:1024 lds
v_rcp_f32 v157, v157
v_mfma_f32_32x32x16_bf16 v[208:223], v[136:139], a[180:183], v[208:223]
ds_read_b64_tr_b16 v[136:137], v168 offset:8192
ds_read_b64_tr_b16 v[138:139], v168 offset:8704
s_mov_b64 exec, s[80:81]
ds_read_b128 v[136:139], v169 offset:768
s_mov_b64 exec, -1
s_waitcnt lgkmcnt(6)
v_mfma_f32_32x32x16_bf16 v[64:79], v[140:143], a[184:187], v[64:79]
s_add_u32 s64, s75, 590080
s_add_u32 m0, s72, 81920
s_add_u32 s65, s64, 73728
v_mul_f32 v158, v158, v156
v_mfma_f32_32x32x16_bf16 v[80:95], v[140:143], a[188:191], v[80:95]
ds_read_b64_tr_b16 v[140:141], v180 offset:36864
ds_read_b64_tr_b16 v[142:143], v180 offset:37376
v_mul_f32 v159, v159, v157
v_cvt_pk_bf16_f32 v171, v158, v159
v_mfma_f32_32x32x16_bf16 v[112:127], v[144:147], a[188:191], v[112:127]
buffer_load_dwordx4 v181, s[32:35], s64 offen lds
v_mul_f32 v160, s76, v20
v_mfma_f32_32x32x16_bf16 v[96:111], v[144:147], a[184:187], v[96:111]
ds_read_b64_tr_b16 v[144:145], v180 offset:37888
ds_read_b64_tr_b16 v[146:147], v180 offset:38400
v_mul_f32 v161, s76, v21
v_mfma_f32_32x32x16_bf16 v[192:207], v[148:151], a[184:187], v[192:207]
buffer_load_dwordx4 v181, s[36:39], s64 offen offset:1024 lds
s_add_u32 m0, s72, 83968
v_exp_f32 v160, v160
v_mfma_f32_32x32x16_bf16 v[208:223], v[148:151], a[188:191], v[208:223]
ds_read_b64_tr_b16 v[148:149], v168 offset:10240
ds_read_b64_tr_b16 v[150:151], v168 offset:10752
s_mov_b64 exec, s[80:81]
ds_read_b128 v[148:151], v169 offset:800
s_mov_b64 exec, -1
s_waitcnt lgkmcnt(6)
v_mfma_f32_32x32x16_bf16 v[64:79], v[128:131], a[192:195], v[64:79]
buffer_load_dwordx4 v181, s[32:35], s65 offen lds
v_exp_f32 v161, v161
v_mfma_f32_32x32x16_bf16 v[80:95], v[128:131], a[196:199], v[80:95]
ds_read_b64_tr_b16 v[128:129], v180 offset:40960
ds_read_b64_tr_b16 v[130:131], v180 offset:41472
v_mul_f32 v162, v20, v52
v_mfma_f32_32x32x16_bf16 v[112:127], v[132:135], a[196:199], v[112:127]
buffer_load_dwordx4 v181, s[36:39], s65 offen offset:1024 lds
v_mul_f32 v163, v21, v53
v_mfma_f32_32x32x16_bf16 v[96:111], v[132:135], a[192:195], v[96:111]
ds_read_b64_tr_b16 v[132:133], v180 offset:41984
ds_read_b64_tr_b16 v[134:135], v180 offset:42496
v_add_f32 v160, 1.0, v160
v_mfma_f32_32x32x16_bf16 v[192:207], v[136:139], a[192:195], v[192:207]
v_add_f32 v161, 1.0, v161
v_rcp_f32 v160, v160
v_mfma_f32_32x32x16_bf16 v[208:223], v[136:139], a[196:199], v[208:223]
ds_read_b64_tr_b16 v[136:137], v168 offset:12288
ds_read_b64_tr_b16 v[138:139], v168 offset:12800
s_mov_b64 exec, s[80:81]
ds_read_b128 v[136:139], v169 offset:832
s_mov_b64 exec, -1
s_waitcnt lgkmcnt(6)
v_mfma_f32_32x32x16_bf16 v[64:79], v[140:143], a[200:203], v[64:79]
v_rcp_f32 v161, v161
v_mfma_f32_32x32x16_bf16 v[80:95], v[140:143], a[204:207], v[80:95]
ds_read_b64_tr_b16 v[140:141], v180 offset:45056
ds_read_b64_tr_b16 v[142:143], v180 offset:45568
v_mul_f32 v162, v162, v160
v_mul_f32 v163, v163, v161
v_mfma_f32_32x32x16_bf16 v[112:127], v[144:147], a[204:207], v[112:127]
v_cvt_pk_bf16_f32 v172, v162, v163
v_mul_f32 v164, s76, v22
v_mfma_f32_32x32x16_bf16 v[96:111], v[144:147], a[200:203], v[96:111]
ds_read_b64_tr_b16 v[144:145], v180 offset:46080
ds_read_b64_tr_b16 v[146:147], v180 offset:46592
v_mul_f32 v165, s76, v23
v_mfma_f32_32x32x16_bf16 v[192:207], v[148:151], a[200:203], v[192:207]
v_exp_f32 v164, v164
v_exp_f32 v165, v165
v_mfma_f32_32x32x16_bf16 v[208:223], v[148:151], a[204:207], v[208:223]
ds_read_b64_tr_b16 v[148:149], v168 offset:14336
ds_read_b64_tr_b16 v[150:151], v168 offset:14848
s_mov_b64 exec, s[80:81]
ds_read_b128 v[148:151], v169 offset:864
s_mov_b64 exec, -1
s_waitcnt lgkmcnt(6)
v_mfma_f32_32x32x16_bf16 v[64:79], v[128:131], a[208:211], v[64:79]
v_mul_f32 v166, v22, v54
v_mfma_f32_32x32x16_bf16 v[80:95], v[128:131], a[212:215], v[80:95]
s_waitcnt vmcnt(11)
s_barrier
ds_read_b64_tr_b16 v[128:129], v179
ds_read_b64_tr_b16 v[130:131], v179 offset:512
v_mul_f32 v167, v23, v55
v_mfma_f32_32x32x16_bf16 v[112:127], v[132:135], a[212:215], v[112:127]
s_add_u32 s64, s75, 1179904
s_add_u32 m0, s72, 49152
s_add_u32 s65, s64, 73728
v_add_f32 v164, 1.0, v164
v_mfma_f32_32x32x16_bf16 v[96:111], v[132:135], a[208:211], v[96:111]
ds_read_b64_tr_b16 v[132:133], v179 offset:1024
ds_read_b64_tr_b16 v[134:135], v179 offset:1536
v_add_f32 v165, 1.0, v165
v_mfma_f32_32x32x16_bf16 v[192:207], v[136:139], a[208:211], v[192:207]
buffer_load_dwordx4 v181, s[32:35], s64 offen lds
v_rcp_f32 v164, v164
v_mfma_f32_32x32x16_bf16 v[208:223], v[136:139], a[212:215], v[208:223]
ds_read_b64_tr_b16 v[136:137], v168 offset:16384
ds_read_b64_tr_b16 v[138:139], v168 offset:16896
s_mov_b64 exec, s[80:81]
ds_read_b128 v[136:139], v169 offset:896
s_mov_b64 exec, -1
s_waitcnt lgkmcnt(6)
v_mfma_f32_32x32x16_bf16 v[64:79], v[140:143], a[216:219], v[64:79]
buffer_load_dwordx4 v181, s[36:39], s64 offen offset:1024 lds
s_add_u32 m0, s72, 51200
v_rcp_f32 v165, v165
v_mfma_f32_32x32x16_bf16 v[80:95], v[140:143], a[220:223], v[80:95]
ds_read_b64_tr_b16 v[140:141], v179 offset:4096
ds_read_b64_tr_b16 v[142:143], v179 offset:4608
v_mul_f32 v166, v166, v164
v_mfma_f32_32x32x16_bf16 v[112:127], v[144:147], a[220:223], v[112:127]
buffer_load_dwordx4 v181, s[32:35], s65 offen lds
v_mul_f32 v167, v167, v165
v_mfma_f32_32x32x16_bf16 v[96:111], v[144:147], a[216:219], v[96:111]
ds_read_b64_tr_b16 v[144:145], v179 offset:5120
ds_read_b64_tr_b16 v[146:147], v179 offset:5632
v_cvt_pk_bf16_f32 v173, v166, v167
s_mov_b32 s85, 0
v_mfma_f32_32x32x16_bf16 v[192:207], v[148:151], a[216:219], v[192:207]
buffer_load_dwordx4 v181, s[36:39], s65 offen offset:1024 lds
v_mfma_f32_32x32x16_bf16 v[208:223], v[148:151], a[220:223], v[208:223]
ds_read_b64_tr_b16 v[148:149], v168 offset:18432
ds_read_b64_tr_b16 v[150:151], v168 offset:18944
s_mov_b64 exec, s[80:81]
ds_read_b128 v[148:151], v169 offset:928
s_mov_b64 exec, -1
s_waitcnt lgkmcnt(6)
v_mfma_f32_32x32x16_bf16 v[64:79], v[128:131], a[224:227], v[64:79]
buffer_store_dwordx4 v[170:173], v183, s[40:43], s85 offen sc1
v_mul_f32 v152, s76, v24
v_mfma_f32_32x32x16_bf16 v[80:95], v[128:131], a[228:231], v[80:95]
ds_read_b64_tr_b16 v[128:129], v179 offset:8192
ds_read_b64_tr_b16 v[130:131], v179 offset:8704
v_mul_f32 v153, s76, v25
v_mfma_f32_32x32x16_bf16 v[112:127], v[132:135], a[228:231], v[112:127]
v_exp_f32 v152, v152
v_mfma_f32_32x32x16_bf16 v[96:111], v[132:135], a[224:227], v[96:111]
ds_read_b64_tr_b16 v[132:133], v179 offset:9216
ds_read_b64_tr_b16 v[134:135], v179 offset:9728
v_exp_f32 v153, v153
v_mfma_f32_32x32x16_bf16 v[192:207], v[136:139], a[224:227], v[192:207]
v_mul_f32 v154, v24, v56
v_mul_f32 v155, v25, v57
v_add_f32 v152, 1.0, v152
v_mfma_f32_32x32x16_bf16 v[208:223], v[136:139], a[228:231], v[208:223]
ds_read_b64_tr_b16 v[136:137], v168 offset:20480
ds_read_b64_tr_b16 v[138:139], v168 offset:20992
s_mov_b64 exec, s[80:81]
ds_read_b128 v[136:139], v169 offset:960
s_mov_b64 exec, -1
s_waitcnt lgkmcnt(6)
v_mfma_f32_32x32x16_bf16 v[64:79], v[140:143], a[232:235], v[64:79]
v_add_f32 v153, 1.0, v153
v_rcp_f32 v152, v152
v_mfma_f32_32x32x16_bf16 v[80:95], v[140:143], a[236:239], v[80:95]
ds_read_b64_tr_b16 v[140:141], v179 offset:12288
ds_read_b64_tr_b16 v[142:143], v179 offset:12800
v_mfma_f32_32x32x16_bf16 v[112:127], v[144:147], a[236:239], v[112:127]
v_rcp_f32 v153, v153
v_mul_f32 v154, v154, v152
v_mfma_f32_32x32x16_bf16 v[96:111], v[144:147], a[232:235], v[96:111]
ds_read_b64_tr_b16 v[144:145], v179 offset:13312
ds_read_b64_tr_b16 v[146:147], v179 offset:13824
v_mul_f32 v155, v155, v153
v_mfma_f32_32x32x16_bf16 v[192:207], v[148:151], a[232:235], v[192:207]
v_cvt_pk_bf16_f32 v174, v154, v155
v_mul_f32 v156, s76, v26
v_mul_f32 v157, s76, v27
v_mfma_f32_32x32x16_bf16 v[208:223], v[148:151], a[236:239], v[208:223]
ds_read_b64_tr_b16 v[148:149], v168 offset:22528
ds_read_b64_tr_b16 v[150:151], v168 offset:23040
s_mov_b64 exec, s[80:81]
ds_read_b128 v[148:151], v169 offset:992
s_mov_b64 exec, -1
s_waitcnt lgkmcnt(6)
v_mfma_f32_32x32x16_bf16 v[64:79], v[128:131], a[240:243], v[64:79]
v_exp_f32 v156, v156
v_mfma_f32_32x32x16_bf16 v[80:95], v[128:131], a[244:247], v[80:95]
s_waitcnt vmcnt(9)
s_barrier
ds_read_b64_tr_b16 v[128:129], v179 offset:16384
ds_read_b64_tr_b16 v[130:131], v179 offset:16896
v_mfma_f32_32x32x16_bf16 v[112:127], v[132:135], a[244:247], v[112:127]
s_add_u32 s64, s75, 1769728
s_add_u32 m0, s72, 65536
s_add_u32 s65, s64, 73728
v_exp_f32 v157, v157
v_mfma_f32_32x32x16_bf16 v[96:111], v[132:135], a[240:243], v[96:111]
ds_read_b64_tr_b16 v[132:133], v179 offset:17408
ds_read_b64_tr_b16 v[134:135], v179 offset:17920
v_mul_f32 v158, v26, v58
v_mul_f32 v159, v27, v59
v_mfma_f32_32x32x16_bf16 v[192:207], v[136:139], a[240:243], v[192:207]
buffer_load_dwordx4 v181, s[32:35], s64 offen lds
v_add_f32 v156, 1.0, v156
v_mfma_f32_32x32x16_bf16 v[208:223], v[136:139], a[244:247], v[208:223]
ds_read_b64_tr_b16 v[136:137], v168 offset:35328
ds_read_b64_tr_b16 v[138:139], v168 offset:35840
s_mov_b64 exec, s[80:81]
ds_read_b128 v[136:139], v169 offset:1024
s_mov_b64 exec, -1
s_waitcnt lgkmcnt(6)
v_mfma_f32_32x32x16_bf16 v[64:79], v[140:143], a[248:251], v[64:79]
buffer_load_dwordx4 v181, s[36:39], s64 offen offset:1024 lds
s_add_u32 m0, s72, 67584
v_add_f32 v157, 1.0, v157
v_mfma_f32_32x32x16_bf16 v[80:95], v[140:143], a[252:255], v[80:95]
ds_read_b64_tr_b16 v[140:141], v179 offset:20480
ds_read_b64_tr_b16 v[142:143], v179 offset:20992
v_rcp_f32 v156, v156
v_mfma_f32_32x32x16_bf16 v[112:127], v[144:147], a[252:255], v[112:127]
buffer_load_dwordx4 v181, s[32:35], s65 offen lds
v_mfma_f32_32x32x16_bf16 v[96:111], v[144:147], a[248:251], v[96:111]
ds_read_b64_tr_b16 v[144:145], v179 offset:21504
ds_read_b64_tr_b16 v[146:147], v179 offset:22016
v_rcp_f32 v157, v157
v_mfma_f32_32x32x16_bf16 v[192:207], v[148:151], a[248:251], v[192:207]
buffer_load_dwordx4 v181, s[36:39], s65 offen offset:1024 lds
v_mul_f32 v158, v158, v156
v_mul_f32 v159, v159, v157
v_mfma_f32_32x32x16_bf16 v[208:223], v[148:151], a[252:255], v[208:223]
ds_read_b64_tr_b16 v[148:149], v168 offset:37376
ds_read_b64_tr_b16 v[150:151], v168 offset:37888
s_mov_b64 exec, s[80:81]
ds_read_b128 v[148:151], v169 offset:1056
s_mov_b64 exec, -1
s_waitcnt lgkmcnt(6)
v_mfma_f32_32x32x16_bf16 v[64:79], v[128:131], v[224:227], v[64:79]
v_cvt_pk_bf16_f32 v175, v158, v159
v_mul_f32 v160, s76, v28
v_mfma_f32_32x32x16_bf16 v[80:95], v[128:131], v[228:231], v[80:95]
ds_read_b64_tr_b16 v[128:129], v179 offset:24576
ds_read_b64_tr_b16 v[130:131], v179 offset:25088
v_mul_f32 v161, s76, v29
v_mfma_f32_32x32x16_bf16 v[112:127], v[132:135], v[228:231], v[112:127]
v_exp_f32 v160, v160
v_mfma_f32_32x32x16_bf16 v[96:111], v[132:135], v[224:227], v[96:111]
ds_read_b64_tr_b16 v[132:133], v179 offset:25600
ds_read_b64_tr_b16 v[134:135], v179 offset:26112
v_exp_f32 v161, v161
v_mfma_f32_32x32x16_bf16 v[192:207], v[136:139], v[224:227], v[192:207]
v_mul_f32 v162, v28, v60
v_mul_f32 v163, v29, v61
v_add_f32 v160, 1.0, v160
v_mfma_f32_32x32x16_bf16 v[208:223], v[136:139], v[228:231], v[208:223]
ds_read_b64_tr_b16 v[136:137], v168 offset:39424
ds_read_b64_tr_b16 v[138:139], v168 offset:39936
s_mov_b64 exec, s[80:81]
ds_read_b128 v[136:139], v169 offset:1088
s_mov_b64 exec, -1
s_waitcnt lgkmcnt(6)
v_mfma_f32_32x32x16_bf16 v[64:79], v[140:143], v[232:235], v[64:79]
v_add_f32 v161, 1.0, v161
v_rcp_f32 v160, v160
v_mfma_f32_32x32x16_bf16 v[80:95], v[140:143], v[236:239], v[80:95]
ds_read_b64_tr_b16 v[140:141], v179 offset:28672
ds_read_b64_tr_b16 v[142:143], v179 offset:29184
v_mfma_f32_32x32x16_bf16 v[112:127], v[144:147], v[236:239], v[112:127]
v_rcp_f32 v161, v161
v_mul_f32 v162, v162, v160
v_mfma_f32_32x32x16_bf16 v[96:111], v[144:147], v[232:235], v[96:111]
ds_read_b64_tr_b16 v[144:145], v179 offset:29696
ds_read_b64_tr_b16 v[146:147], v179 offset:30208
v_mul_f32 v163, v163, v161
v_cvt_pk_bf16_f32 v176, v162, v163
v_mfma_f32_32x32x16_bf16 v[192:207], v[148:151], v[232:235], v[192:207]
v_mul_f32 v164, s76, v30
v_mul_f32 v165, s76, v31
v_mfma_f32_32x32x16_bf16 v[208:223], v[148:151], v[236:239], v[208:223]
ds_read_b64_tr_b16 v[148:149], v168 offset:41472
ds_read_b64_tr_b16 v[150:151], v168 offset:41984
s_mov_b64 exec, s[80:81]
ds_read_b128 v[148:151], v169 offset:1120
s_mov_b64 exec, -1
s_waitcnt lgkmcnt(6)
v_mfma_f32_32x32x16_bf16 v[64:79], v[128:131], v[240:243], v[64:79]
v_exp_f32 v164, v164
v_mfma_f32_32x32x16_bf16 v[80:95], v[128:131], v[244:247], v[80:95]
s_barrier
ds_read_b64_tr_b16 v[128:129], v179 offset:32768
ds_read_b64_tr_b16 v[130:131], v179 offset:33280
v_exp_f32 v165, v165
v_mfma_f32_32x32x16_bf16 v[112:127], v[132:135], v[244:247], v[112:127]
s_add_u32 s64, s75, 2359552
s_add_u32 m0, s72, 0
s_add_u32 s65, s64, 73728
v_mfma_f32_32x32x16_bf16 v[96:111], v[132:135], v[240:243], v[96:111]
ds_read_b64_tr_b16 v[132:133], v179 offset:33792
ds_read_b64_tr_b16 v[134:135], v179 offset:34304
v_mul_f32 v166, v30, v62
v_mul_f32 v167, v31, v63
v_mfma_f32_32x32x16_bf16 v[192:207], v[136:139], v[240:243], v[192:207]
buffer_load_dwordx4 v181, s[32:35], s64 offen lds
v_add_f32 v164, 1.0, v164
v_mfma_f32_32x32x16_bf16 v[208:223], v[136:139], v[244:247], v[208:223]
buffer_load_dwordx4 v181, s[36:39], s64 offen offset:1024 lds
s_add_u32 m0, s72, 2048
v_add_f32 v165, 1.0, v165
s_waitcnt lgkmcnt(4)
v_mfma_f32_32x32x16_bf16 v[64:79], v[140:143], v[248:251], v[64:79]
ds_read_b64_tr_b16 v[136:137], v179 offset:36864
ds_read_b64_tr_b16 v[138:139], v179 offset:37376
v_rcp_f32 v164, v164
v_mfma_f32_32x32x16_bf16 v[80:95], v[140:143], v[252:255], v[80:95]
ds_read_b64_tr_b16 v[140:141], v179 offset:37888
ds_read_b64_tr_b16 v[142:143], v179 offset:38400
v_mfma_f32_32x32x16_bf16 v[112:127], v[144:147], v[252:255], v[112:127]
buffer_load_dwordx4 v181, s[32:35], s65 offen lds
v_rcp_f32 v165, v165
v_mfma_f32_32x32x16_bf16 v[96:111], v[144:147], v[248:251], v[96:111]
buffer_load_dwordx4 v181, s[36:39], s65 offen offset:1024 lds
v_mul_f32 v166, v166, v164
v_mul_f32 v167, v167, v165
v_mfma_f32_32x32x16_bf16 v[192:207], v[148:151], v[248:251], v[192:207]
v_cvt_pk_bf16_f32 v177, v166, v167
s_mov_b32 s85, 0
v_mfma_f32_32x32x16_bf16 v[208:223], v[148:151], v[252:255], v[208:223]
buffer_store_dwordx4 v[174:177], v183, s[40:43], s85 offen offset:1024 sc1
; ==== p2 (1 iteration(s))
s_waitcnt lgkmcnt(4)
v_mfma_f32_32x32x16_bf16 v[0:15], v[128:131], a[0:3], 0
ds_read_b64_tr_b16 v[144:145], v179 offset:40960
ds_read_b64_tr_b16 v[146:147], v179 offset:41472
v_xor_b32 v190, 32, v185
v_and_b32 v152, 31, v178
v_lshrrev_b32 v153, 5, v178
v_mfma_f32_32x32x16_bf16 v[16:31], v[128:131], a[4:7], 0
ds_read_b64_tr_b16 v[148:149], v179 offset:41984
ds_read_b64_tr_b16 v[150:151], v179 offset:42496
s_mul_i32 s86, s57, 18
s_lshl_b32 s87, s60, 4
s_add_u32 s86, s86, s87
s_lshl_b32 s86, s86, 13
v_mfma_f32_32x32x16_bf16 v[48:63], v[132:135], a[4:7], 0
s_add_u32 s86, s86, 45056
v_lshlrev_b32 v154, 14, v153
v_lshl_add_u32 v154, v152, 2, v154
v_add_u32 v154, s86, v154
v_mov_b32 v155, 2113929216
v_mfma_f32_32x32x16_bf16 v[32:47], v[132:135], a[0:3], 0
v_lshlrev_b32 v156, 2, v152
v_add_u32 v156, 2404352, v156
v_cmp_eq_u32 vcc, 0, v153
s_cmp_eq_u32 s60, 0
s_nop 2
s_cselect_b64 s[0:1], -1, vcc
s_cselect_b64 s[2:3], -1, 0
s_waitcnt lgkmcnt(4)
v_mfma_f32_32x32x16_bf16 v[0:15], v[136:139], a[8:11], v[0:15]
ds_read_b64_tr_b16 v[128:129], v179 offset:45056
ds_read_b64_tr_b16 v[130:131], v179 offset:45568
s_xor_b32 s87, s60, 1
s_or_b32 s87, s87, s57
v_mfma_f32_32x32x16_bf16 v[16:31], v[136:139], a[12:15], v[16:31]
ds_read_b64_tr_b16 v[132:133], v179 offset:46080
ds_read_b64_tr_b16 v[134:135], v179 offset:46592
s_cmp_eq_u32 s87, 0
s_cselect_b64 s[90:91], -1, 0
v_cndmask_b32 v157, v155, v156, s[90:91]
v_cndmask_b32 v158, v157, v154, s[0:1]
v_mfma_f32_32x32x16_bf16 v[48:63], v[140:143], a[12:15], v[48:63]
v_cndmask_b32 v159, v155, v154, s[0:1]
v_cndmask_b32 v160, v155, v154, s[2:3]
v_lshlrev_b32 v161, 2, v152
s_lshl_b32 s87, s59, 8
s_add_u32 s87, s87, 149504
v_add_u32 v161, s87, v161
v_mfma_f32_32x32x16_bf16 v[32:47], v[140:143], a[8:11], v[32:47]
v_cmp_lt_u32 vcc, 31, v178
s_cmp_eq_u32 s60, 1
s_nop 2
s_cselect_b64 s[90:91], vcc, 0
v_mov_b32 v166, 196608
v_cndmask_b32 v161, v166, v161, s[90:91]
s_waitcnt lgkmcnt(4)
v_mfma_f32_32x32x16_bf16 v[0:15], v[144:147], a[16:19], v[0:15]
s_waitcnt vmcnt(14)
s_barrier
ds_read_b64_tr_b16 v[136:137], v180 offset:49152
ds_read_b64_tr_b16 v[138:139], v180 offset:49664
s_lshl_b32 s86, s56, 1
s_add_u32 s86, s86, s59
v_mfma_f32_32x32x16_bf16 v[16:31], v[144:147], a[20:23], v[16:31]
ds_read_b64_tr_b16 v[140:141], v180 offset:50176
ds_read_b64_tr_b16 v[142:143], v180 offset:50688
s_lshl_b32 s86, s86, 1
s_xor_b32 s87, s60, 0
s_add_u32 s86, s86, s87
v_mfma_f32_32x32x16_bf16 v[48:63], v[148:151], a[20:23], v[48:63]
s_add_u32 s64, s75, 2949376
s_add_u32 m0, s72, 16384
s_add_u32 s65, s64, 73728
s_lshl_b32 s86, s86, 7
v_cvt_pk_bf16_f32 v162, v192, v193
s_add_u32 s87, s86, 0
v_mfma_f32_32x32x16_bf16 v[32:47], v[148:151], a[16:19], v[32:47]
buffer_load_dwordx4 v181, s[32:35], s64 offen lds
buffer_store_dword v162, v158, s[40:43], s87 offen sc1
s_waitcnt lgkmcnt(4)
v_mfma_f32_32x32x16_bf16 v[0:15], v[128:131], a[24:27], v[0:15]
ds_read_b64_tr_b16 v[144:145], v180 offset:53248
ds_read_b64_tr_b16 v[146:147], v180 offset:53760
v_cvt_pk_bf16_f32 v163, v194, v195
s_add_u32 s87, s86, 8192
v_mfma_f32_32x32x16_bf16 v[16:31], v[128:131], a[28:31], v[16:31]
ds_read_b64_tr_b16 v[148:149], v180 offset:54272
ds_read_b64_tr_b16 v[150:151], v180 offset:54784
buffer_store_dword v163, v159, s[40:43], s87 offen sc1
v_cvt_pk_bf16_f32 v164, v196, v197
s_add_u32 s87, s86, 32768
v_mfma_f32_32x32x16_bf16 v[48:63], v[132:135], a[28:31], v[48:63]
buffer_load_dwordx4 v181, s[36:39], s64 offen offset:1024 lds
s_add_u32 m0, s72, 18432
buffer_store_dword v164, v160, s[40:43], s87 offen sc1
v_mfma_f32_32x32x16_bf16 v[32:47], v[132:135], a[24:27], v[32:47]
buffer_load_dwordx4 v181, s[32:35], s65 offen lds
v_cvt_pk_bf16_f32 v165, v198, v199
s_add_u32 s87, s86, 40960
s_waitcnt lgkmcnt(4)
v_mfma_f32_32x32x16_bf16 v[0:15], v[136:139], a[32:35], v[0:15]
ds_read_b64_tr_b16 v[128:129], v180 offset:57344
ds_read_b64_tr_b16 v[130:131], v180 offset:57856
buffer_store_dword v165, v160, s[40:43], s87 offen sc1
v_cvt_pk_bf16_f32 v162, v200, v201
v_mfma_f32_32x32x16_bf16 v[16:31], v[136:139], a[36:39], v[16:31]
ds_read_b64_tr_b16 v[132:133], v180 offset:58368
ds_read_b64_tr_b16 v[134:135], v180 offset:58880
s_add_u32 s87, s86, 65536
buffer_store_dword v162, v160, s[40:43], s87 offen sc1
v_mfma_f32_32x32x16_bf16 v[48:63], v[140:143], a[36:39], v[48:63]
buffer_load_dwordx4 v181, s[36:39], s65 offen offset:1024 lds
v_cvt_pk_bf16_f32 v163, v202, v203
s_add_u32 s87, s86, 73728
buffer_store_dword v163, v160, s[40:43], s87 offen sc1
v_mfma_f32_32x32x16_bf16 v[32:47], v[140:143], a[32:35], v[32:47]
v_cvt_pk_bf16_f32 v164, v204, v205
s_add_u32 s87, s86, 98304
buffer_store_dword v164, v160, s[40:43], s87 offen sc1
s_waitcnt lgkmcnt(4)
v_mfma_f32_32x32x16_bf16 v[0:15], v[144:147], a[40:43], v[0:15]
ds_read_b64_tr_b16 v[136:137], v180 offset:61440
ds_read_b64_tr_b16 v[138:139], v180 offset:61952
v_cvt_pk_bf16_f32 v165, v206, v207
s_add_u32 s87, s86, 106496
v_mfma_f32_32x32x16_bf16 v[16:31], v[144:147], a[44:47], v[16:31]
ds_read_b64_tr_b16 v[140:141], v180 offset:62464
ds_read_b64_tr_b16 v[142:143], v180 offset:62976
buffer_store_dword v165, v160, s[40:43], s87 offen sc1
ds_write_b32 v161, v194 offset:128
v_mfma_f32_32x32x16_bf16 v[48:63], v[148:151], a[44:47], v[48:63]
s_lshl_b32 s86, s56, 1
s_add_u32 s86, s86, s59
s_lshl_b32 s86, s86, 1
s_xor_b32 s87, s60, 1
s_add_u32 s86, s86, s87
s_lshl_b32 s86, s86, 7
v_mfma_f32_32x32x16_bf16 v[32:47], v[148:151], a[40:43], v[32:47]
v_cvt_pk_bf16_f32 v162, v208, v209
s_add_u32 s87, s86, 0
buffer_store_dword v162, v158, s[40:43], s87 offen sc1
v_cvt_pk_bf16_f32 v163, v210, v211
s_waitcnt lgkmcnt(5)
v_mfma_f32_32x32x16_bf16 v[0:15], v[128:131], a[48:51], v[0:15]
s_waitcnt vmcnt(23)
s_barrier
ds_read_b64_tr_b16 v[144:145], v179 offset:49152
ds_read_b64_tr_b16 v[146:147], v179 offset:49664
s_add_u32 s87, s86, 8192
v_mfma_f32_32x32x16_bf16 v[16:31], v[128:131], a[52:55], v[16:31]
ds_read_b64_tr_b16 v[148:149], v179 offset:50176
ds_read_b64_tr_b16 v[150:151], v179 offset:50688
buffer_store_dword v163, v159, s[40:43], s87 offen sc1
v_cvt_pk_bf16_f32 v164, v212, v213
v_mfma_f32_32x32x16_bf16 v[48:63], v[132:135], a[52:55], v[48:63]
s_add_u32 s64, s75, 3539200
s_add_u32 m0, s72, 32768
s_add_u32 s65, s64, 73728
s_add_u32 s87, s86, 32768
buffer_store_dword v164, v160, s[40:43], s87 offen sc1
v_mfma_f32_32x32x16_bf16 v[32:47], v[132:135], a[48:51], v[32:47]
buffer_load_dwordx4 v181, s[32:35], s64 offen lds
v_cvt_pk_bf16_f32 v165, v214, v215
s_add_u32 s87, s86, 40960
s_waitcnt lgkmcnt(5)
v_mfma_f32_32x32x16_bf16 v[0:15], v[136:139], a[56:59], v[0:15]
ds_read_b64_tr_b16 v[128:129], v179 offset:53248
ds_read_b64_tr_b16 v[130:131], v179 offset:53760
buffer_store_dword v165, v160, s[40:43], s87 offen sc1
v_cvt_pk_bf16_f32 v162, v216, v217
v_mfma_f32_32x32x16_bf16 v[16:31], v[136:139], a[60:63], v[16:31]
ds_read_b64_tr_b16 v[132:133], v179 offset:54272
ds_read_b64_tr_b16 v[134:135], v179 offset:54784
s_add_u32 s87, s86, 65536
buffer_store_dword v162, v160, s[40:43], s87 offen sc1
v_mfma_f32_32x32x16_bf16 v[48:63], v[140:143], a[60:63], v[48:63]
buffer_load_dwordx4 v181, s[36:39], s64 offen offset:1024 lds
s_add_u32 m0, s72, 34816
v_cvt_pk_bf16_f32 v163, v218, v219
s_add_u32 s87, s86, 73728
v_mfma_f32_32x32x16_bf16 v[32:47], v[140:143], a[56:59], v[32:47]
buffer_load_dwordx4 v181, s[32:35], s65 offen lds
buffer_store_dword v163, v160, s[40:43], s87 offen sc1
s_waitcnt lgkmcnt(4)
v_mfma_f32_32x32x16_bf16 v[0:15], v[144:147], a[64:67], v[0:15]
ds_read_b64_tr_b16 v[136:137], v179 offset:57344
ds_read_b64_tr_b16 v[138:139], v179 offset:57856
v_cvt_pk_bf16_f32 v164, v220, v221
s_add_u32 s87, s86, 98304
buffer_store_dword v164, v160, s[40:43], s87 offen sc1
v_mfma_f32_32x32x16_bf16 v[16:31], v[144:147], a[68:71], v[16:31]
ds_read_b64_tr_b16 v[140:141], v179 offset:58368
ds_read_b64_tr_b16 v[142:143], v179 offset:58880
v_cvt_pk_bf16_f32 v165, v222, v223
s_add_u32 s87, s86, 106496
v_mfma_f32_32x32x16_bf16 v[48:63], v[148:151], a[68:71], v[48:63]
buffer_load_dwordx4 v181, s[36:39], s65 offen offset:1024 lds
buffer_store_dword v165, v160, s[40:43], s87 offen sc1
ds_write_b32 v161, v210
v_mfma_f32_32x32x16_bf16 v[32:47], v[148:151], a[64:67], v[32:47]
v_lshrrev_b32 v192, 4, v178
v_and_b32 v193, 15, v178
s_lshl_b32 s86, s56, 7
s_lshl_b32 s87, s58, 5
s_add_u32 s86, s86, s87
s_waitcnt lgkmcnt(5)
v_mfma_f32_32x32x16_bf16 v[0:15], v[128:131], a[72:75], v[0:15]
ds_read_b64_tr_b16 v[144:145], v179 offset:61440
ds_read_b64_tr_b16 v[146:147], v179 offset:61952
v_add_u32 v194, s86, v192
s_mov_b32 s87, 578
v_mul_lo_u32 v194, v194, s87
v_mfma_f32_32x32x16_bf16 v[16:31], v[128:131], a[76:79], v[16:31]
ds_read_b64_tr_b16 v[148:149], v179 offset:62464
ds_read_b64_tr_b16 v[150:151], v179 offset:62976
s_mul_i32 s87, s57, 36
v_lshl_add_u32 v195, v193, 2, s87
v_add_u32 v194, v194, v195
v_mfma_f32_32x32x16_bf16 v[48:63], v[132:135], a[76:79], v[48:63]
v_cmp_gt_u32 vcc, 10, v193
v_mov_b32 v195, 2113929216
s_nop 0
v_cndmask_b32 v194, v195, v194, vcc
v_and_b32 v192, 31, v178
v_add_u32 v192, s86, v192
s_mov_b32 s87, 578
v_mfma_f32_32x32x16_bf16 v[32:47], v[132:135], a[72:75], v[32:47]
v_mul_lo_u32 v192, v192, s87
v_add_u32 v192, 576, v192
v_mul_f32 v152, s76, v64
v_mul_f32 v153, s76, v65
v_exp_f32 v152, v152
s_waitcnt lgkmcnt(5)
v_mfma_f32_32x32x16_bf16 v[0:15], v[136:139], a[80:83], v[0:15]
s_waitcnt vmcnt(29)
s_barrier
ds_read_b64_tr_b16 v[128:129], v180 offset:32768
ds_read_b64_tr_b16 v[130:131], v180 offset:33280
v_mfma_f32_32x32x16_bf16 v[16:31], v[136:139], a[84:87], v[16:31]
ds_read_b64_tr_b16 v[132:133], v180 offset:33792
ds_read_b64_tr_b16 v[134:135], v180 offset:34304
v_exp_f32 v153, v153
v_mul_f32 v154, v64, v96
v_mul_f32 v155, v65, v97
v_mfma_f32_32x32x16_bf16 v[48:63], v[140:143], a[84:87], v[48:63]
s_add_u32 s64, s75, 4129024
s_add_u32 m0, s72, 81920
s_add_u32 s65, s64, 73728
v_add_f32 v152, 1.0, v152
v_add_f32 v153, 1.0, v153
v_rcp_f32 v152, v152
v_mfma_f32_32x32x16_bf16 v[32:47], v[140:143], a[80:83], v[32:47]
buffer_load_dwordx4 v181, s[32:35], s64 offen lds
v_rcp_f32 v153, v153
s_waitcnt lgkmcnt(4)
v_mfma_f32_32x32x16_bf16 v[0:15], v[144:147], a[88:91], v[0:15]
ds_read_b64_tr_b16 v[136:137], v180 offset:36864
ds_read_b64_tr_b16 v[138:139], v180 offset:37376
s_mov_b32 s94, 0
buffer_load_dword v206, v194, s[44:47], s94 offen
v_mfma_f32_32x32x16_bf16 v[16:31], v[144:147], a[92:95], v[16:31]
ds_read_b64_tr_b16 v[140:141], v180 offset:37888
ds_read_b64_tr_b16 v[142:143], v180 offset:38400
buffer_load_dword v214, v194, s[48:51], s94 offen
v_mfma_f32_32x32x16_bf16 v[48:63], v[148:151], a[92:95], v[48:63]
buffer_load_dwordx4 v181, s[36:39], s64 offen offset:1024 lds
s_add_u32 m0, s72, 83968
v_mul_f32 v154, v154, v152
v_mul_f32 v155, v155, v153
v_cvt_pk_bf16_f32 v170, v154, v155
v_mfma_f32_32x32x16_bf16 v[32:47], v[148:151], a[88:91], v[32:47]
buffer_load_dwordx4 v181, s[32:35], s65 offen lds
v_mul_f32 v156, s76, v66
v_mul_f32 v157, s76, v67
s_waitcnt lgkmcnt(4)
v_mfma_f32_32x32x16_bf16 v[0:15], v[128:131], a[96:99], v[0:15]
ds_read_b64_tr_b16 v[144:145], v180 offset:40960
ds_read_b64_tr_b16 v[146:147], v180 offset:41472
v_exp_f32 v156, v156
v_exp_f32 v157, v157
v_mfma_f32_32x32x16_bf16 v[16:31], v[128:131], a[100:103], v[16:31]
ds_read_b64_tr_b16 v[148:149], v180 offset:41984
ds_read_b64_tr_b16 v[150:151], v180 offset:42496
v_mul_f32 v158, v66, v98
v_mul_f32 v159, v67, v99
v_add_f32 v156, 1.0, v156
v_mfma_f32_32x32x16_bf16 v[48:63], v[132:135], a[100:103], v[48:63]
buffer_load_dwordx4 v181, s[36:39], s65 offen offset:1024 lds
v_add_f32 v157, 1.0, v157
v_rcp_f32 v156, v156
v_mfma_f32_32x32x16_bf16 v[32:47], v[132:135], a[96:99], v[32:47]
v_rcp_f32 v157, v157
v_mul_f32 v158, v158, v156
v_mul_f32 v159, v159, v157
v_cvt_pk_bf16_f32 v171, v158, v159
s_waitcnt lgkmcnt(4)
v_mfma_f32_32x32x16_bf16 v[0:15], v[136:139], a[104:107], v[0:15]
ds_read_b64_tr_b16 v[128:129], v180 offset:45056
ds_read_b64_tr_b16 v[130:131], v180 offset:45568
v_mul_f32 v160, s76, v68
v_mul_f32 v161, s76, v69
v_exp_f32 v160, v160
v_mfma_f32_32x32x16_bf16 v[16:31], v[136:139], a[108:111], v[16:31]
ds_read_b64_tr_b16 v[132:133], v180 offset:46080
ds_read_b64_tr_b16 v[134:135], v180 offset:46592
s_mov_b32 s94, 2312
buffer_load_dword v207, v194, s[44:47], s94 offen
v_mfma_f32_32x32x16_bf16 v[48:63], v[140:143], a[108:111], v[48:63]
buffer_load_dword v215, v194, s[48:51], s94 offen
v_exp_f32 v161, v161
v_mul_f32 v162, v68, v100
v_mfma_f32_32x32x16_bf16 v[32:47], v[140:143], a[104:107], v[32:47]
v_mul_f32 v163, v69, v101
v_add_f32 v160, 1.0, v160
v_add_f32 v161, 1.0, v161
v_rcp_f32 v160, v160
s_waitcnt lgkmcnt(4)
v_mfma_f32_32x32x16_bf16 v[0:15], v[144:147], a[112:115], v[0:15]
s_waitcnt vmcnt(33)
s_barrier
ds_read_b64_tr_b16 v[136:137], v179
ds_read_b64_tr_b16 v[138:139], v179 offset:512
v_rcp_f32 v161, v161
v_mfma_f32_32x32x16_bf16 v[16:31], v[144:147], a[116:119], v[16:31]
ds_read_b64_tr_b16 v[140:141], v179 offset:1024
ds_read_b64_tr_b16 v[142:143], v179 offset:1536
v_mul_f32 v162, v162, v160
v_mul_f32 v163, v163, v161
v_cvt_pk_bf16_f32 v172, v162, v163
v_mfma_f32_32x32x16_bf16 v[48:63], v[148:151], a[116:119], v[48:63]
s_add_u32 s64, s75, 4718848
s_add_u32 m0, s72, 49152
s_add_u32 s65, s64, 73728
v_mul_f32 v164, s76, v70
v_mul_f32 v165, s76, v71
v_mfma_f32_32x32x16_bf16 v[32:47], v[148:151], a[112:115], v[32:47]
buffer_load_dwordx4 v181, s[32:35], s64 offen lds
v_exp_f32 v164, v164
v_exp_f32 v165, v165
s_waitcnt lgkmcnt(4)
v_mfma_f32_32x32x16_bf16 v[0:15], v[128:131], a[120:123], v[0:15]
ds_read_b64_tr_b16 v[144:145], v179 offset:4096
ds_read_b64_tr_b16 v[146:147], v179 offset:4608
v_mul_f32 v166, v70, v102
v_mul_f32 v167, v71, v103
v_add_f32 v164, 1.0, v164
v_mfma_f32_32x32x16_bf16 v[16:31], v[128:131], a[124:127], v[16:31]
ds_read_b64_tr_b16 v[148:149], v179 offset:5120
ds_read_b64_tr_b16 v[150:151], v179 offset:5632
v_add_f32 v165, 1.0, v165
v_rcp_f32 v164, v164
v_mfma_f32_32x32x16_bf16 v[48:63], v[132:135], a[124:127], v[48:63]
buffer_load_dwordx4 v181, s[36:39], s64 offen offset:1024 lds
s_add_u32 m0, s72, 51200
s_mov_b32 s94, 4624
buffer_load_dword v208, v194, s[44:47], s94 offen
v_mfma_f32_32x32x16_bf16 v[32:47], v[132:135], a[120:123], v[32:47]
buffer_load_dwordx4 v181, s[32:35], s65 offen lds
buffer_load_dword v216, v194, s[48:51], s94 offen
s_waitcnt lgkmcnt(4)
v_mfma_f32_32x32x16_bf16 v[0:15], v[136:139], a[128:131], v[0:15]
ds_read_b64_tr_b16 v[128:129], v179 offset:8192
ds_read_b64_tr_b16 v[130:131], v179 offset:8704
v_rcp_f32 v165, v165
v_mfma_f32_32x32x16_bf16 v[16:31], v[136:139], a[132:135], v[16:31]
ds_read_b64_tr_b16 v[132:133], v179 offset:9216
ds_read_b64_tr_b16 v[134:135], v179 offset:9728
v_mul_f32 v166, v166, v164
v_mul_f32 v167, v167, v165
v_cvt_pk_bf16_f32 v173, v166, v167
s_mov_b32 s85, 16384
v_mfma_f32_32x32x16_bf16 v[48:63], v[140:143], a[132:135], v[48:63]
buffer_load_dwordx4 v181, s[36:39], s65 offen offset:1024 lds
buffer_store_dwordx4 v[170:173], v182, s[40:43], s85 offen sc1
v_mul_f32 v152, s76, v72
v_mfma_f32_32x32x16_bf16 v[32:47], v[140:143], a[128:131], v[32:47]
v_mul_f32 v153, s76, v73
v_exp_f32 v152, v152
v_exp_f32 v153, v153
s_waitcnt lgkmcnt(4)
v_mfma_f32_32x32x16_bf16 v[0:15], v[144:147], a[136:139], v[0:15]
ds_read_b64_tr_b16 v[136:137], v179 offset:12288
ds_read_b64_tr_b16 v[138:139], v179 offset:12800
v_mul_f32 v154, v72, v104
v_mul_f32 v155, v73, v105
v_add_f32 v152, 1.0, v152
v_mfma_f32_32x32x16_bf16 v[16:31], v[144:147], a[140:143], v[16:31]
ds_read_b64_tr_b16 v[140:141], v179 offset:13312
ds_read_b64_tr_b16 v[142:143], v179 offset:13824
v_add_f32 v153, 1.0, v153
v_rcp_f32 v152, v152
v_mfma_f32_32x32x16_bf16 v[48:63], v[148:151], a[140:143], v[48:63]
v_rcp_f32 v153, v153
v_mul_f32 v154, v154, v152
v_mul_f32 v155, v155, v153
v_cvt_pk_bf16_f32 v174, v154, v155
v_mul_f32 v156, s76, v74
v_mfma_f32_32x32x16_bf16 v[32:47], v[148:151], a[136:139], v[32:47]
s_mov_b32 s94, 6936
buffer_load_dword v209, v194, s[44:47], s94 offen
buffer_load_dword v217, v194, s[48:51], s94 offen
s_waitcnt lgkmcnt(4)
v_mfma_f32_32x32x16_bf16 v[0:15], v[128:131], a[144:147], v[0:15]
s_waitcnt vmcnt(32)
s_barrier
ds_read_b64_tr_b16 v[144:145], v179 offset:16384
ds_read_b64_tr_b16 v[146:147], v179 offset:16896
v_mul_f32 v157, s76, v75
v_mfma_f32_32x32x16_bf16 v[16:31], v[128:131], a[148:151], v[16:31]
ds_read_b64_tr_b16 v[148:149], v179 offset:17408
ds_read_b64_tr_b16 v[150:151], v179 offset:17920
v_exp_f32 v156, v156
v_mfma_f32_32x32x16_bf16 v[48:63], v[132:135], a[148:151], v[48:63]
s_add_u32 s64, s75, 384
s_add_u32 m0, s72, 65536
s_add_u32 s65, s64, 73728
v_exp_f32 v157, v157
v_mul_f32 v158, v74, v106
v_mul_f32 v159, v75, v107
v_mfma_f32_32x32x16_bf16 v[32:47], v[132:135], a[144:147], v[32:47]
buffer_load_dwordx4 v181, s[32:35], s64 offen lds
v_add_f32 v156, 1.0, v156
v_add_f32 v157, 1.0, v157
s_waitcnt lgkmcnt(4)
v_mfma_f32_32x32x16_bf16 v[0:15], v[136:139], a[152:155], v[0:15]
ds_read_b64_tr_b16 v[128:129], v179 offset:20480
ds_read_b64_tr_b16 v[130:131], v179 offset:20992
v_rcp_f32 v156, v156
v_rcp_f32 v157, v157
v_mfma_f32_32x32x16_bf16 v[16:31], v[136:139], a[156:159], v[16:31]
ds_read_b64_tr_b16 v[132:133], v179 offset:21504
ds_read_b64_tr_b16 v[134:135], v179 offset:22016
v_mul_f32 v158, v158, v156
v_mul_f32 v159, v159, v157
v_cvt_pk_bf16_f32 v175, v158, v159
v_mfma_f32_32x32x16_bf16 v[48:63], v[140:143], a[156:159], v[48:63]
buffer_load_dwordx4 v181, s[36:39], s64 offen offset:1024 lds
s_add_u32 m0, s72, 67584
v_mul_f32 v160, s76, v76
v_mul_f32 v161, s76, v77
v_mfma_f32_32x32x16_bf16 v[32:47], v[140:143], a[152:155], v[32:47]
buffer_load_dwordx4 v181, s[32:35], s65 offen lds
v_exp_f32 v160, v160
v_exp_f32 v161, v161
s_waitcnt lgkmcnt(4)
v_mfma_f32_32x32x16_bf16 v[0:15], v[144:147], a[160:163], v[0:15]
ds_read_b64_tr_b16 v[136:137], v179 offset:24576
ds_read_b64_tr_b16 v[138:139], v179 offset:25088
v_mul_f32 v162, v76, v108
v_mul_f32 v163, v77, v109
v_add_f32 v160, 1.0, v160
v_mfma_f32_32x32x16_bf16 v[16:31], v[144:147], a[164:167], v[16:31]
ds_read_b64_tr_b16 v[140:141], v179 offset:25600
ds_read_b64_tr_b16 v[142:143], v179 offset:26112
s_mov_b32 s94, 9248
buffer_load_dword v210, v194, s[44:47], s94 offen
v_mfma_f32_32x32x16_bf16 v[48:63], v[148:151], a[164:167], v[48:63]
buffer_load_dwordx4 v181, s[36:39], s65 offen offset:1024 lds
buffer_load_dword v218, v194, s[48:51], s94 offen
v_mfma_f32_32x32x16_bf16 v[32:47], v[148:151], a[160:163], v[32:47]
v_add_f32 v161, 1.0, v161
v_rcp_f32 v160, v160
v_rcp_f32 v161, v161
s_waitcnt lgkmcnt(4)
v_mfma_f32_32x32x16_bf16 v[0:15], v[128:131], a[168:171], v[0:15]
ds_read_b64_tr_b16 v[144:145], v179 offset:28672
ds_read_b64_tr_b16 v[146:147], v179 offset:29184
v_mul_f32 v162, v162, v160
v_mul_f32 v163, v163, v161
v_cvt_pk_bf16_f32 v176, v162, v163
v_mul_f32 v164, s76, v78
v_mfma_f32_32x32x16_bf16 v[16:31], v[128:131], a[172:175], v[16:31]
ds_read_b64_tr_b16 v[148:149], v179 offset:29696
ds_read_b64_tr_b16 v[150:151], v179 offset:30208
v_mul_f32 v165, s76, v79
v_exp_f32 v164, v164
v_mfma_f32_32x32x16_bf16 v[48:63], v[132:135], a[172:175], v[48:63]
v_exp_f32 v165, v165
v_mul_f32 v166, v78, v110
v_mul_f32 v167, v79, v111
v_add_f32 v164, 1.0, v164
v_add_f32 v165, 1.0, v165
v_mfma_f32_32x32x16_bf16 v[32:47], v[132:135], a[168:171], v[32:47]
v_rcp_f32 v164, v164
v_rcp_f32 v165, v165
v_mul_f32 v166, v166, v164
s_waitcnt lgkmcnt(4)
v_mfma_f32_32x32x16_bf16 v[0:15], v[136:139], a[176:179], v[0:15]
s_waitcnt vmcnt(24)
s_barrier
ds_read_b64_tr_b16 v[128:129], v179 offset:32768
ds_read_b64_tr_b16 v[130:131], v179 offset:33280
v_mul_f32 v167, v167, v165
v_mfma_f32_32x32x16_bf16 v[16:31], v[136:139], a[180:183], v[16:31]
ds_read_b64_tr_b16 v[132:133], v179 offset:33792
ds_read_b64_tr_b16 v[134:135], v179 offset:34304
v_cvt_pk_bf16_f32 v177, v166, v167
s_mov_b32 s94, 11560
buffer_load_dword v211, v194, s[44:47], s94 offen
v_mfma_f32_32x32x16_bf16 v[48:63], v[140:143], a[180:183], v[48:63]
s_add_u32 s64, s75, 590208
s_add_u32 m0, s72, 0
s_add_u32 s65, s64, 73728
buffer_load_dword v219, v194, s[48:51], s94 offen
v_mfma_f32_32x32x16_bf16 v[32:47], v[140:143], a[176:179], v[32:47]
buffer_load_dwordx4 v181, s[32:35], s64 offen lds
s_mov_b32 s85, 16384
s_waitcnt lgkmcnt(4)
v_mfma_f32_32x32x16_bf16 v[0:15], v[144:147], a[184:187], v[0:15]
ds_read_b64_tr_b16 v[136:137], v179 offset:36864
ds_read_b64_tr_b16 v[138:139], v179 offset:37376
buffer_store_dwordx4 v[174:177], v182, s[40:43], s85 offen offset:1024 sc1
s_add_u32 s94, s95, 384
v_mfma_f32_32x32x16_bf16 v[16:31], v[144:147], a[188:191], v[16:31]
ds_read_b64_tr_b16 v[140:141], v179 offset:37888
ds_read_b64_tr_b16 v[142:143], v179 offset:38400
buffer_load_dword v191, v187, s[32:35], s94 offen
s_add_u32 s94, s94, 1024
v_mfma_f32_32x32x16_bf16 v[48:63], v[148:151], a[188:191], v[48:63]
buffer_load_dwordx4 v181, s[36:39], s64 offen offset:1024 lds
s_add_u32 m0, s72, 2048
buffer_load_dword v191, v187, s[36:39], s94 offen
v_mul_f32 v152, s76, v80
v_mfma_f32_32x32x16_bf16 v[32:47], v[148:151], a[184:187], v[32:47]
buffer_load_dwordx4 v181, s[32:35], s65 offen lds
v_mul_f32 v153, s76, v81
v_exp_f32 v152, v152
s_waitcnt lgkmcnt(4)
v_mfma_f32_32x32x16_bf16 v[0:15], v[128:131], a[192:195], v[0:15]
ds_read_b64_tr_b16 v[144:145], v179 offset:40960
ds_read_b64_tr_b16 v[146:147], v179 offset:41472
v_exp_f32 v153, v153
v_mul_f32 v154, v80, v112
v_mfma_f32_32x32x16_bf16 v[16:31], v[128:131], a[196:199], v[16:31]
ds_read_b64_tr_b16 v[148:149], v179 offset:41984
ds_read_b64_tr_b16 v[150:151], v179 offset:42496
v_mul_f32 v155, v81, v113
v_add_f32 v152, 1.0, v152
v_add_f32 v153, 1.0, v153
v_mfma_f32_32x32x16_bf16 v[48:63], v[132:135], a[196:199], v[48:63]
buffer_load_dwordx4 v181, s[36:39], s65 offen offset:1024 lds
v_rcp_f32 v152, v152
v_rcp_f32 v153, v153
v_mfma_f32_32x32x16_bf16 v[32:47], v[132:135], a[192:195], v[32:47]
v_mul_f32 v154, v154, v152
v_mul_f32 v155, v155, v153
v_cvt_pk_bf16_f32 v170, v154, v155
v_mul_f32 v156, s76, v82
s_waitcnt lgkmcnt(4)
v_mfma_f32_32x32x16_bf16 v[0:15], v[136:139], a[200:203], v[0:15]
ds_read_b64_tr_b16 v[128:129], v179 offset:45056
ds_read_b64_tr_b16 v[130:131], v179 offset:45568
v_mul_f32 v157, s76, v83
v_exp_f32 v156, v156
v_mfma_f32_32x32x16_bf16 v[16:31], v[136:139], a[204:207], v[16:31]
ds_read_b64_tr_b16 v[132:133], v179 offset:46080
ds_read_b64_tr_b16 v[134:135], v179 offset:46592
v_exp_f32 v157, v157
s_mov_b32 s94, 13872
v_mfma_f32_32x32x16_bf16 v[48:63], v[140:143], a[204:207], v[48:63]
buffer_load_dword v212, v194, s[44:47], s94 offen
buffer_load_dword v220, v194, s[48:51], s94 offen
v_mul_f32 v158, v82, v114
v_mul_f32 v159, v83, v115
v_mfma_f32_32x32x16_bf16 v[32:47], v[140:143], a[200:203], v[32:47]
v_add_f32 v156, 1.0, v156
v_add_f32 v157, 1.0, v157
v_rcp_f32 v156, v156
s_waitcnt lgkmcnt(4)
v_mfma_f32_32x32x16_bf16 v[0:15], v[144:147], a[208:211], v[0:15]
s_waitcnt vmcnt(28)
s_barrier
ds_read_b64_tr_b16 v[136:137], v180 offset:49152
ds_read_b64_tr_b16 v[138:139], v180 offset:49664
v_rcp_f32 v157, v157
v_mfma_f32_32x32x16_bf16 v[16:31], v[144:147], a[212:215], v[16:31]
ds_read_b64_tr_b16 v[140:141], v180 offset:50176
ds_read_b64_tr_b16 v[142:143], v180 offset:50688
v_mul_f32 v158, v158, v156
v_mul_f32 v159, v159, v157
v_cvt_pk_bf16_f32 v171, v158, v159
v_mul_f32 v160, s76, v84
v_mfma_f32_32x32x16_bf16 v[48:63], v[148:151], a[212:215], v[48:63]
s_add_u32 s64, s75, 1180032
s_add_u32 m0, s72, 16384
s_add_u32 s65, s64, 73728
v_mul_f32 v161, s76, v85
v_exp_f32 v160, v160
v_mfma_f32_32x32x16_bf16 v[32:47], v[148:151], a[208:211], v[32:47]
buffer_load_dwordx4 v181, s[32:35], s64 offen lds
v_exp_f32 v161, v161
s_waitcnt lgkmcnt(4)
v_mfma_f32_32x32x16_bf16 v[0:15], v[128:131], a[216:219], v[0:15]
ds_read_b64_tr_b16 v[144:145], v180 offset:53248
ds_read_b64_tr_b16 v[146:147], v180 offset:53760
v_mul_f32 v162, v84, v116
v_mul_f32 v163, v85, v117
v_add_f32 v160, 1.0, v160
v_add_f32 v161, 1.0, v161
v_mfma_f32_32x32x16_bf16 v[16:31], v[128:131], a[220:223], v[16:31]
ds_read_b64_tr_b16 v[148:149], v180 offset:54272
ds_read_b64_tr_b16 v[150:151], v180 offset:54784
v_rcp_f32 v160, v160
v_mfma_f32_32x32x16_bf16 v[48:63], v[132:135], a[220:223], v[48:63]
buffer_load_dwordx4 v181, s[36:39], s64 offen offset:1024 lds
s_add_u32 m0, s72, 18432
v_rcp_f32 v161, v161
s_mov_b32 s94, 16184
v_mfma_f32_32x32x16_bf16 v[32:47], v[132:135], a[216:219], v[32:47]
buffer_load_dwordx4 v181, s[32:35], s65 offen lds
buffer_load_dword v213, v194, s[44:47], s94 offen
s_waitcnt lgkmcnt(4)
v_mfma_f32_32x32x16_bf16 v[0:15], v[136:139], a[224:227], v[0:15]
ds_read_b64_tr_b16 v[128:129], v180 offset:57344
ds_read_b64_tr_b16 v[130:131], v180 offset:57856
buffer_load_dword v221, v194, s[48:51], s94 offen
v_mul_f32 v162, v162, v160
v_mfma_f32_32x32x16_bf16 v[16:31], v[136:139], a[228:231], v[16:31]
ds_read_b64_tr_b16 v[132:133], v180 offset:58368
ds_read_b64_tr_b16 v[134:135], v180 offset:58880
v_mul_f32 v163, v163, v161
v_cvt_pk_bf16_f32 v172, v162, v163
v_mul_f32 v164, s76, v86
v_mul_f32 v165, s76, v87
v_mfma_f32_32x32x16_bf16 v[48:63], v[140:143], a[228:231], v[48:63]
buffer_load_dwordx4 v181, s[36:39], s65 offen offset:1024 lds
v_exp_f32 v164, v164
v_mfma_f32_32x32x16_bf16 v[32:47], v[140:143], a[224:227], v[32:47]
v_exp_f32 v165, v165
v_mul_f32 v166, v86, v118
v_mul_f32 v167, v87, v119
v_add_f32 v164, 1.0, v164
v_add_f32 v165, 1.0, v165
s_waitcnt lgkmcnt(4)
v_mfma_f32_32x32x16_bf16 v[0:15], v[144:147], a[232:235], v[0:15]
ds_read_b64_tr_b16 v[136:137], v180 offset:61440
ds_read_b64_tr_b16 v[138:139], v180 offset:61952
v_rcp_f32 v164, v164
v_rcp_f32 v165, v165
v_mfma_f32_32x32x16_bf16 v[16:31], v[144:147], a[236:239], v[16:31]
ds_read_b64_tr_b16 v[140:141], v180 offset:62464
ds_read_b64_tr_b16 v[142:143], v180 offset:62976
v_mul_f32 v166, v166, v164
v_mul_f32 v167, v167, v165
v_cvt_pk_bf16_f32 v173, v166, v167
v_mfma_f32_32x32x16_bf16 v[48:63], v[148:151], a[236:239], v[48:63]
s_mov_b32 s85, 16384
buffer_store_dwordx4 v[170:173], v183, s[40:43], s85 offen sc1
v_mul_f32 v152, s76, v88
v_mul_f32 v153, s76, v89
v_mfma_f32_32x32x16_bf16 v[32:47], v[148:151], a[232:235], v[32:47]
buffer_load_ushort v205, v192, s[44:47], 0 offen
buffer_load_ushort v223, v192, s[48:51], 0 offen
s_waitcnt lgkmcnt(4)
v_mfma_f32_32x32x16_bf16 v[0:15], v[128:131], a[240:243], v[0:15]
s_waitcnt vmcnt(29)
s_barrier
ds_read_b64_tr_b16 v[144:145], v179 offset:49152
ds_read_b64_tr_b16 v[146:147], v179 offset:49664
v_exp_f32 v152, v152
v_mfma_f32_32x32x16_bf16 v[16:31], v[128:131], a[244:247], v[16:31]
ds_read_b64_tr_b16 v[148:149], v179 offset:50176
ds_read_b64_tr_b16 v[150:151], v179 offset:50688
v_exp_f32 v153, v153
v_mul_f32 v154, v88, v120
v_mfma_f32_32x32x16_bf16 v[48:63], v[132:135], a[244:247], v[48:63]
s_add_u32 s64, s75, 1769856
s_add_u32 m0, s72, 32768
s_add_u32 s65, s64, 73728
v_mul_f32 v155, v89, v121
v_add_f32 v152, 1.0, v152
v_add_f32 v153, 1.0, v153
v_mfma_f32_32x32x16_bf16 v[32:47], v[132:135], a[240:243], v[32:47]
buffer_load_dwordx4 v181, s[32:35], s64 offen lds
v_rcp_f32 v152, v152
s_waitcnt lgkmcnt(4)
v_mfma_f32_32x32x16_bf16 v[0:15], v[136:139], a[248:251], v[0:15]
ds_read_b64_tr_b16 v[128:129], v179 offset:53248
ds_read_b64_tr_b16 v[130:131], v179 offset:53760
v_rcp_f32 v153, v153
v_mul_f32 v154, v154, v152
v_mul_f32 v155, v155, v153
v_mfma_f32_32x32x16_bf16 v[16:31], v[136:139], a[252:255], v[16:31]
ds_read_b64_tr_b16 v[132:133], v179 offset:54272
ds_read_b64_tr_b16 v[134:135], v179 offset:54784
v_cvt_pk_bf16_f32 v174, v154, v155
v_mul_f32 v156, s76, v90
v_mul_f32 v157, s76, v91
v_mfma_f32_32x32x16_bf16 v[48:63], v[140:143], a[252:255], v[48:63]
buffer_load_dwordx4 v181, s[36:39], s64 offen offset:1024 lds
s_add_u32 m0, s72, 34816
v_exp_f32 v156, v156
v_mfma_f32_32x32x16_bf16 v[32:47], v[140:143], a[248:251], v[32:47]
buffer_load_dwordx4 v181, s[32:35], s65 offen lds
v_exp_f32 v157, v157
v_mul_f32 v158, v90, v122
s_waitcnt lgkmcnt(4)
v_mfma_f32_32x32x16_bf16 v[0:15], v[144:147], v[224:227], v[0:15]
ds_read_b64_tr_b16 v[136:137], v179 offset:57344
ds_read_b64_tr_b16 v[138:139], v179 offset:57856
v_mul_f32 v159, v91, v123
v_add_f32 v156, 1.0, v156
v_add_f32 v157, 1.0, v157
v_mfma_f32_32x32x16_bf16 v[16:31], v[144:147], v[228:231], v[16:31]
ds_read_b64_tr_b16 v[140:141], v179 offset:58368
ds_read_b64_tr_b16 v[142:143], v179 offset:58880
v_rcp_f32 v156, v156
v_rcp_f32 v157, v157
v_mfma_f32_32x32x16_bf16 v[48:63], v[148:151], v[228:231], v[48:63]
buffer_load_dwordx4 v181, s[36:39], s65 offen offset:1024 lds
v_mul_f32 v158, v158, v156
v_mul_f32 v159, v159, v157
v_cvt_pk_bf16_f32 v175, v158, v159
v_mfma_f32_32x32x16_bf16 v[32:47], v[148:151], v[224:227], v[32:47]
v_mul_f32 v160, s76, v92
v_mul_f32 v161, s76, v93
v_exp_f32 v160, v160
v_exp_f32 v161, v161
s_waitcnt lgkmcnt(4)
v_mfma_f32_32x32x16_bf16 v[0:15], v[128:131], v[232:235], v[0:15]
ds_read_b64_tr_b16 v[144:145], v179 offset:61440
ds_read_b64_tr_b16 v[146:147], v179 offset:61952
v_mul_f32 v162, v92, v124
v_mul_f32 v163, v93, v125
v_add_f32 v160, 1.0, v160
v_mfma_f32_32x32x16_bf16 v[16:31], v[128:131], v[236:239], v[16:31]
ds_read_b64_tr_b16 v[148:149], v179 offset:62464
ds_read_b64_tr_b16 v[150:151], v179 offset:62976
s_mul_i32 s94, s58, 147456
s_sub_u32 s94, s84, s94
v_mfma_f32_32x32x16_bf16 v[48:63], v[132:135], v[236:239], v[48:63]
buffer_load_dword v191, v187, s[32:35], s94 offen
s_add_u32 s94, s94, 1024
buffer_load_dword v191, v187, s[36:39], s94 offen
v_add_f32 v161, 1.0, v161
v_mfma_f32_32x32x16_bf16 v[32:47], v[132:135], v[232:235], v[32:47]
v_rcp_f32 v160, v160
v_rcp_f32 v161, v161
v_mul_f32 v162, v162, v160
s_waitcnt lgkmcnt(4)
v_mfma_f32_32x32x16_bf16 v[0:15], v[136:139], v[240:243], v[0:15]
s_waitcnt vmcnt(27)
s_barrier
ds_read_b64_tr_b16 v[128:129], v180 offset:32768
ds_read_b64_tr_b16 v[130:131], v180 offset:33280
v_mul_f32 v163, v163, v161
v_mfma_f32_32x32x16_bf16 v[16:31], v[136:139], v[244:247], v[16:31]
ds_read_b64_tr_b16 v[132:133], v180 offset:33792
ds_read_b64_tr_b16 v[134:135], v180 offset:34304
v_cvt_pk_bf16_f32 v176, v162, v163
v_mul_f32 v164, s76, v94
v_mul_f32 v165, s76, v95
v_mfma_f32_32x32x16_bf16 v[48:63], v[140:143], v[244:247], v[48:63]
s_add_u32 s64, s75, 2359680
s_add_u32 m0, s72, 81920
s_add_u32 s65, s64, 73728
v_exp_f32 v164, v164
v_exp_f32 v165, v165
v_mfma_f32_32x32x16_bf16 v[32:47], v[140:143], v[240:243], v[32:47]
buffer_load_dwordx4 v181, s[32:35], s64 offen lds
v_mul_f32 v166, v94, v126
v_mul_f32 v167, v95, v127
s_waitcnt lgkmcnt(4)
v_mfma_f32_32x32x16_bf16 v[0:15], v[144:147], v[248:251], v[0:15]
ds_read_b64_tr_b16 v[136:137], v180 offset:36864
ds_read_b64_tr_b16 v[138:139], v180 offset:37376
v_add_f32 v164, 1.0, v164
v_add_f32 v165, 1.0, v165
v_rcp_f32 v164, v164
v_mfma_f32_32x32x16_bf16 v[16:31], v[144:147], v[252:255], v[16:31]
ds_read_b64_tr_b16 v[140:141], v180 offset:37888
ds_read_b64_tr_b16 v[142:143], v180 offset:38400
v_rcp_f32 v165, v165
v_mul_f32 v166, v166, v164
v_mfma_f32_32x32x16_bf16 v[48:63], v[148:151], v[252:255], v[48:63]
buffer_load_dwordx4 v181, s[36:39], s64 offen offset:1024 lds
s_add_u32 m0, s72, 83968
v_mul_f32 v167, v167, v165
v_cvt_pk_bf16_f32 v177, v166, v167
v_mfma_f32_32x32x16_bf16 v[32:47], v[148:151], v[248:251], v[32:47]
buffer_load_dwordx4 v181, s[32:35], s65 offen lds
s_mov_b32 s85, 16384
buffer_store_dwordx4 v[174:177], v183, s[40:43], s85 offen offset:1024 sc1
; ==== p3 (1 iteration(s))
s_waitcnt lgkmcnt(4)
v_mfma_f32_32x32x16_bf16 v[64:79], v[128:131], a[0:3], 0
ds_read_b64_tr_b16 v[144:145], v180 offset:40960
ds_read_b64_tr_b16 v[146:147], v180 offset:41472
s_waitcnt vmcnt(10)
v_lshl_or_b32 v152, v223, 16, v205
v_and_b32 v153, 31, v178
s_lshl_b32 s86, s56, 7
s_lshl_b32 s87, s58, 5
v_mfma_f32_32x32x16_bf16 v[80:95], v[128:131], a[4:7], 0
ds_read_b64_tr_b16 v[148:149], v180 offset:41984
ds_read_b64_tr_b16 v[150:151], v180 offset:42496
s_add_u32 s86, s86, s87
v_add_u32 v153, s86, v153
v_lshlrev_b32 v153, 2, v153
v_mfma_f32_32x32x16_bf16 v[112:127], v[132:135], a[4:7], 0
buffer_load_dwordx4 v181, s[36:39], s65 offen offset:1024 lds
v_add_u32 v153, 4771840, v153
v_cmp_gt_u32 vcc, 32, v178
v_mov_b32 v154, 2113929216
s_cmp_eq_u32 s57, 0
s_nop 1
s_cselect_b64 s[2:3], vcc, 0
v_mfma_f32_32x32x16_bf16 v[96:111], v[132:135], a[0:3], 0
v_cndmask_b32 v153, v154, v153, s[2:3]
buffer_store_dword v152, v153, s[40:43], 0 offen sc1
v_and_b32 v155, 15, v178
s_waitcnt lgkmcnt(4)
v_mfma_f32_32x32x16_bf16 v[64:79], v[136:139], a[8:11], v[64:79]
ds_read_b64_tr_b16 v[128:129], v180 offset:45056
ds_read_b64_tr_b16 v[130:131], v180 offset:45568
v_lshrrev_b32 v156, 4, v178
s_mov_b32 s86, 288
v_mul_u32_u24 v157, s86, v155
v_mfma_f32_32x32x16_bf16 v[80:95], v[136:139], a[12:15], v[80:95]
ds_read_b64_tr_b16 v[132:133], v180 offset:46080
ds_read_b64_tr_b16 v[134:135], v180 offset:46592
v_lshl_add_u32 v157, v156, 2, v157
s_mul_i32 s86, s58, 2880
s_add_u32 s86, s86, 150016
v_add_u32 v157, s86, v157
v_mfma_f32_32x32x16_bf16 v[112:127], v[140:143], a[12:15], v[112:127]
v_cmp_gt_u32 vcc, 10, v155
v_mov_b32 v158, 196608
s_nop 0
v_cndmask_b32 v157, v158, v157, vcc
v_perm_b32 v159, v214, v206, s77
v_perm_b32 v160, v214, v206, s78
v_mfma_f32_32x32x16_bf16 v[96:111], v[140:143], a[8:11], v[96:111]
ds_write_b32 v157, v159
ds_write_b32 v157, v160 offset:144
v_perm_b32 v161, v215, v207, s77
v_perm_b32 v162, v215, v207, s78
ds_write_b32 v157, v161 offset:16
s_waitcnt lgkmcnt(7)
v_mfma_f32_32x32x16_bf16 v[64:79], v[144:147], a[16:19], v[64:79]
s_barrier
ds_read_b64_tr_b16 v[136:137], v179
ds_read_b64_tr_b16 v[138:139], v179 offset:512
ds_write_b32 v157, v162 offset:160
v_mfma_f32_32x32x16_bf16 v[80:95], v[144:147], a[20:23], v[80:95]
ds_read_b64_tr_b16 v[140:141], v179 offset:1024
ds_read_b64_tr_b16 v[142:143], v179 offset:1536
v_perm_b32 v159, v216, v208, s77
v_perm_b32 v160, v216, v208, s78
ds_write_b32 v157, v159 offset:32
v_mfma_f32_32x32x16_bf16 v[112:127], v[148:151], a[20:23], v[112:127]
s_add_u32 s64, s75, 2949504
s_add_u32 m0, s72, 49152
s_add_u32 s65, s64, 73728
ds_write_b32 v157, v160 offset:176
v_perm_b32 v161, v217, v209, s77
v_mfma_f32_32x32x16_bf16 v[96:111], v[148:151], a[16:19], v[96:111]
buffer_load_dwordx4 v181, s[32:35], s64 offen lds
v_perm_b32 v162, v217, v209, s78
ds_write_b32 v157, v161 offset:48
ds_write_b32 v157, v162 offset:192
s_waitcnt lgkmcnt(12)
v_mfma_f32_32x32x16_bf16 v[64:79], v[128:131], a[24:27], v[64:79]
ds_read_b64_tr_b16 v[144:145], v179 offset:4096
ds_read_b64_tr_b16 v[146:147], v179 offset:4608
v_perm_b32 v159, v218, v210, s77
v_perm_b32 v160, v218, v210, s78
ds_write_b32 v157, v159 offset:64
v_mfma_f32_32x32x16_bf16 v[80:95], v[128:131], a[28:31], v[80:95]
ds_read_b64_tr_b16 v[148:149], v179 offset:5120
ds_read_b64_tr_b16 v[150:151], v179 offset:5632
ds_write_b32 v157, v160 offset:208
v_perm_b32 v161, v219, v211, s77
v_mfma_f32_32x32x16_bf16 v[112:127], v[132:135], a[28:31], v[112:127]
buffer_load_dwordx4 v181, s[36:39], s64 offen offset:1024 lds
s_add_u32 m0, s72, 51200
v_perm_b32 v162, v219, v211, s78
ds_write_b32 v157, v161 offset:80
v_mfma_f32_32x32x16_bf16 v[96:111], v[132:135], a[24:27], v[96:111]
buffer_load_dwordx4 v181, s[32:35], s65 offen lds
ds_write_b32 v157, v162 offset:224
v_perm_b32 v159, v220, v212, s77
v_perm_b32 v160, v220, v212, s78
s_waitcnt lgkmcnt(12)
v_mfma_f32_32x32x16_bf16 v[64:79], v[136:139], a[32:35], v[64:79]
ds_read_b64_tr_b16 v[128:129], v179 offset:8192
ds_read_b64_tr_b16 v[130:131], v179 offset:8704
ds_write_b32 v157, v159 offset:96
ds_write_b32 v157, v160 offset:240
v_mfma_f32_32x32x16_bf16 v[80:95], v[136:139], a[36:39], v[80:95]
ds_read_b64_tr_b16 v[132:133], v179 offset:9216
ds_read_b64_tr_b16 v[134:135], v179 offset:9728
v_perm_b32 v161, v221, v213, s77
v_perm_b32 v162, v221, v213, s78
ds_write_b32 v157, v161 offset:112
v_mfma_f32_32x32x16_bf16 v[112:127], v[140:143], a[36:39], v[112:127]
buffer_load_dwordx4 v181, s[36:39], s65 offen offset:1024 lds
ds_write_b32 v157, v162 offset:256
v_lshrrev_b32 v155, 3, v178
v_and_b32 v156, 7, v178
s_mov_b32 s87, 144
v_mfma_f32_32x32x16_bf16 v[96:111], v[140:143], a[32:35], v[96:111]
v_mul_u32_u24 v158, s87, v155
v_lshl_add_u32 v158, v156, 4, v158
v_add_u32 v158, s86, v158
s_waitcnt lgkmcnt(0)
ds_read_b128 v[206:209], v158
v_mfma_f32_32x32x16_bf16 v[64:79], v[144:147], a[40:43], v[64:79]
ds_read_b64_tr_b16 v[136:137], v179 offset:12288
ds_read_b64_tr_b16 v[138:139], v179 offset:12800
ds_read_b128 v[210:213], v158 offset:1152
ds_read_b128 v[214:217], v158 offset:2304
s_mul_i32 s86, s57, 147456
v_mfma_f32_32x32x16_bf16 v[80:95], v[144:147], a[44:47], v[80:95]
ds_read_b64_tr_b16 v[140:141], v179 offset:13312
ds_read_b64_tr_b16 v[142:143], v179 offset:13824
s_lshl_b32 s87, s56, 9
s_add_u32 s86, s86, s87
s_lshl_b32 s87, s58, 7
v_mfma_f32_32x32x16_bf16 v[112:127], v[148:151], a[44:47], v[112:127]
s_add_u32 s86, s86, s87
s_add_u32 s86, s86, 2412544
v_lshlrev_b32 v163, 13, v155
v_lshl_add_u32 v163, v156, 4, v163
v_add_u32 v163, s86, v163
v_cmp_gt_u32 vcc, 2, v155
v_mov_b32 v164, 2113929216
s_nop 0
v_cndmask_b32 v164, v164, v163, vcc
v_mfma_f32_32x32x16_bf16 v[96:111], v[148:151], a[40:43], v[96:111]
s_mov_b32 s87, 0
s_waitcnt lgkmcnt(6)
buffer_store_dwordx4 v[206:209], v163, s[40:43], s87 offen sc1
v_mfma_f32_32x32x16_bf16 v[64:79], v[128:131], a[48:51], v[64:79]
s_barrier
ds_read_b64_tr_b16 v[144:145], v179 offset:16384
ds_read_b64_tr_b16 v[146:147], v179 offset:16896
s_mov_b32 s87, 65536
v_mfma_f32_32x32x16_bf16 v[80:95], v[128:131], a[52:55], v[80:95]
ds_read_b64_tr_b16 v[148:149], v179 offset:17408
ds_read_b64_tr_b16 v[150:151], v179 offset:17920
s_waitcnt lgkmcnt(7)
buffer_store_dwordx4 v[210:213], v163, s[40:43], s87 offen sc1
s_mov_b32 s87, 131072
s_waitcnt lgkmcnt(6)
buffer_store_dwordx4 v[214:217], v164, s[40:43], s87 offen sc1
v_mfma_f32_32x32x16_bf16 v[112:127], v[132:135], a[52:55], v[112:127]
s_add_u32 s64, s75, 3539328
s_add_u32 m0, s72, 65536
s_add_u32 s65, s64, 73728
s_mov_b32 s24, s14
s_and_b32 s25, s15, 65535
v_mfma_f32_32x32x16_bf16 v[96:111], v[132:135], a[48:51], v[96:111]
buffer_load_dwordx4 v181, s[32:35], s64 offen lds
s_mov_b32 s26, 5308416
s_mov_b32 s27, 131072
s_waitcnt lgkmcnt(4)
v_mfma_f32_32x32x16_bf16 v[64:79], v[136:139], a[56:59], v[64:79]
ds_read_b64_tr_b16 v[128:129], v179 offset:20480
ds_read_b64_tr_b16 v[130:131], v179 offset:20992
s_add_u32 s12, s14, 662528
s_addc_u32 s13, s15, 0
s_and_b32 s13, s13, 65535
s_mov_b32 s14, 4645888
v_mfma_f32_32x32x16_bf16 v[80:95], v[136:139], a[60:63], v[80:95]
ds_read_b64_tr_b16 v[132:133], v179 offset:21504
ds_read_b64_tr_b16 v[134:135], v179 offset:22016
s_mov_b32 s15, 131072
s_getpc_b64 s[0:1]
s_add_u32 s0, s0, spec_T3@rel32@lo+4
s_addc_u32 s1, s1, spec_T3@rel32@hi+12
s_mov_b32 s52, s0
s_and_b32 s53, s1, 65535
v_mfma_f32_32x32x16_bf16 v[112:127], v[140:143], a[60:63], v[112:127]
buffer_load_dwordx4 v181, s[36:39], s64 offen offset:1024 lds
s_add_u32 m0, s72, 67584
s_mov_b32 s54, 331776
s_mov_b32 s55, 131072
v_mfma_f32_32x32x16_bf16 v[96:111], v[140:143], a[56:59], v[96:111]
buffer_load_dwordx4 v181, s[32:35], s65 offen lds
s_getpc_b64 s[0:1]
s_add_u32 s0, s0, spec_S2@rel32@lo+4
s_addc_u32 s1, s1, spec_S2@rel32@hi+12
s_mov_b32 s44, s0
s_and_b32 s45, s1, 65535
s_waitcnt lgkmcnt(4)
v_mfma_f32_32x32x16_bf16 v[64:79], v[144:147], a[64:67], v[64:79]
ds_read_b64_tr_b16 v[136:137], v179 offset:24576
ds_read_b64_tr_b16 v[138:139], v179 offset:25088
s_mov_b32 s46, 45056
s_mov_b32 s47, 131072
s_mov_b32 s48, s20
s_and_b32 s49, s21, 65535
v_mfma_f32_32x32x16_bf16 v[80:95], v[144:147], a[68:71], v[80:95]
ds_read_b64_tr_b16 v[140:141], v179 offset:25600
ds_read_b64_tr_b16 v[142:143], v179 offset:26112
s_mov_b32 s50, 2359296
s_mov_b32 s51, 131072
s_lshl_b32 s86, s83, 1
v_mfma_f32_32x32x16_bf16 v[112:127], v[148:151], a[68:71], v[112:127]
buffer_load_dwordx4 v181, s[36:39], s65 offen offset:1024 lds
s_cmp_eq_u32 s83, 0
s_cselect_b32 s87, 0, 1
s_add_u32 s86, s86, s87
s_lshl_b32 s8, s86, 1
v_mfma_f32_32x32x16_bf16 v[96:111], v[148:151], a[64:67], v[96:111]
s_lshl_b32 s86, s86, 7
s_lshr_b32 s9, s83, 1
s_add_u32 s9, s9, 4
s_mul_i32 s87, s82, 1152
s_lshl_b32 s6, s58, 4
s_waitcnt lgkmcnt(4)
v_mfma_f32_32x32x16_bf16 v[64:79], v[128:131], a[72:75], v[64:79]
ds_read_b64_tr_b16 v[144:145], v179 offset:28672
ds_read_b64_tr_b16 v[146:147], v179 offset:29184
s_add_u32 s87, s87, s6
s_mul_i32 s87, s87, 1152
s_add_u32 s4, s87, s86
s_lshl_b32 s86, s9, 6
v_mfma_f32_32x32x16_bf16 v[80:95], v[128:131], a[76:79], v[80:95]
ds_read_b64_tr_b16 v[148:149], v179 offset:29696
ds_read_b64_tr_b16 v[150:151], v179 offset:30208
s_add_u32 s6, s87, s86
v_lshrrev_b32 v152, 3, v178
v_and_b32 v153, 7, v178
v_mfma_f32_32x32x16_bf16 v[112:127], v[132:135], a[76:79], v[112:127]
v_lshlrev_b32 v154, 1, v152
v_and_b32 v154, 4, v154
v_xor_b32 v154, v154, v153
v_lshlrev_b32 v154, 4, v154
s_mov_b32 s86, 1152
v_mad_u32_u24 v192, v152, s86, v154
v_mfma_f32_32x32x16_bf16 v[96:111], v[132:135], a[72:75], v[96:111]
v_lshrrev_b32 v154, 2, v178
v_and_b32 v155, 3, v178
v_lshlrev_b32 v155, 4, v155
v_mad_u32_u24 v193, v154, s86, v155
v_lshrrev_b32 v152, 4, v178
v_and_b32 v153, 15, v178
s_waitcnt lgkmcnt(4)
v_mfma_f32_32x32x16_bf16 v[64:79], v[136:139], a[80:83], v[64:79]
s_waitcnt vmcnt(19)
s_barrier
ds_read_b64_tr_b16 v[128:129], v179 offset:32768
ds_read_b64_tr_b16 v[130:131], v179 offset:33280
v_lshrrev_b32 v154, 2, v153
v_and_b32 v155, 3, v153
v_mfma_f32_32x32x16_bf16 v[80:95], v[136:139], a[84:87], v[80:95]
ds_read_b64_tr_b16 v[132:133], v179 offset:33792
ds_read_b64_tr_b16 v[134:135], v179 offset:34304
v_lshrrev_b32 v156, 1, v152
v_and_b32 v157, 1, v152
v_lshlrev_b32 v157, 1, v157
v_mfma_f32_32x32x16_bf16 v[112:127], v[140:143], a[84:87], v[112:127]
s_add_u32 s64, s75, 4129152
s_add_u32 m0, s72, 0
s_add_u32 s65, s64, 73728
v_lshrrev_b32 v158, 1, v155
v_add_u32 v157, v157, v158
v_lshrrev_b32 v158, 1, v154
v_mfma_f32_32x32x16_bf16 v[96:111], v[140:143], a[80:83], v[96:111]
buffer_load_dwordx4 v181, s[32:35], s64 offen lds
v_lshlrev_b32 v158, 2, v158
v_xor_b32 v157, v157, v158
v_lshlrev_b32 v157, 4, v157
s_waitcnt lgkmcnt(4)
v_mfma_f32_32x32x16_bf16 v[64:79], v[144:147], a[88:91], v[64:79]
ds_read_b64_tr_b16 v[136:137], v179 offset:36864
ds_read_b64_tr_b16 v[138:139], v179 offset:37376
v_and_b32 v158, 1, v155
v_lshl_add_u32 v157, v158, 3, v157
v_lshl_add_u32 v158, v156, 2, v154
v_lshl_add_u32 v157, v158, 7, v157
v_mfma_f32_32x32x16_bf16 v[80:95], v[144:147], a[92:95], v[80:95]
ds_read_b64_tr_b16 v[140:141], v179 offset:37888
ds_read_b64_tr_b16 v[142:143], v179 offset:38400
s_lshl_b32 s86, s60, 10
v_add_u32 v194, s86, v157
v_xor_b32 v195, 64, v194
v_mfma_f32_32x32x16_bf16 v[112:127], v[148:151], a[92:95], v[112:127]
buffer_load_dwordx4 v181, s[36:39], s64 offen offset:1024 lds
s_add_u32 m0, s72, 2048
v_add_u32 v196, 32768, v194
v_add_u32 v197, 32768, v195
v_and_b32 v157, 1, v152
v_mfma_f32_32x32x16_bf16 v[96:111], v[148:151], a[88:91], v[96:111]
buffer_load_dwordx4 v181, s[32:35], s65 offen lds
v_lshlrev_b32 v157, 5, v157
v_lshl_add_u32 v157, v155, 3, v157
v_lshl_add_u32 v157, v158, 6, v157
s_waitcnt lgkmcnt(4)
v_mfma_f32_32x32x16_bf16 v[64:79], v[128:131], a[96:99], v[64:79]
ds_read_b64_tr_b16 v[144:145], v179 offset:40960
ds_read_b64_tr_b16 v[146:147], v179 offset:41472
s_lshl_b32 s86, s60, 10
v_add_u32 v198, s86, v157
v_add_u32 v199, 32768, v198
v_mfma_f32_32x32x16_bf16 v[80:95], v[128:131], a[100:103], v[80:95]
ds_read_b64_tr_b16 v[148:149], v179 offset:41984
ds_read_b64_tr_b16 v[150:151], v179 offset:42496
s_lshl_b32 s86, s56, 4
s_lshl_b32 s87, s82, 2
s_add_u32 s86, s86, s87
s_lshl_b32 s87, s60, 1
v_mfma_f32_32x32x16_bf16 v[112:127], v[132:135], a[100:103], v[112:127]
buffer_load_dwordx4 v181, s[36:39], s65 offen offset:1024 lds
s_add_u32 s86, s86, s87
s_mul_i32 s86, s86, 73728
s_add_u32 s86, s86, 7139328
v_mfma_f32_32x32x16_bf16 v[96:111], v[132:135], a[96:99], v[96:111]
s_lshl_b32 s87, s59, 12
s_add_u32 s86, s86, s87
v_lshlrev_b32 v157, 4, v178
s_lshl_b32 s87, s60, 11
s_add_u32 s87, s86, s87
v_add_u32 v200, s87, v157
s_waitcnt lgkmcnt(4)
v_mfma_f32_32x32x16_bf16 v[64:79], v[136:139], a[104:107], v[64:79]
ds_read_b64_tr_b16 v[128:129], v179 offset:45056
ds_read_b64_tr_b16 v[130:131], v179 offset:45568
s_lshl_b32 s87, s60, 11
s_xor_b32 s87, s87, 2048
s_add_u32 s87, s86, s87
v_mfma_f32_32x32x16_bf16 v[80:95], v[136:139], a[108:111], v[80:95]
ds_read_b64_tr_b16 v[132:133], v179 offset:46080
ds_read_b64_tr_b16 v[134:135], v179 offset:46592
v_add_u32 v201, s87, v157
s_lshl_b32 s86, s82, 4
s_add_u32 s86, s86, s56
s_mul_i32 s86, s86, 147456
v_mfma_f32_32x32x16_bf16 v[112:127], v[140:143], a[108:111], v[112:127]
s_add_u32 s86, s86, 26013696
s_lshl_b32 s87, s59, 12
s_add_u32 s86, s86, s87
s_lshl_b32 s87, s60, 11
s_add_u32 s86, s86, s87
v_add_u32 v202, s86, v157
v_mfma_f32_32x32x16_bf16 v[96:111], v[140:143], a[104:107], v[96:111]
s_lshl_b32 s86, s58, 13
s_add_u32 s87, s86, 114688
v_add_u32 v203, s87, v157
s_xor_b32 s86, s86, 16384
s_add_u32 s87, s86, 114688
s_waitcnt lgkmcnt(4)
v_mfma_f32_32x32x16_bf16 v[64:79], v[144:147], a[112:115], v[64:79]
s_waitcnt vmcnt(16)
s_barrier
ds_read_b64_tr_b16 v[136:137], v180 offset:49152
ds_read_b64_tr_b16 v[138:139], v180 offset:49664
v_add_u32 v204, s87, v157
v_add_u32 v222, 32768, v184
v_mfma_f32_32x32x16_bf16 v[80:95], v[144:147], a[116:119], v[80:95]
ds_read_b64_tr_b16 v[140:141], v180 offset:50176
ds_read_b64_tr_b16 v[142:143], v180 offset:50688
s_lshl_b32 s23, s56, 4
s_add_u32 s23, s23, s57
s_lshl_b32 s61, s56, 3
v_mfma_f32_32x32x16_bf16 v[112:127], v[148:151], a[116:119], v[112:127]
s_add_u32 s64, s75, 4718976
s_add_u32 m0, s72, 16384
s_add_u32 s65, s64, 73728
s_lshr_b32 s86, s57, 1
s_add_u32 s61, s61, s86
s_add_u32 s61, s61, 256
v_mfma_f32_32x32x16_bf16 v[96:111], v[148:151], a[112:115], v[96:111]
buffer_load_dwordx4 v181, s[32:35], s64 offen lds
s_and_b32 s86, s57, 1
s_cmp_eq_u32 s86, 0
s_cselect_b32 s61, 999, s61
s_waitcnt lgkmcnt(4)
v_mfma_f32_32x32x16_bf16 v[64:79], v[128:131], a[120:123], v[64:79]
ds_read_b64_tr_b16 v[144:145], v180 offset:53248
ds_read_b64_tr_b16 v[146:147], v180 offset:53760
s_mul_i32 s86, s58, 18
v_add_u32 v152, s86, v178
s_mul_i32 s87, s82, 1152
s_mul_i32 s86, s56, 72
v_mfma_f32_32x32x16_bf16 v[80:95], v[128:131], a[124:127], v[80:95]
ds_read_b64_tr_b16 v[148:149], v180 offset:54272
ds_read_b64_tr_b16 v[150:151], v180 offset:54784
s_add_u32 s87, s87, s86
v_add_u32 v205, s87, v152
s_mov_b32 s87, 1152
v_mfma_f32_32x32x16_bf16 v[112:127], v[132:135], a[124:127], v[112:127]
buffer_load_dwordx4 v181, s[36:39], s64 offen offset:1024 lds
s_add_u32 m0, s72, 18432
v_mul_lo_u32 v205, v205, s87
v_cmp_gt_u32 vcc, 18, v178
v_mov_b32 v155, 2113929216
s_nop 0
v_cndmask_b32 v205, v155, v205, vcc
v_mfma_f32_32x32x16_bf16 v[96:111], v[132:135], a[120:123], v[96:111]
buffer_load_dwordx4 v181, s[32:35], s65 offen lds
s_mul_i32 s86, s58, 3
v_add_u32 v152, s86, v178
s_waitcnt lgkmcnt(4)
v_mfma_f32_32x32x16_bf16 v[64:79], v[136:139], a[128:131], v[64:79]
ds_read_b64_tr_b16 v[128:129], v180 offset:57344
ds_read_b64_tr_b16 v[130:131], v180 offset:57856
s_mul_i32 s87, s22, 12
v_add_u32 v223, s87, v152
v_lshlrev_b32 v223, 7, v223
v_mfma_f32_32x32x16_bf16 v[80:95], v[136:139], a[132:135], v[80:95]
ds_read_b64_tr_b16 v[132:133], v180 offset:58368
ds_read_b64_tr_b16 v[134:135], v180 offset:58880
v_cmp_gt_u32 vcc, 3, v178
v_mov_b32 v155, 2113929216
s_nop 0
v_cndmask_b32 v223, v155, v223, vcc
v_mfma_f32_32x32x16_bf16 v[112:127], v[140:143], a[132:135], v[112:127]
buffer_load_dwordx4 v181, s[36:39], s65 offen offset:1024 lds
buffer_load_dword v191, v223, s[52:55], 0 offen
s_lshl_b32 s87, s22, 2
s_add_u32 s87, s87, s58
v_mfma_f32_32x32x16_bf16 v[96:111], v[140:143], a[128:131], v[96:111]
v_add_u32 v223, s87, v178
v_lshlrev_b32 v223, 7, v223
v_cmp_gt_u32 vcc, 1, v178
v_mov_b32 v155, 2113929216
s_nop 0
v_cndmask_b32 v223, v155, v223, vcc
s_waitcnt lgkmcnt(4)
v_mfma_f32_32x32x16_bf16 v[64:79], v[144:147], a[136:139], v[64:79]
ds_read_b64_tr_b16 v[136:137], v180 offset:61440
ds_read_b64_tr_b16 v[138:139], v180 offset:61952
buffer_load_dword v191, v223, s[44:47], 0 offen
v_mul_f32 v152, s76, v0
v_mfma_f32_32x32x16_bf16 v[80:95], v[144:147], a[140:143], v[80:95]
ds_read_b64_tr_b16 v[140:141], v180 offset:62464
ds_read_b64_tr_b16 v[142:143], v180 offset:62976
v_mul_f32 v153, s76, v1
v_exp_f32 v152, v152
v_mfma_f32_32x32x16_bf16 v[112:127], v[148:151], a[140:143], v[112:127]
v_exp_f32 v153, v153
v_mul_f32 v154, v0, v32
v_mul_f32 v155, v1, v33
v_add_f32 v152, 1.0, v152
v_add_f32 v153, 1.0, v153
v_rcp_f32 v152, v152
v_mfma_f32_32x32x16_bf16 v[96:111], v[148:151], a[136:139], v[96:111]
v_rcp_f32 v153, v153
v_mul_f32 v154, v154, v152
v_mul_f32 v155, v155, v153
s_waitcnt lgkmcnt(4)
v_mfma_f32_32x32x16_bf16 v[64:79], v[128:131], a[144:147], v[64:79]
s_waitcnt vmcnt(17)
s_barrier
ds_read_b64_tr_b16 v[144:145], v179 offset:49152
ds_read_b64_tr_b16 v[146:147], v179 offset:49664
v_cvt_pk_bf16_f32 v170, v154, v155
v_mul_f32 v156, s76, v2
v_mfma_f32_32x32x16_bf16 v[80:95], v[128:131], a[148:151], v[80:95]
ds_read_b64_tr_b16 v[148:149], v179 offset:50176
ds_read_b64_tr_b16 v[150:151], v179 offset:50688
v_mul_f32 v157, s76, v3
v_exp_f32 v156, v156
v_mfma_f32_32x32x16_bf16 v[112:127], v[132:135], a[148:151], v[112:127]
s_add_u32 s64, s84, 0
s_add_u32 m0, s70, 32768
v_exp_f32 v157, v157
v_mul_f32 v158, v2, v34
v_mul_f32 v159, v3, v35
v_add_f32 v156, 1.0, v156
v_mfma_f32_32x32x16_bf16 v[96:111], v[132:135], a[144:147], v[96:111]
buffer_load_dwordx4 v185, s[32:35], s64 offen lds
v_add_f32 v157, 1.0, v157
v_rcp_f32 v156, v156
s_waitcnt lgkmcnt(4)
v_mfma_f32_32x32x16_bf16 v[64:79], v[136:139], a[152:155], v[64:79]
ds_read_b64_tr_b16 v[128:129], v179 offset:53248
ds_read_b64_tr_b16 v[130:131], v179 offset:53760
v_rcp_f32 v157, v157
v_mul_f32 v158, v158, v156
v_mfma_f32_32x32x16_bf16 v[80:95], v[136:139], a[156:159], v[80:95]
ds_read_b64_tr_b16 v[132:133], v179 offset:54272
ds_read_b64_tr_b16 v[134:135], v179 offset:54784
v_mul_f32 v159, v159, v157
v_cvt_pk_bf16_f32 v171, v158, v159
v_mul_f32 v160, s76, v4
v_mul_f32 v161, s76, v5
v_mfma_f32_32x32x16_bf16 v[112:127], v[140:143], a[156:159], v[112:127]
buffer_load_dwordx4 v190, s[36:39], s64 offen offset:1024 lds
v_exp_f32 v160, v160
v_exp_f32 v161, v161
v_mfma_f32_32x32x16_bf16 v[96:111], v[140:143], a[152:155], v[96:111]
v_mul_f32 v162, v4, v36
v_mul_f32 v163, v5, v37
v_add_f32 v160, 1.0, v160
v_add_f32 v161, 1.0, v161
s_waitcnt lgkmcnt(4)
v_mfma_f32_32x32x16_bf16 v[64:79], v[144:147], a[160:163], v[64:79]
ds_read_b64_tr_b16 v[136:137], v179 offset:57344
ds_read_b64_tr_b16 v[138:139], v179 offset:57856
v_rcp_f32 v160, v160
v_rcp_f32 v161, v161
v_mfma_f32_32x32x16_bf16 v[80:95], v[144:147], a[164:167], v[80:95]
ds_read_b64_tr_b16 v[140:141], v179 offset:58368
ds_read_b64_tr_b16 v[142:143], v179 offset:58880
v_mul_f32 v162, v162, v160
v_mul_f32 v163, v163, v161
v_cvt_pk_bf16_f32 v172, v162, v163
v_mul_f32 v164, s76, v6
v_mfma_f32_32x32x16_bf16 v[112:127], v[148:151], a[164:167], v[112:127]
v_mul_f32 v165, s76, v7
v_exp_f32 v164, v164
v_exp_f32 v165, v165
v_mul_f32 v166, v6, v38
v_mfma_f32_32x32x16_bf16 v[96:111], v[148:151], a[160:163], v[96:111]
v_mul_f32 v167, v7, v39
v_add_f32 v164, 1.0, v164
v_add_f32 v165, 1.0, v165
v_rcp_f32 v164, v164
s_waitcnt lgkmcnt(4)
v_mfma_f32_32x32x16_bf16 v[64:79], v[128:131], a[168:171], v[64:79]
ds_read_b64_tr_b16 v[144:145], v179 offset:61440
ds_read_b64_tr_b16 v[146:147], v179 offset:61952
v_rcp_f32 v165, v165
v_mul_f32 v166, v166, v164
v_mul_f32 v167, v167, v165
v_mfma_f32_32x32x16_bf16 v[80:95], v[128:131], a[172:175], v[80:95]
ds_read_b64_tr_b16 v[148:149], v179 offset:62464
ds_read_b64_tr_b16 v[150:151], v179 offset:62976
v_cvt_pk_bf16_f32 v173, v166, v167
s_mov_b32 s85, 32768
buffer_store_dwordx4 v[170:173], v182, s[40:43], s85 offen sc1
v_mfma_f32_32x32x16_bf16 v[112:127], v[132:135], a[172:175], v[112:127]
s_waitcnt vmcnt(17)
s_lshl_b32 s66, s57, 4
s_add_u32 s66, s66, s56
s_lshl_b32 s66, s66, 2
s_add_u32 s66, s66, s58
v_mfma_f32_32x32x16_bf16 v[96:111], v[132:135], a[168:171], v[96:111]
s_lshl_b32 s66, s66, 3
s_add_u32 s66, s66, 1024
s_waitcnt lgkmcnt(4)
v_mfma_f32_32x32x16_bf16 v[64:79], v[136:139], a[176:179], v[64:79]
s_waitcnt vmcnt(13)
s_barrier
ds_read_b64_tr_b16 v[128:129], v180 offset:32768
ds_read_b64_tr_b16 v[130:131], v180 offset:33280
s_mov_b64 exec, 1
v_mov_b32 v186, s62
v_mov_b32 v187, s63
v_mov_b32 v188, s66
buffer_store_dwordx2 v[186:187], v188, s[40:43], 0 offen sc1
s_mov_b64 exec, -1
v_mfma_f32_32x32x16_bf16 v[80:95], v[136:139], a[180:183], v[80:95]
ds_read_b64_tr_b16 v[132:133], v180 offset:33792
ds_read_b64_tr_b16 v[134:135], v180 offset:34304
v_mul_f32 v152, s76, v8
v_mfma_f32_32x32x16_bf16 v[112:127], v[140:143], a[180:183], v[112:127]
s_add_u32 s64, s84, 589824
s_add_u32 m0, s70, 81920
v_mul_f32 v153, s76, v9
v_exp_f32 v152, v152
v_exp_f32 v153, v153
v_mfma_f32_32x32x16_bf16 v[96:111], v[140:143], a[176:179], v[96:111]
buffer_load_dwordx4 v185, s[32:35], s64 offen lds
v_mul_f32 v154, v8, v40
v_mul_f32 v155, v9, v41
s_waitcnt lgkmcnt(4)
v_mfma_f32_32x32x16_bf16 v[64:79], v[144:147], a[184:187], v[64:79]
ds_read_b64_tr_b16 v[136:137], v180 offset:36864
ds_read_b64_tr_b16 v[138:139], v180 offset:37376
v_add_f32 v152, 1.0, v152
v_add_f32 v153, 1.0, v153
v_rcp_f32 v152, v152
v_mfma_f32_32x32x16_bf16 v[80:95], v[144:147], a[188:191], v[80:95]
ds_read_b64_tr_b16 v[140:141], v180 offset:37888
ds_read_b64_tr_b16 v[142:143], v180 offset:38400
v_rcp_f32 v153, v153
v_mul_f32 v154, v154, v152
v_mfma_f32_32x32x16_bf16 v[112:127], v[148:151], a[188:191], v[112:127]
buffer_load_dwordx4 v190, s[36:39], s64 offen offset:1024 lds
v_mul_f32 v155, v155, v153
v_cvt_pk_bf16_f32 v174, v154, v155
v_mul_f32 v156, s76, v10
v_mul_f32 v157, s76, v11
v_mfma_f32_32x32x16_bf16 v[96:111], v[148:151], a[184:187], v[96:111]
v_exp_f32 v156, v156
v_exp_f32 v157, v157
v_mul_f32 v158, v10, v42
s_waitcnt lgkmcnt(4)
v_mfma_f32_32x32x16_bf16 v[64:79], v[128:131], a[192:195], v[64:79]
ds_read_b64_tr_b16 v[144:145], v180 offset:40960
ds_read_b64_tr_b16 v[146:147], v180 offset:41472
v_mul_f32 v159, v11, v43
v_add_f32 v156, 1.0, v156
v_add_f32 v157, 1.0, v157
v_mfma_f32_32x32x16_bf16 v[80:95], v[128:131], a[196:199], v[80:95]
ds_read_b64_tr_b16 v[148:149], v180 offset:41984
ds_read_b64_tr_b16 v[150:151], v180 offset:42496
v_rcp_f32 v156, v156
v_rcp_f32 v157, v157
v_mfma_f32_32x32x16_bf16 v[112:127], v[132:135], a[196:199], v[112:127]
v_mul_f32 v158, v158, v156
v_mul_f32 v159, v159, v157
v_cvt_pk_bf16_f32 v175, v158, v159
v_mul_f32 v160, s76, v12
v_mul_f32 v161, s76, v13
v_exp_f32 v160, v160
v_mfma_f32_32x32x16_bf16 v[96:111], v[132:135], a[192:195], v[96:111]
s_lshl_b32 s94, s8, 6
buffer_load_dword v191, v205, s[24:27], s94 offen
v_exp_f32 v161, v161
s_waitcnt lgkmcnt(4)
v_mfma_f32_32x32x16_bf16 v[64:79], v[136:139], a[200:203], v[64:79]
ds_read_b64_tr_b16 v[128:129], v180 offset:45056
ds_read_b64_tr_b16 v[130:131], v180 offset:45568
v_mul_f32 v162, v12, v44
v_mul_f32 v163, v13, v45
v_add_f32 v160, 1.0, v160
v_mfma_f32_32x32x16_bf16 v[80:95], v[136:139], a[204:207], v[80:95]
ds_read_b64_tr_b16 v[132:133], v180 offset:46080
ds_read_b64_tr_b16 v[134:135], v180 offset:46592
v_add_f32 v161, 1.0, v161
v_rcp_f32 v160, v160
v_mfma_f32_32x32x16_bf16 v[112:127], v[140:143], a[204:207], v[112:127]
v_rcp_f32 v161, v161
v_mul_f32 v162, v162, v160
v_mul_f32 v163, v163, v161
v_cvt_pk_bf16_f32 v176, v162, v163
v_mul_f32 v164, s76, v14
v_mfma_f32_32x32x16_bf16 v[96:111], v[140:143], a[200:203], v[96:111]
v_mul_f32 v165, s76, v15
v_exp_f32 v164, v164
v_exp_f32 v165, v165
v_mul_f32 v166, v14, v46
s_waitcnt lgkmcnt(4)
v_mfma_f32_32x32x16_bf16 v[64:79], v[144:147], a[208:211], v[64:79]
s_waitcnt vmcnt(13)
s_barrier
ds_read_b64_tr_b16 v[136:137], v179
ds_read_b64_tr_b16 v[138:139], v179 offset:512
v_mul_f32 v167, v15, v47
v_add_f32 v164, 1.0, v164
v_mfma_f32_32x32x16_bf16 v[80:95], v[144:147], a[212:215], v[80:95]
ds_read_b64_tr_b16 v[140:141], v179 offset:1024
ds_read_b64_tr_b16 v[142:143], v179 offset:1536
v_add_f32 v165, 1.0, v165
v_rcp_f32 v164, v164
v_mfma_f32_32x32x16_bf16 v[112:127], v[148:151], a[212:215], v[112:127]
s_add_u32 s64, s84, 1179648
s_add_u32 m0, s70, 49152
v_rcp_f32 v165, v165
v_mul_f32 v166, v166, v164
v_mul_f32 v167, v167, v165
v_mfma_f32_32x32x16_bf16 v[96:111], v[148:151], a[208:211], v[96:111]
buffer_load_dwordx4 v185, s[32:35], s64 offen lds
v_cvt_pk_bf16_f32 v177, v166, v167
s_mov_b32 s85, 32768
s_waitcnt lgkmcnt(4)
v_mfma_f32_32x32x16_bf16 v[64:79], v[128:131], a[216:219], v[64:79]
ds_read_b64_tr_b16 v[144:145], v179 offset:4096
ds_read_b64_tr_b16 v[146:147], v179 offset:4608
buffer_store_dwordx4 v[174:177], v182, s[40:43], s85 offen offset:1024 sc1
v_mul_f32 v152, s76, v16
v_mul_f32 v153, s76, v17
v_mfma_f32_32x32x16_bf16 v[80:95], v[128:131], a[220:223], v[80:95]
ds_read_b64_tr_b16 v[148:149], v179 offset:5120
ds_read_b64_tr_b16 v[150:151], v179 offset:5632
v_exp_f32 v152, v152
v_exp_f32 v153, v153
v_mfma_f32_32x32x16_bf16 v[112:127], v[132:135], a[220:223], v[112:127]
buffer_load_dwordx4 v190, s[36:39], s64 offen offset:1024 lds
v_mul_f32 v154, v16, v48
v_mul_f32 v155, v17, v49
v_add_f32 v152, 1.0, v152
v_mfma_f32_32x32x16_bf16 v[96:111], v[132:135], a[216:219], v[96:111]
v_add_f32 v153, 1.0, v153
v_rcp_f32 v152, v152
v_rcp_f32 v153, v153
v_mul_f32 v154, v154, v152
s_waitcnt lgkmcnt(4)
v_mfma_f32_32x32x16_bf16 v[64:79], v[136:139], a[224:227], v[64:79]
ds_read_b64_tr_b16 v[128:129], v179 offset:8192
ds_read_b64_tr_b16 v[130:131], v179 offset:8704
s_lshl_b32 s94, s8, 6
s_add_u32 s94, s94, 128
v_mfma_f32_32x32x16_bf16 v[80:95], v[136:139], a[228:231], v[80:95]
ds_read_b64_tr_b16 v[132:133], v179 offset:9216
ds_read_b64_tr_b16 v[134:135], v179 offset:9728
buffer_load_dword v191, v205, s[24:27], s94 offen
v_mul_f32 v155, v155, v153
v_cvt_pk_bf16_f32 v170, v154, v155
v_mfma_f32_32x32x16_bf16 v[112:127], v[140:143], a[228:231], v[112:127]
v_mul_f32 v156, s76, v18
v_mul_f32 v157, s76, v19
v_exp_f32 v156, v156
v_exp_f32 v157, v157
v_mfma_f32_32x32x16_bf16 v[96:111], v[140:143], a[224:227], v[96:111]
v_mul_f32 v158, v18, v50
v_mul_f32 v159, v19, v51
v_add_f32 v156, 1.0, v156
v_add_f32 v157, 1.0, v157
v_rcp_f32 v156, v156
s_waitcnt lgkmcnt(4)
v_mfma_f32_32x32x16_bf16 v[64:79], v[144:147], a[232:235], v[64:79]
ds_read_b64_tr_b16 v[136:137], v179 offset:12288
ds_read_b64_tr_b16 v[138:139], v179 offset:12800
v_rcp_f32 v157, v157
v_mul_f32 v158, v158, v156
v_mfma_f32_32x32x16_bf16 v[80:95], v[144:147], a[236:239], v[80:95]
ds_read_b64_tr_b16 v[140:141], v179 offset:13312
ds_read_b64_tr_b16 v[142:143], v179 offset:13824
v_mul_f32 v159, v159, v157
v_cvt_pk_bf16_f32 v171, v158, v159
v_mul_f32 v160, s76, v20
v_mul_f32 v161, s76, v21
v_mfma_f32_32x32x16_bf16 v[112:127], v[148:151], a[236:239], v[112:127]
v_exp_f32 v160, v160
v_exp_f32 v161, v161
v_mul_f32 v162, v20, v52
v_mul_f32 v163, v21, v53
v_mfma_f32_32x32x16_bf16 v[96:111], v[148:151], a[232:235], v[96:111]
v_add_f32 v160, 1.0, v160
v_add_f32 v161, 1.0, v161
v_rcp_f32 v160, v160
v_rcp_f32 v161, v161
s_waitcnt lgkmcnt(4)
v_mfma_f32_32x32x16_bf16 v[64:79], v[128:131], a[240:243], v[64:79]
s_waitcnt vmcnt(13)
s_barrier
ds_read_b64_tr_b16 v[144:145], v179 offset:16384
ds_read_b64_tr_b16 v[146:147], v179 offset:16896
v_mul_f32 v162, v162, v160
v_mfma_f32_32x32x16_bf16 v[80:95], v[128:131], a[244:247], v[80:95]
ds_read_b64_tr_b16 v[148:149], v179 offset:17408
ds_read_b64_tr_b16 v[150:151], v179 offset:17920
v_mul_f32 v163, v163, v161
v_cvt_pk_bf16_f32 v172, v162, v163
v_mul_f32 v164, s76, v22
v_mul_f32 v165, s76, v23
v_mfma_f32_32x32x16_bf16 v[112:127], v[132:135], a[244:247], v[112:127]
s_add_u32 s64, s84, 1769472
s_add_u32 m0, s70, 65536
v_exp_f32 v164, v164
v_exp_f32 v165, v165
v_mfma_f32_32x32x16_bf16 v[96:111], v[132:135], a[240:243], v[96:111]
buffer_load_dwordx4 v185, s[32:35], s64 offen lds
v_mul_f32 v166, v22, v54
v_mul_f32 v167, v23, v55
s_waitcnt lgkmcnt(4)
v_mfma_f32_32x32x16_bf16 v[64:79], v[136:139], a[248:251], v[64:79]
ds_read_b64_tr_b16 v[128:129], v179 offset:20480
ds_read_b64_tr_b16 v[130:131], v179 offset:20992
v_add_f32 v164, 1.0, v164
v_add_f32 v165, 1.0, v165
s_mov_b32 s94, 256
v_mfma_f32_32x32x16_bf16 v[80:95], v[136:139], a[252:255], v[80:95]
ds_read_b64_tr_b16 v[132:133], v179 offset:21504
ds_read_b64_tr_b16 v[134:135], v179 offset:22016
buffer_load_dword v191, v205, s[24:27], s94 offen
v_rcp_f32 v164, v164
v_mfma_f32_32x32x16_bf16 v[112:127], v[140:143], a[252:255], v[112:127]
buffer_load_dwordx4 v190, s[36:39], s64 offen offset:1024 lds
v_rcp_f32 v165, v165
v_mul_f32 v166, v166, v164
v_mul_f32 v167, v167, v165
v_mfma_f32_32x32x16_bf16 v[96:111], v[140:143], a[248:251], v[96:111]
v_cvt_pk_bf16_f32 v173, v166, v167
s_mov_b32 s85, 32768
buffer_store_dwordx4 v[170:173], v183, s[40:43], s85 offen sc1
s_waitcnt lgkmcnt(4)
v_mfma_f32_32x32x16_bf16 v[64:79], v[144:147], v[224:227], v[64:79]
ds_read_b64_tr_b16 v[136:137], v179 offset:24576
ds_read_b64_tr_b16 v[138:139], v179 offset:25088
v_mul_f32 v152, s76, v24
v_mul_f32 v153, s76, v25
v_exp_f32 v152, v152
v_mfma_f32_32x32x16_bf16 v[80:95], v[144:147], v[228:231], v[80:95]
ds_read_b64_tr_b16 v[140:141], v179 offset:25600
ds_read_b64_tr_b16 v[142:143], v179 offset:26112
v_exp_f32 v153, v153
v_mul_f32 v154, v24, v56
v_mul_f32 v155, v25, v57
v_mfma_f32_32x32x16_bf16 v[112:127], v[148:151], v[228:231], v[112:127]
v_add_f32 v152, 1.0, v152
v_add_f32 v153, 1.0, v153
v_rcp_f32 v152, v152
v_rcp_f32 v153, v153
v_mfma_f32_32x32x16_bf16 v[96:111], v[148:151], v[224:227], v[96:111]
v_mul_f32 v154, v154, v152
v_mul_f32 v155, v155, v153
v_cvt_pk_bf16_f32 v174, v154, v155
v_mul_f32 v156, s76, v26
v_mul_f32 v157, s76, v27
s_waitcnt lgkmcnt(4)
v_mfma_f32_32x32x16_bf16 v[64:79], v[128:131], v[232:235], v[64:79]
ds_read_b64_tr_b16 v[144:145], v179 offset:28672
ds_read_b64_tr_b16 v[146:147], v179 offset:29184
v_exp_f32 v156, v156
v_exp_f32 v157, v157
v_mfma_f32_32x32x16_bf16 v[80:95], v[128:131], v[236:239], v[80:95]
ds_read_b64_tr_b16 v[148:149], v179 offset:29696
ds_read_b64_tr_b16 v[150:151], v179 offset:30208
v_mul_f32 v158, v26, v58
v_mul_f32 v159, v27, v59
v_add_f32 v156, 1.0, v156
v_mfma_f32_32x32x16_bf16 v[112:127], v[132:135], v[236:239], v[112:127]
v_add_f32 v157, 1.0, v157
v_rcp_f32 v156, v156
v_rcp_f32 v157, v157
v_mul_f32 v158, v158, v156
v_mfma_f32_32x32x16_bf16 v[96:111], v[132:135], v[232:235], v[96:111]
v_mul_f32 v159, v159, v157
v_cvt_pk_bf16_f32 v175, v158, v159
v_mul_f32 v160, s76, v28
v_mul_f32 v161, s76, v29
v_exp_f32 v160, v160
s_waitcnt lgkmcnt(4)
v_mfma_f32_32x32x16_bf16 v[64:79], v[136:139], v[240:243], v[64:79]
s_waitcnt vmcnt(13)
s_barrier
ds_read_b64_tr_b16 v[128:129], v184 offset:32768
ds_read_b64_tr_b16 v[130:131], v184 offset:33024
v_exp_f32 v161, v161
v_mfma_f32_32x32x16_bf16 v[80:95], v[136:139], v[244:247], v[80:95]
s_add_u32 s64, s84, 2359296
s_add_u32 m0, s70, 0
v_mul_f32 v162, v28, v60
v_mul_f32 v163, v29, v61
v_add_f32 v160, 1.0, v160
v_mfma_f32_32x32x16_bf16 v[112:127], v[140:143], v[244:247], v[112:127]
buffer_load_dwordx4 v185, s[32:35], s64 offen lds
v_add_f32 v161, 1.0, v161
v_rcp_f32 v160, v160
v_mfma_f32_32x32x16_bf16 v[96:111], v[140:143], v[240:243], v[96:111]
buffer_load_dwordx4 v190, s[36:39], s64 offen offset:1024 lds
v_rcp_f32 v161, v161
v_mul_f32 v162, v162, v160
v_mul_f32 v163, v163, v161
s_waitcnt lgkmcnt(2)
v_mfma_f32_32x32x16_bf16 v[64:79], v[144:147], v[248:251], v[64:79]
ds_read_b64_tr_b16 v[132:133], v184 offset:34816
ds_read_b64_tr_b16 v[134:135], v184 offset:35072
v_cvt_pk_bf16_f32 v176, v162, v163
v_mul_f32 v164, s76, v30
v_mul_f32 v165, s76, v31
v_mfma_f32_32x32x16_bf16 v[80:95], v[144:147], v[252:255], v[80:95]
v_exp_f32 v164, v164
v_exp_f32 v165, v165
v_mul_f32 v166, v30, v62
v_mul_f32 v167, v31, v63
v_mfma_f32_32x32x16_bf16 v[112:127], v[148:151], v[252:255], v[112:127]
v_add_f32 v164, 1.0, v164
v_add_f32 v165, 1.0, v165
v_rcp_f32 v164, v164
v_rcp_f32 v165, v165
v_mfma_f32_32x32x16_bf16 v[96:111], v[148:151], v[248:251], v[96:111]
v_mul_f32 v166, v166, v164
v_mul_f32 v167, v167, v165
v_cvt_pk_bf16_f32 v177, v166, v167
s_mov_b32 s85, 32768
buffer_store_dwordx4 v[174:177], v183, s[40:43], s85 offen offset:1024 sc1
; ==== half (1 iteration(s))
s_waitcnt lgkmcnt(2)
v_mfma_f32_32x32x16_bf16 v[0:15], v[128:131], a[0:3], 0
ds_read_b64_tr_b16 v[136:137], v184 offset:36864
ds_read_b64_tr_b16 v[138:139], v184 offset:37120
s_cmp_ge_u32 s23, 288
s_cselect_b32 s86, 0, s23
s_mul_i32 s86, s86, 3641
s_lshr_b32 s86, s86, 16
s_lshl_b32 s86, s86, 9
s_add_u32 s86, s86, 1024
v_lshlrev_b32 v208, 3, v178
v_mfma_f32_32x32x16_bf16 v[16:31], v[128:131], a[4:7], 0
v_add_u32 v208, s86, v208
buffer_load_dwordx2 v[206:207], v208, s[40:43], 0 offen sc1
s_cmp_ge_u32 s61, 288
s_cselect_b32 s86, 0, s61
s_mul_i32 s86, s86, 3641
s_lshr_b32 s86, s86, 16
s_waitcnt lgkmcnt(2)
v_mfma_f32_32x32x16_bf16 v[0:15], v[132:135], a[8:11], v[0:15]
ds_read_b64_tr_b16 v[140:141], v184 offset:38912
ds_read_b64_tr_b16 v[142:143], v184 offset:39168
s_lshl_b32 s86, s86, 9
s_add_u32 s86, s86, 1024
v_lshlrev_b32 v212, 3, v178
v_add_u32 v212, s86, v212
buffer_load_dwordx2 v[210:211], v212, s[40:43], 0 offen sc1
v_mfma_f32_32x32x16_bf16 v[16:31], v[132:135], a[12:15], v[16:31]
v_mul_f32 v152, s76, v64
v_mul_f32 v153, s76, v65
v_exp_f32 v152, v152
v_exp_f32 v153, v153
v_mul_f32 v154, v64, v96
v_mul_f32 v155, v65, v97
v_add_f32 v152, 1.0, v152
s_waitcnt lgkmcnt(2)
v_mfma_f32_32x32x16_bf16 v[0:15], v[136:139], a[16:19], v[0:15]
s_waitcnt vmcnt(14)
s_barrier
ds_read_b64_tr_b16 v[144:145], v222 offset:49152
ds_read_b64_tr_b16 v[146:147], v222 offset:49408
s_add_u32 s64, s84, 2949120
s_add_u32 m0, s70, 16384
v_add_f32 v153, 1.0, v153
v_rcp_f32 v152, v152
v_mfma_f32_32x32x16_bf16 v[16:31], v[136:139], a[20:23], v[16:31]
buffer_load_dwordx4 v185, s[32:35], s64 offen lds
v_rcp_f32 v153, v153
v_mul_f32 v154, v154, v152
v_mul_f32 v155, v155, v153
v_cvt_pk_bf16_f32 v170, v154, v155
s_waitcnt lgkmcnt(2)
v_mfma_f32_32x32x16_bf16 v[0:15], v[140:143], a[24:27], v[0:15]
ds_read_b64_tr_b16 v[148:149], v222 offset:51200
ds_read_b64_tr_b16 v[150:151], v222 offset:51456
buffer_load_dwordx4 v190, s[36:39], s64 offen offset:1024 lds
v_mul_f32 v156, s76, v66
v_mul_f32 v157, s76, v67
v_exp_f32 v156, v156
v_mfma_f32_32x32x16_bf16 v[16:31], v[140:143], a[28:31], v[16:31]
v_exp_f32 v157, v157
v_mul_f32 v158, v66, v98
v_mul_f32 v159, v67, v99
v_add_f32 v156, 1.0, v156
v_add_f32 v157, 1.0, v157
v_rcp_f32 v156, v156
s_waitcnt lgkmcnt(2)
v_mfma_f32_32x32x16_bf16 v[0:15], v[144:147], a[32:35], v[0:15]
ds_read_b64_tr_b16 v[128:129], v222 offset:53248
ds_read_b64_tr_b16 v[130:131], v222 offset:53504
v_rcp_f32 v157, v157
v_mul_f32 v158, v158, v156
v_mul_f32 v159, v159, v157
v_cvt_pk_bf16_f32 v171, v158, v159
v_mul_f32 v160, s76, v68
v_mul_f32 v161, s76, v69
v_mfma_f32_32x32x16_bf16 v[16:31], v[144:147], a[36:39], v[16:31]
v_exp_f32 v160, v160
v_exp_f32 v161, v161
v_mul_f32 v162, v68, v100
v_mul_f32 v163, v69, v101
v_add_f32 v160, 1.0, v160
v_add_f32 v161, 1.0, v161
s_waitcnt lgkmcnt(2)
v_mfma_f32_32x32x16_bf16 v[0:15], v[148:151], a[40:43], v[0:15]
ds_read_b64_tr_b16 v[132:133], v222 offset:55296
ds_read_b64_tr_b16 v[134:135], v222 offset:55552
v_rcp_f32 v160, v160
v_rcp_f32 v161, v161
v_mul_f32 v162, v162, v160
v_mul_f32 v163, v163, v161
v_cvt_pk_bf16_f32 v172, v162, v163
v_mfma_f32_32x32x16_bf16 v[16:31], v[148:151], a[44:47], v[16:31]
v_mul_f32 v164, s76, v70
v_mul_f32 v165, s76, v71
v_exp_f32 v164, v164
v_exp_f32 v165, v165
v_mul_f32 v166, v70, v102
v_mul_f32 v167, v71, v103
s_waitcnt lgkmcnt(2)
v_mfma_f32_32x32x16_bf16 v[0:15], v[128:131], a[48:51], v[0:15]
s_waitcnt vmcnt(12)
s_barrier
ds_read_b64_tr_b16 v[136:137], v184 offset:49152
ds_read_b64_tr_b16 v[138:139], v184 offset:49408
s_add_u32 s64, s84, 3538944
s_add_u32 m0, s70, 32768
v_add_f32 v164, 1.0, v164
v_add_f32 v165, 1.0, v165
v_rcp_f32 v164, v164
v_mfma_f32_32x32x16_bf16 v[16:31], v[128:131], a[52:55], v[16:31]
buffer_load_dwordx4 v185, s[32:35], s64 offen lds
v_rcp_f32 v165, v165
v_mul_f32 v166, v166, v164
v_mul_f32 v167, v167, v165
v_cvt_pk_bf16_f32 v173, v166, v167
s_waitcnt lgkmcnt(2)
v_mfma_f32_32x32x16_bf16 v[0:15], v[132:135], a[56:59], v[0:15]
ds_read_b64_tr_b16 v[140:141], v184 offset:51200
ds_read_b64_tr_b16 v[142:143], v184 offset:51456
buffer_load_dwordx4 v190, s[36:39], s64 offen offset:1024 lds
s_mov_b32 s85, 49152
buffer_store_dwordx4 v[170:173], v182, s[40:43], s85 offen sc1
v_mfma_f32_32x32x16_bf16 v[16:31], v[132:135], a[60:63], v[16:31]
v_mul_f32 v152, s76, v72
v_mul_f32 v153, s76, v73
v_exp_f32 v152, v152
v_lshlrev_b32 v223, 4, v178
s_lshl_b32 s94, s58, 12
v_exp_f32 v153, v153
v_mul_f32 v154, v72, v104
s_waitcnt lgkmcnt(2)
v_mfma_f32_32x32x16_bf16 v[0:15], v[136:139], a[64:67], v[0:15]
ds_read_b64_tr_b16 v[144:145], v184 offset:53248
ds_read_b64_tr_b16 v[146:147], v184 offset:53504
v_mul_f32 v155, v73, v105
v_add_f32 v152, 1.0, v152
v_add_f32 v153, 1.0, v153
buffer_load_dwordx4 a[0:3], v223, s[44:47], s94 offen
v_mfma_f32_32x32x16_bf16 v[16:31], v[136:139], a[68:71], v[16:31]
v_rcp_f32 v152, v152
v_rcp_f32 v153, v153
v_mul_f32 v154, v154, v152
v_mul_f32 v155, v155, v153
v_cvt_pk_bf16_f32 v174, v154, v155
v_mul_f32 v156, s76, v74
v_mul_f32 v157, s76, v75
s_waitcnt lgkmcnt(2)
v_mfma_f32_32x32x16_bf16 v[0:15], v[140:143], a[72:75], v[0:15]
ds_read_b64_tr_b16 v[148:149], v184 offset:55296
ds_read_b64_tr_b16 v[150:151], v184 offset:55552
buffer_load_dwordx4 a[8:11], v223, s[44:47], s94 offen offset:1024
v_exp_f32 v156, v156
v_exp_f32 v157, v157
v_mfma_f32_32x32x16_bf16 v[16:31], v[140:143], a[76:79], v[16:31]
v_mul_f32 v158, v74, v106
v_mul_f32 v159, v75, v107
v_add_f32 v156, 1.0, v156
v_add_f32 v157, 1.0, v157
buffer_load_dwordx4 a[4:7], v223, s[44:47], s94 offen offset:2048
v_rcp_f32 v156, v156
s_waitcnt lgkmcnt(2)
v_mfma_f32_32x32x16_bf16 v[0:15], v[144:147], a[80:83], v[0:15]
s_waitcnt vmcnt(14)
s_barrier
ds_read_b64_tr_b16 v[128:129], v222 offset:32768
ds_read_b64_tr_b16 v[130:131], v222 offset:33024
s_add_u32 s64, s84, 4128768
s_add_u32 m0, s70, 81920
v_rcp_f32 v157, v157
v_mul_f32 v158, v158, v156
v_mfma_f32_32x32x16_bf16 v[16:31], v[144:147], a[84:87], v[16:31]
buffer_load_dwordx4 v185, s[32:35], s64 offen lds
v_mul_f32 v159, v159, v157
v_cvt_pk_bf16_f32 v175, v158, v159
v_mul_f32 v160, s76, v76
v_mul_f32 v161, s76, v77
buffer_load_dwordx4 a[12:15], v223, s[44:47], s94 offen offset:3072
s_waitcnt lgkmcnt(2)
v_mfma_f32_32x32x16_bf16 v[0:15], v[148:151], a[88:91], v[0:15]
ds_read_b64_tr_b16 v[132:133], v222 offset:34816
ds_read_b64_tr_b16 v[134:135], v222 offset:35072
buffer_load_dwordx4 v190, s[36:39], s64 offen offset:1024 lds
v_exp_f32 v160, v160
v_mfma_f32_32x32x16_bf16 v[16:31], v[148:151], a[92:95], v[16:31]
v_exp_f32 v161, v161
v_mul_f32 v162, v76, v108
v_mul_f32 v163, v77, v109
v_add_f32 v160, 1.0, v160
v_add_f32 v161, 1.0, v161
s_mov_b32 s94, 32768
v_rcp_f32 v160, v160
s_waitcnt lgkmcnt(2)
v_mfma_f32_32x32x16_bf16 v[0:15], v[128:131], a[96:99], v[0:15]
ds_read_b64_tr_b16 v[136:137], v222 offset:36864
ds_read_b64_tr_b16 v[138:139], v222 offset:37120
v_rcp_f32 v161, v161
v_mul_f32 v162, v162, v160
v_mul_f32 v163, v163, v161
v_cvt_pk_bf16_f32 v176, v162, v163
v_mul_f32 v164, s76, v78
v_mul_f32 v165, s76, v79
v_mfma_f32_32x32x16_bf16 v[16:31], v[128:131], a[100:103], v[16:31]
buffer_load_dwordx4 a[16:19], v223, s[44:47], s94 offen
v_exp_f32 v164, v164
v_exp_f32 v165, v165
v_mul_f32 v166, v78, v110
s_waitcnt lgkmcnt(2)
v_mfma_f32_32x32x16_bf16 v[0:15], v[132:135], a[104:107], v[0:15]
ds_read_b64_tr_b16 v[140:141], v222 offset:38912
ds_read_b64_tr_b16 v[142:143], v222 offset:39168
v_mul_f32 v167, v79, v111
v_add_f32 v164, 1.0, v164
v_add_f32 v165, 1.0, v165
v_rcp_f32 v164, v164
buffer_load_dwordx4 a[20:23], v223, s[44:47], s94 offen offset:1024
v_mfma_f32_32x32x16_bf16 v[16:31], v[132:135], a[108:111], v[16:31]
v_rcp_f32 v165, v165
v_mul_f32 v166, v166, v164
v_mul_f32 v167, v167, v165
v_cvt_pk_bf16_f32 v177, v166, v167
s_mov_b32 s85, 49152
buffer_store_dwordx4 v[174:177], v182, s[40:43], s85 offen offset:1024 sc1
s_waitcnt lgkmcnt(2)
v_mfma_f32_32x32x16_bf16 v[0:15], v[136:139], a[112:115], v[0:15]
s_waitcnt vmcnt(17)
s_barrier
ds_read_b64_tr_b16 v[144:145], v184
ds_read_b64_tr_b16 v[146:147], v184 offset:256
s_add_u32 s64, s84, 4718592
s_add_u32 m0, s70, 49152
buffer_load_dwordx4 a[24:27], v223, s[44:47], s94 offen offset:2048
v_mfma_f32_32x32x16_bf16 v[16:31], v[136:139], a[116:119], v[16:31]
buffer_load_dwordx4 v185, s[32:35], s64 offen lds
v_mul_f32 v152, s76, v80
v_mul_f32 v153, s76, v81
v_exp_f32 v152, v152
v_exp_f32 v153, v153
s_waitcnt lgkmcnt(2)
v_mfma_f32_32x32x16_bf16 v[0:15], v[140:143], a[120:123], v[0:15]
ds_read_b64_tr_b16 v[148:149], v184 offset:2048
ds_read_b64_tr_b16 v[150:151], v184 offset:2304
buffer_load_dwordx4 v190, s[36:39], s64 offen offset:1024 lds
v_mul_f32 v154, v80, v112
s_waitcnt vmcnt(18)
v_cmp_eq_u64 vcc, s[62:63], v[206:207]
s_nop 3
s_cmp_eq_u64 vcc, -1
s_cbranch_scc1 L_ok3
s_mov_b32 s17, 0x20000
L_poll2:
s_sleep 1
buffer_load_dwordx2 v[206:207], v208, s[40:43], 0 offen sc1
s_waitcnt vmcnt(0)
v_cmp_eq_u64 vcc, s[62:63], v[206:207]
s_nop 4
s_cmp_eq_u64 vcc, -1
s_cbranch_scc1 L_ok3
s_sub_u32 s17, s17, 1
s_cmp_lg_u32 s17, 0
s_cbranch_scc1 L_poll2
L_ok3:
v_mfma_f32_32x32x16_bf16 v[16:31], v[140:143], a[124:127], v[16:31]
s_waitcnt vmcnt(17)
v_cmp_eq_u64 vcc, s[62:63], v[210:211]
s_nop 3
s_cmp_eq_u64 vcc, -1
s_cbranch_scc1 L_ok5
s_mov_b32 s17, 0x20000
L_poll4:
s_sleep 1
buffer_load_dwordx2 v[210:211], v212, s[40:43], 0 offen sc1
s_waitcnt vmcnt(0)
v_cmp_eq_u64 vcc, s[62:63], v[210:211]
s_nop 4
s_cmp_eq_u64 vcc, -1
s_cbranch_scc1 L_ok5
s_sub_u32 s17, s17, 1
s_cmp_lg_u32 s17, 0
s_cbranch_scc1 L_poll4
L_ok5:
v_lshlrev_b32 v205, 4, v178
s_lshl_b32 s86, s58, 11
s_add_u32 s86, s86, 45056
v_add_u32 v188, s86, v205
v_and_b32 v205, 15, v178
s_waitcnt lgkmcnt(2)
v_mfma_f32_32x32x16_bf16 v[0:15], v[144:147], a[128:131], v[0:15]
ds_read_b64_tr_b16 v[128:129], v184 offset:4096
ds_read_b64_tr_b16 v[130:131], v184 offset:4352
v_lshrrev_b32 v189, 4, v178
s_lshl_b32 s86, s58, 4
v_add_u32 v205, s86, v205
v_lshl_add_u32 v205, v189, 8, v205
v_lshlrev_b32 v205, 2, v205
v_add_u32 v189, 2412544, v205
v_mfma_f32_32x32x16_bf16 v[16:31], v[144:147], a[132:135], v[16:31]
s_lshl_b32 s86, s23, 13
buffer_load_dwordx4 v[206:209], v188, s[40:43], s86 offen sc1
buffer_load_dwordx4 v[210:213], v188, s[40:43], s86 offen offset:1024 sc1
v_mul_f32 v155, v81, v113
v_add_f32 v152, 1.0, v152
s_waitcnt lgkmcnt(2)
v_mfma_f32_32x32x16_bf16 v[0:15], v[148:151], a[136:139], v[0:15]
ds_read_b64_tr_b16 v[132:133], v184 offset:6144
ds_read_b64_tr_b16 v[134:135], v184 offset:6400
buffer_load_dwordx4 a[28:31], v223, s[44:47], s94 offen offset:3072
v_add_f32 v153, 1.0, v153
v_rcp_f32 v152, v152
v_rcp_f32 v153, v153
v_mfma_f32_32x32x16_bf16 v[16:31], v[148:151], a[140:143], v[16:31]
v_mul_f32 v154, v154, v152
s_lshl_b32 s66, s56, 4
s_add_u32 s66, s66, s57
s_lshl_b32 s66, s66, 2
s_add_u32 s66, s66, s58
s_lshl_b32 s66, s66, 3
s_add_u32 s66, s66, 17408
s_waitcnt lgkmcnt(2)
v_mfma_f32_32x32x16_bf16 v[0:15], v[128:131], a[144:147], v[0:15]
s_waitcnt vmcnt(18)
s_barrier
ds_read_b64_tr_b16 v[136:137], v184 offset:16384
ds_read_b64_tr_b16 v[138:139], v184 offset:16640
s_add_u32 s64, s4, 0
s_add_u32 m0, s72, 65536
s_add_u32 s65, s64, 9216
v_mfma_f32_32x32x16_bf16 v[16:31], v[128:131], a[148:151], v[16:31]
buffer_load_dwordx4 v192, s[24:27], s64 offen lds
s_mov_b64 exec, 1
v_mov_b32 v186, s62
v_mov_b32 v187, s63
v_mov_b32 v205, s66
buffer_store_dwordx2 v[186:187], v205, s[40:43], 0 offen sc1
s_mov_b64 exec, -1
v_mul_f32 v155, v155, v153
s_waitcnt lgkmcnt(2)
v_mfma_f32_32x32x16_bf16 v[0:15], v[132:135], a[152:155], v[0:15]
ds_read_b64_tr_b16 v[140:141], v184 offset:18432
ds_read_b64_tr_b16 v[142:143], v184 offset:18688
buffer_load_dwordx4 v192, s[12:15], s64 offen offset:1024 lds
s_add_u32 m0, s72, 67584
v_cvt_pk_bf16_f32 v170, v154, v155
s_mov_b32 s94, 36864
v_mul_f32 v156, s76, v82
v_mfma_f32_32x32x16_bf16 v[16:31], v[132:135], a[156:159], v[16:31]
buffer_load_dwordx4 v192, s[24:27], s65 offen lds
v_mul_f32 v157, s76, v83
v_exp_f32 v156, v156
v_exp_f32 v157, v157
v_mul_f32 v158, v82, v114
s_waitcnt lgkmcnt(2)
v_mfma_f32_32x32x16_bf16 v[0:15], v[136:139], a[160:163], v[0:15]
ds_read_b64_tr_b16 v[144:145], v184 offset:20480
ds_read_b64_tr_b16 v[146:147], v184 offset:20736
buffer_load_dwordx4 v192, s[12:15], s65 offen offset:1024 lds
v_mul_f32 v159, v83, v115
v_add_f32 v156, 1.0, v156
buffer_load_dwordx4 a[32:35], v223, s[44:47], s94 offen
v_mfma_f32_32x32x16_bf16 v[16:31], v[136:139], a[164:167], v[16:31]
v_add_f32 v157, 1.0, v157
v_rcp_f32 v156, v156
v_rcp_f32 v157, v157
v_mul_f32 v158, v158, v156
v_mul_f32 v159, v159, v157
v_cvt_pk_bf16_f32 v171, v158, v159
s_waitcnt lgkmcnt(2)
v_mfma_f32_32x32x16_bf16 v[0:15], v[140:143], a[168:171], v[0:15]
ds_read_b64_tr_b16 v[148:149], v184 offset:22528
ds_read_b64_tr_b16 v[150:151], v184 offset:22784
v_mul_f32 v160, s76, v84
buffer_load_dwordx4 a[36:39], v223, s[44:47], s94 offen offset:1024
v_mul_f32 v161, s76, v85
v_exp_f32 v160, v160
v_mfma_f32_32x32x16_bf16 v[16:31], v[140:143], a[172:175], v[16:31]
v_exp_f32 v161, v161
v_mul_f32 v162, v84, v116
v_mul_f32 v163, v85, v117
v_add_f32 v160, 1.0, v160
buffer_load_dwordx4 a[40:43], v223, s[44:47], s94 offen offset:2048
s_waitcnt lgkmcnt(2)
v_mfma_f32_32x32x16_bf16 v[0:15], v[144:147], a[176:179], v[0:15]
s_waitcnt vmcnt(24)
s_barrier
ds_read_b64_tr_b16 v[128:129], v184 offset:32768
ds_read_b64_tr_b16 v[130:131], v184 offset:33024
s_add_u32 s64, s4, 73728
s_add_u32 m0, s72, 0
s_add_u32 s65, s64, 9216
v_add_f32 v161, 1.0, v161
v_rcp_f32 v160, v160
v_mfma_f32_32x32x16_bf16 v[16:31], v[144:147], a[180:183], v[16:31]
buffer_load_dwordx4 v192, s[24:27], s64 offen lds
v_rcp_f32 v161, v161
v_mul_f32 v162, v162, v160
v_mul_f32 v163, v163, v161
v_cvt_pk_bf16_f32 v172, v162, v163
v_mul_f32 v164, s76, v86
s_waitcnt lgkmcnt(2)
v_mfma_f32_32x32x16_bf16 v[0:15], v[148:151], a[184:187], v[0:15]
ds_read_b64_tr_b16 v[132:133], v184 offset:34816
ds_read_b64_tr_b16 v[134:135], v184 offset:35072
buffer_load_dwordx4 v192, s[12:15], s64 offen offset:1024 lds
s_add_u32 m0, s72, 2048
buffer_load_dwordx4 a[44:47], v223, s[44:47], s94 offen offset:3072
v_mul_f32 v165, s76, v87
v_mfma_f32_32x32x16_bf16 v[16:31], v[148:151], a[188:191], v[16:31]
buffer_load_dwordx4 v192, s[24:27], s65 offen lds
v_exp_f32 v164, v164
v_exp_f32 v165, v165
v_mul_f32 v166, v86, v118
s_waitcnt lgkmcnt(2)
v_mfma_f32_32x32x16_bf16 v[0:15], v[128:131], a[192:195], v[0:15]
ds_read_b64_tr_b16 v[136:137], v184 offset:36864
ds_read_b64_tr_b16 v[138:139], v184 offset:37120
buffer_load_dwordx4 v192, s[12:15], s65 offen offset:1024 lds
s_lshl_b32 s86, s23, 13
buffer_load_dword v214, v189, s[40:43], s86 offen sc1
v_mfma_f32_32x32x16_bf16 v[16:31], v[128:131], a[196:199], v[16:31]
buffer_load_dword v215, v189, s[40:43], s86 offen offset:256 sc1
buffer_load_dword v216, v189, s[40:43], s86 offen offset:512 sc1
buffer_load_dword v217, v189, s[40:43], s86 offen offset:768 sc1
s_lshl_b32 s86, s23, 13
s_add_u32 s86, s86, 4096
s_waitcnt lgkmcnt(2)
v_mfma_f32_32x32x16_bf16 v[0:15], v[132:135], a[200:203], v[0:15]
ds_read_b64_tr_b16 v[140:141], v184 offset:38912
ds_read_b64_tr_b16 v[142:143], v184 offset:39168
buffer_load_dword v218, v189, s[40:43], s86 offen sc1
buffer_load_dword v219, v189, s[40:43], s86 offen offset:256 sc1
v_mfma_f32_32x32x16_bf16 v[16:31], v[132:135], a[204:207], v[16:31]
buffer_load_dword v220, v189, s[40:43], s86 offen offset:512 sc1
buffer_load_dword v221, v189, s[40:43], s86 offen offset:768 sc1
v_mul_f32 v167, v87, v119
v_add_f32 v164, 1.0, v164
s_mov_b32 s94, 40960
v_add_f32 v165, 1.0, v165
s_waitcnt lgkmcnt(2)
v_mfma_f32_32x32x16_bf16 v[0:15], v[136:139], a[208:211], v[0:15]
s_waitcnt vmcnt(30)
s_barrier
ds_read_b64_tr_b16 v[144:145], v222 offset:49152
ds_read_b64_tr_b16 v[146:147], v222 offset:49408
s_add_u32 s64, s4, 368640
s_add_u32 m0, s72, 16384
s_add_u32 s65, s64, 9216
v_rcp_f32 v164, v164
v_rcp_f32 v165, v165
v_mfma_f32_32x32x16_bf16 v[16:31], v[136:139], a[212:215], v[16:31]
buffer_load_dwordx4 v192, s[24:27], s64 offen lds
v_mul_f32 v166, v166, v164
v_mul_f32 v167, v167, v165
v_cvt_pk_bf16_f32 v173, v166, v167
s_mov_b32 s85, 49152
s_waitcnt lgkmcnt(2)
v_mfma_f32_32x32x16_bf16 v[0:15], v[140:143], a[216:219], v[0:15]
ds_read_b64_tr_b16 v[148:149], v222 offset:51200
ds_read_b64_tr_b16 v[150:151], v222 offset:51456
buffer_load_dwordx4 v192, s[12:15], s64 offen offset:1024 lds
s_add_u32 m0, s72, 18432
buffer_load_dwordx4 a[48:51], v223, s[44:47], s94 offen
buffer_store_dwordx4 v[170:173], v183, s[40:43], s85 offen sc1
v_mfma_f32_32x32x16_bf16 v[16:31], v[140:143], a[220:223], v[16:31]
buffer_load_dwordx4 v192, s[24:27], s65 offen lds
v_mul_f32 v152, s76, v88
v_mul_f32 v153, s76, v89
v_exp_f32 v152, v152
s_waitcnt lgkmcnt(2)
v_mfma_f32_32x32x16_bf16 v[0:15], v[144:147], a[224:227], v[0:15]
ds_read_b64_tr_b16 v[128:129], v222 offset:53248
ds_read_b64_tr_b16 v[130:131], v222 offset:53504
buffer_load_dwordx4 v192, s[12:15], s65 offen offset:1024 lds
v_exp_f32 v153, v153
v_mul_f32 v154, v88, v120
v_mul_f32 v155, v89, v121
v_mfma_f32_32x32x16_bf16 v[16:31], v[144:147], a[228:231], v[16:31]
buffer_load_dwordx4 a[52:55], v223, s[44:47], s94 offen offset:1024
v_add_f32 v152, 1.0, v152
v_add_f32 v153, 1.0, v153
v_rcp_f32 v152, v152
v_rcp_f32 v153, v153
v_mul_f32 v154, v154, v152
s_waitcnt lgkmcnt(2)
v_mfma_f32_32x32x16_bf16 v[0:15], v[148:151], a[232:235], v[0:15]
ds_read_b64_tr_b16 v[132:133], v222 offset:55296
ds_read_b64_tr_b16 v[134:135], v222 offset:55552
v_mul_f32 v155, v155, v153
buffer_load_dwordx4 a[56:59], v223, s[44:47], s94 offen offset:2048
v_cvt_pk_bf16_f32 v174, v154, v155
v_mul_f32 v156, s76, v90
v_mul_f32 v157, s76, v91
v_mfma_f32_32x32x16_bf16 v[16:31], v[148:151], a[236:239], v[16:31]
v_exp_f32 v156, v156
v_exp_f32 v157, v157
v_mul_f32 v158, v90, v122
v_mul_f32 v159, v91, v123
buffer_load_dwordx4 a[60:63], v223, s[44:47], s94 offen offset:3072
s_waitcnt lgkmcnt(2)
v_mfma_f32_32x32x16_bf16 v[0:15], v[128:131], a[240:243], v[0:15]
s_waitcnt vmcnt(33)
s_barrier
ds_read_b64_tr_b16 v[136:137], v184 offset:49152
ds_read_b64_tr_b16 v[138:139], v184 offset:49408
s_add_u32 s64, s4, 442368
s_add_u32 m0, s72, 32768
s_add_u32 s65, s64, 9216
v_add_f32 v156, 1.0, v156
v_add_f32 v157, 1.0, v157
v_mfma_f32_32x32x16_bf16 v[16:31], v[128:131], a[244:247], v[16:31]
buffer_load_dwordx4 v192, s[24:27], s64 offen lds
v_rcp_f32 v156, v156
v_rcp_f32 v157, v157
v_mul_f32 v158, v158, v156
v_mul_f32 v159, v159, v157
s_waitcnt lgkmcnt(2)
v_mfma_f32_32x32x16_bf16 v[0:15], v[132:135], a[248:251], v[0:15]
ds_read_b64_tr_b16 v[140:141], v184 offset:51200
ds_read_b64_tr_b16 v[142:143], v184 offset:51456
buffer_load_dwordx4 v192, s[12:15], s64 offen offset:1024 lds
s_add_u32 m0, s72, 34816
v_cvt_pk_bf16_f32 v175, v158, v159
v_mul_f32 v160, s76, v92
v_mul_f32 v161, s76, v93
v_mfma_f32_32x32x16_bf16 v[16:31], v[132:135], a[252:255], v[16:31]
buffer_load_dwordx4 v192, s[24:27], s65 offen lds
v_exp_f32 v160, v160
v_exp_f32 v161, v161
v_mul_f32 v162, v92, v124
v_mul_f32 v163, v93, v125
s_waitcnt lgkmcnt(2)
v_mfma_f32_32x32x16_bf16 v[0:15], v[136:139], v[224:227], v[0:15]
ds_read_b64_tr_b16 v[144:145], v184 offset:53248
ds_read_b64_tr_b16 v[146:147], v184 offset:53504
buffer_load_dwordx4 v192, s[12:15], s65 offen offset:1024 lds
v_add_f32 v160, 1.0, v160
v_add_f32 v161, 1.0, v161
v_rcp_f32 v160, v160
v_mfma_f32_32x32x16_bf16 v[16:31], v[136:139], v[228:231], v[16:31]
v_rcp_f32 v161, v161
v_mul_f32 v162, v162, v160
v_mul_f32 v163, v163, v161
v_cvt_pk_bf16_f32 v176, v162, v163
v_mul_f32 v164, s76, v94
v_mul_f32 v165, s76, v95
v_exp_f32 v164, v164
s_waitcnt lgkmcnt(2)
v_mfma_f32_32x32x16_bf16 v[0:15], v[140:143], v[232:235], v[0:15]
ds_read_b64_tr_b16 v[148:149], v184 offset:55296
ds_read_b64_tr_b16 v[150:151], v184 offset:55552
v_exp_f32 v165, v165
v_mul_f32 v166, v94, v126
v_mul_f32 v167, v95, v127
v_add_f32 v164, 1.0, v164
v_add_f32 v165, 1.0, v165
v_mfma_f32_32x32x16_bf16 v[16:31], v[140:143], v[236:239], v[16:31]
v_rcp_f32 v164, v164
s_lshl_b32 s86, s56, 2
s_add_u32 s86, s86, s82
s_lshl_b32 s86, s86, 7
s_add_u32 s86, s86, 17408
v_and_b32 v223, 15, v178
v_lshlrev_b32 v223, 3, v223
s_waitcnt lgkmcnt(2)
v_mfma_f32_32x32x16_bf16 v[0:15], v[144:147], v[240:243], v[0:15]
s_waitcnt vmcnt(29)
s_barrier
ds_read_b64_tr_b16 v[128:129], v196 offset:32768
ds_read_b64_tr_b16 v[130:131], v196 offset:34816
s_add_u32 s64, s4, 147456
s_add_u32 m0, s72, 81920
s_add_u32 s65, s64, 9216
v_add_u32 v223, s86, v223
buffer_load_dwordx2 v[186:187], v223, s[40:43], 0 offen sc1
v_mfma_f32_32x32x16_bf16 v[16:31], v[144:147], v[244:247], v[16:31]
ds_read_b64_tr_b16 v[132:133], v197 offset:32768
ds_read_b64_tr_b16 v[134:135], v197 offset:34816
buffer_load_dwordx4 v192, s[24:27], s64 offen lds
v_rcp_f32 v165, v165
v_mul_f32 v166, v166, v164
s_waitcnt lgkmcnt(4)
v_mfma_f32_32x32x16_bf16 v[0:15], v[148:151], v[248:251], v[0:15]
ds_read_b64_tr_b16 v[136:137], v196 offset:36864
ds_read_b64_tr_b16 v[138:139], v196 offset:38912
buffer_load_dwordx4 v192, s[12:15], s64 offen offset:1024 lds
s_add_u32 m0, s72, 83968
v_mul_f32 v167, v167, v165
v_cvt_pk_bf16_f32 v177, v166, v167
s_mov_b32 s85, 49152
v_mfma_f32_32x32x16_bf16 v[16:31], v[148:151], v[252:255], v[16:31]
ds_read_b64_tr_b16 v[140:141], v197 offset:36864
ds_read_b64_tr_b16 v[142:143], v197 offset:38912
buffer_load_dwordx4 v192, s[24:27], s65 offen lds
buffer_load_dwordx4 v192, s[12:15], s65 offen offset:1024 lds
buffer_store_dwordx4 v[174:177], v183, s[40:43], s85 offen offset:1024 sc1
; ==== s2pre (1 iteration(s))
v_lshlrev_b32 v166, 4, v178
s_lshl_b32 s66, s58, 12
s_add_u32 s66, s66, 16384
buffer_load_dwordx4 v[80:83], v166, s[44:47], s66 offen
buffer_load_dwordx4 v[84:87], v166, s[44:47], s66 offen offset:1024
buffer_load_dwordx4 v[88:91], v166, s[44:47], s66 offen offset:2048
buffer_load_dwordx4 v[92:95], v166, s[44:47], s66 offen offset:3072
s_mul_i32 s69, s60, 7168
s_sub_u32 s69, 65536, s69
v_mul_f32 v152, s76, v0
v_mul_f32 v153, s76, v1
v_exp_f32 v152, v152
v_exp_f32 v153, v153
v_mul_f32 v154, v0, v8
v_mul_f32 v155, v1, v9
v_add_f32 v152, 1.0, v152
v_add_f32 v153, 1.0, v153
v_rcp_f32 v152, v152
v_rcp_f32 v153, v153
v_mul_f32 v154, v154, v152
v_mul_f32 v155, v155, v153
v_cvt_pk_bf16_f32 v170, v154, v155
v_mul_f32 v156, s76, v2
v_mul_f32 v157, s76, v3
v_exp_f32 v156, v156
v_exp_f32 v157, v157
v_mul_f32 v158, v2, v10
v_mul_f32 v159, v3, v11
v_add_f32 v156, 1.0, v156
v_add_f32 v157, 1.0, v157
v_rcp_f32 v156, v156
v_rcp_f32 v157, v157
v_mul_f32 v158, v158, v156
v_mul_f32 v159, v159, v157
v_cvt_pk_bf16_f32 v171, v158, v159
v_mul_f32 v160, s76, v4
v_mul_f32 v161, s76, v5
v_exp_f32 v160, v160
v_exp_f32 v161, v161
v_mul_f32 v162, v4, v12
v_mul_f32 v163, v5, v13
v_add_f32 v160, 1.0, v160
v_add_f32 v161, 1.0, v161
v_rcp_f32 v160, v160
v_rcp_f32 v161, v161
v_mul_f32 v162, v162, v160
v_mul_f32 v163, v163, v161
v_cvt_pk_bf16_f32 v172, v162, v163
v_mul_f32 v164, s76, v6
v_mul_f32 v165, s76, v7
v_exp_f32 v164, v164
v_exp_f32 v165, v165
v_mul_f32 v166, v6, v14
v_mul_f32 v167, v7, v15
v_add_f32 v164, 1.0, v164
v_add_f32 v165, 1.0, v165
v_rcp_f32 v164, v164
v_rcp_f32 v165, v165
v_mul_f32 v166, v166, v164
v_mul_f32 v167, v167, v165
v_cvt_pk_bf16_f32 v173, v166, v167
buffer_store_dwordx4 v[170:173], v182, s[40:43], s69 offen sc1
v_mul_f32 v152, s76, v16
v_mul_f32 v153, s76, v17
v_exp_f32 v152, v152
v_exp_f32 v153, v153
v_mul_f32 v154, v16, v24
v_mul_f32 v155, v17, v25
v_add_f32 v152, 1.0, v152
v_add_f32 v153, 1.0, v153
v_rcp_f32 v152, v152
v_rcp_f32 v153, v153
v_mul_f32 v154, v154, v152
v_mul_f32 v155, v155, v153
v_cvt_pk_bf16_f32 v174, v154, v155
v_mul_f32 v156, s76, v18
v_mul_f32 v157, s76, v19
v_exp_f32 v156, v156
v_exp_f32 v157, v157
v_mul_f32 v158, v18, v26
v_mul_f32 v159, v19, v27
v_add_f32 v156, 1.0, v156
v_add_f32 v157, 1.0, v157
v_rcp_f32 v156, v156
v_rcp_f32 v157, v157
v_mul_f32 v158, v158, v156
v_mul_f32 v159, v159, v157
v_cvt_pk_bf16_f32 v175, v158, v159
v_mul_f32 v160, s76, v20
v_mul_f32 v161, s76, v21
v_exp_f32 v160, v160
v_exp_f32 v161, v161
v_mul_f32 v162, v20, v28
v_mul_f32 v163, v21, v29
v_add_f32 v160, 1.0, v160
v_add_f32 v161, 1.0, v161
v_rcp_f32 v160, v160
v_rcp_f32 v161, v161
v_mul_f32 v162, v162, v160
v_mul_f32 v163, v163, v161
v_cvt_pk_bf16_f32 v176, v162, v163
v_mul_f32 v164, s76, v22
v_mul_f32 v165, s76, v23
v_exp_f32 v164, v164
v_exp_f32 v165, v165
v_mul_f32 v166, v22, v30
v_mul_f32 v167, v23, v31
v_add_f32 v164, 1.0, v164
v_add_f32 v165, 1.0, v165
v_rcp_f32 v164, v164
v_rcp_f32 v165, v165
v_mul_f32 v166, v166, v164
v_mul_f32 v167, v167, v165
v_cvt_pk_bf16_f32 v177, v166, v167
buffer_store_dwordx4 v[174:177], v183, s[40:43], s69 offen sc1
v_lshrrev_b32 v160, 3, v178
v_and_b32 v161, 7, v178
v_lshrrev_b32 v162, 1, v160
v_and_b32 v162, 1, v162
v_lshrrev_b32 v163, 1, v161
v_xor_b32 v162, v162, v163
v_lshlrev_b32 v162, 5, v162
v_and_b32 v163, 1, v161
v_lshl_add_u32 v162, v163, 4, v162
v_lshl_add_u32 v162, v160, 7, v162
s_lshl_b32 s66, s58, 11
s_add_u32 s66, s66, 98304
v_add_u32 v170, s66, v162
v_lshrrev_b32 v164, 4, v178
v_and_b32 v165, 15, v178
v_lshrrev_b32 v160, 2, v165
v_lshl_add_u32 v160, v164, 3, v160
v_lshrrev_b32 v161, 3, v165
v_and_b32 v162, 1, v164
v_lshl_or_b32 v161, v162, 1, v161
v_and_b32 v162, 3, v165
v_lshlrev_b32 v162, 3, v162
v_lshl_add_u32 v162, v161, 5, v162
v_lshl_add_u32 v162, v160, 7, v162
v_add_u32 v162, 98304, v162
v_xor_b32 v171, 0, v162
v_xor_b32 v172, 32, v162
v_xor_b32 v173, 64, v162
v_xor_b32 v174, 96, v162
v_lshrrev_b32 v161, 1, v165
v_and_b32 v161, 1, v161
v_lshrrev_b32 v163, 2, v165
v_and_b32 v163, 2, v163
v_or_b32 v161, v161, v163
v_and_b32 v163, 1, v165
v_lshl_or_b32 v161, v163, 2, v161
v_and_b32 v163, 4, v165
v_lshl_or_b32 v161, v163, 1, v161
v_xor_b32 v161, v161, v164
v_lshlrev_b32 v161, 3, v161
v_lshl_add_u32 v161, v165, 7, v161
s_lshl_b32 s66, s58, 11
s_add_u32 s66, s66, 106496
v_add_u32 v175, s66, v161
v_lshrrev_b32 v160, 2, v165
v_lshl_add_u32 v160, v164, 3, v160
v_lshrrev_b32 v161, 1, v160
v_and_b32 v161, 1, v161
v_lshrrev_b32 v163, 2, v160
v_and_b32 v163, 2, v163
v_or_b32 v161, v161, v163
v_and_b32 v163, 1, v160
v_lshl_or_b32 v161, v163, 2, v161
v_and_b32 v163, 4, v160
v_lshl_or_b32 v161, v163, 1, v161
v_and_b32 v162, 3, v165
v_lshlrev_b32 v162, 2, v162
v_xor_b32 v161, v161, v162
v_lshlrev_b32 v161, 3, v161
v_lshl_add_u32 v161, v160, 7, v161
v_add_u32 v176, 106496, v161
v_lshlrev_b32 v160, 6, v165
v_lshl_add_u32 v160, v164, 4, v160
s_lshl_b32 s66, s58, 10
s_add_u32 s66, s66, 4780032
v_add_u32 v169, s66, v160
s_waitcnt vmcnt(11)
v_cmp_eq_u64 vcc, s[62:63], v[186:187]
s_nop 3
s_cmp_eq_u64 vcc, -1
s_cbranch_scc1 L_ok7
s_mov_b32 s17, 0x20000
L_poll6:
s_sleep 1
buffer_load_dwordx2 v[186:187], v223, s[40:43], 0 offen sc1
s_waitcnt vmcnt(0)
v_cmp_eq_u64 vcc, s[62:63], v[186:187]
s_nop 4
s_cmp_eq_u64 vcc, -1
s_cbranch_scc1 L_ok7
s_sub_u32 s17, s17, 1
s_cmp_lg_u32 s17, 0
s_cbranch_scc1 L_poll6
L_ok7:
; ==== s2 (1 iteration(s))
v_xor_b32 v160, 64, v170
ds_write_b128 v170, v[206:209]
ds_write_b128 v160, v[210:213] offset:1024
s_waitcnt lgkmcnt(0)
s_barrier
ds_read_b64_tr_b16 v[56:57], v171
ds_read_b64_tr_b16 v[58:59], v171 offset:512
ds_read_b64_tr_b16 v[60:61], v171 offset:4096
ds_read_b64_tr_b16 v[62:63], v171 offset:4608
ds_read_b64_tr_b16 v[152:153], v172
ds_read_b64_tr_b16 v[154:155], v172 offset:512
s_waitcnt lgkmcnt(4)
v_mfma_f32_16x16x32_bf16 v[0:3], v[56:59], a[0:3], 0
s_mov_b32 s94, 0
buffer_load_dwordx4 a[64:67], v200, s[40:43], s94 offen sc1
v_mfma_f32_16x16x32_bf16 v[16:19], v[56:59], a[8:11], 0
ds_read_b64_tr_b16 v[156:157], v172 offset:4096
ds_read_b64_tr_b16 v[158:159], v172 offset:4608
s_waitcnt lgkmcnt(4)
v_mfma_f32_16x16x32_bf16 v[0:3], v[60:63], a[4:7], v[0:3]
buffer_load_dwordx4 a[68:71], v201, s[40:43], s94 offen sc1
v_mfma_f32_16x16x32_bf16 v[16:19], v[60:63], a[12:15], v[16:19]
ds_read_b64_tr_b16 v[56:57], v173
ds_read_b64_tr_b16 v[58:59], v173 offset:512
s_waitcnt lgkmcnt(4)
v_mfma_f32_16x16x32_bf16 v[4:7], v[152:155], a[0:3], 0
buffer_load_dwordx4 a[72:75], v200, s[40:43], s94 offen offset:1024 sc1
v_mfma_f32_16x16x32_bf16 v[20:23], v[152:155], a[8:11], 0
ds_read_b64_tr_b16 v[60:61], v173 offset:4096
ds_read_b64_tr_b16 v[62:63], v173 offset:4608
s_waitcnt lgkmcnt(4)
v_mfma_f32_16x16x32_bf16 v[4:7], v[156:159], a[4:7], v[4:7]
buffer_load_dwordx4 a[76:79], v201, s[40:43], s94 offen offset:1024 sc1
v_mfma_f32_16x16x32_bf16 v[20:23], v[156:159], a[12:15], v[20:23]
ds_read_b64_tr_b16 v[152:153], v174
ds_read_b64_tr_b16 v[154:155], v174 offset:512
s_mov_b32 s94, 8192
s_waitcnt lgkmcnt(4)
v_mfma_f32_16x16x32_bf16 v[8:11], v[56:59], a[0:3], 0
buffer_load_dwordx4 a[80:83], v200, s[40:43], s94 offen sc1
v_mfma_f32_16x16x32_bf16 v[24:27], v[56:59], a[8:11], 0
ds_read_b64_tr_b16 v[156:157], v174 offset:4096
ds_read_b64_tr_b16 v[158:159], v174 offset:4608
s_waitcnt lgkmcnt(4)
v_mfma_f32_16x16x32_bf16 v[8:11], v[60:63], a[4:7], v[8:11]
buffer_load_dwordx4 a[84:87], v201, s[40:43], s94 offen sc1
v_mfma_f32_16x16x32_bf16 v[24:27], v[60:63], a[12:15], v[24:27]
buffer_load_dwordx4 a[88:91], v200, s[40:43], s94 offen offset:1024 sc1
s_waitcnt lgkmcnt(2)
v_mfma_f32_16x16x32_bf16 v[12:15], v[152:155], a[0:3], 0
buffer_load_dwordx4 a[92:95], v201, s[40:43], s94 offen offset:1024 sc1
v_mfma_f32_16x16x32_bf16 v[28:31], v[152:155], a[8:11], 0
s_mov_b32 s94, 16384
buffer_load_dwordx4 a[96:99], v200, s[40:43], s94 offen sc1
s_waitcnt lgkmcnt(0)
v_mfma_f32_16x16x32_bf16 v[12:15], v[156:159], a[4:7], v[12:15]
buffer_load_dwordx4 a[100:103], v201, s[40:43], s94 offen sc1
v_mfma_f32_16x16x32_bf16 v[28:31], v[156:159], a[12:15], v[28:31]
s_cmp_le_u32 s61, 288
s_cselect_b32 s68, s61, 258048
s_lshl_b32 s69, s68, 13
buffer_load_dwordx4 v[206:209], v188, s[40:43], s69 offen sc1
buffer_load_dwordx4 v[210:213], v188, s[40:43], s69 offen offset:1024 sc1
v_add_f32 v160, v0, v17
v_sub_f32 v161, v1, v16
s_waitcnt vmcnt(17)
v_mul_f32 v164, v160, v80
v_mul_f32 v166, v161, v80
v_fmac_f32 v164, v161, v81
v_fma_f32 v166, -v160, v81, v166
v_add_f32 v162, v2, v19
v_sub_f32 v163, v3, v18
v_mul_f32 v165, v162, v82
v_mul_f32 v167, v163, v82
v_fmac_f32 v165, v163, v83
v_fma_f32 v167, -v162, v83, v167
v_cvt_pk_bf16_f32 v120, v164, v165
v_cvt_pk_bf16_f32 v124, v166, v167
v_add_f32 v160, v4, v21
v_sub_f32 v161, v5, v20
s_waitcnt vmcnt(16)
v_mul_f32 v164, v160, v84
v_mul_f32 v166, v161, v84
v_fmac_f32 v164, v161, v85
v_fma_f32 v166, -v160, v85, v166
v_add_f32 v162, v6, v23
v_sub_f32 v163, v7, v22
v_mul_f32 v165, v162, v86
v_mul_f32 v167, v163, v86
v_fmac_f32 v165, v163, v87
v_fma_f32 v167, -v162, v87, v167
v_cvt_pk_bf16_f32 v121, v164, v165
v_cvt_pk_bf16_f32 v125, v166, v167
v_add_f32 v160, v8, v25
v_sub_f32 v161, v9, v24
s_waitcnt vmcnt(15)
v_mul_f32 v164, v160, v88
v_mul_f32 v166, v161, v88
v_fmac_f32 v164, v161, v89
v_fma_f32 v166, -v160, v89, v166
v_add_f32 v162, v10, v27
v_sub_f32 v163, v11, v26
v_mul_f32 v165, v162, v90
v_mul_f32 v167, v163, v90
v_fmac_f32 v165, v163, v91
v_fma_f32 v167, -v162, v91, v167
v_cvt_pk_bf16_f32 v122, v164, v165
v_cvt_pk_bf16_f32 v126, v166, v167
v_add_f32 v160, v12, v29
v_sub_f32 v161, v13, v28
s_waitcnt vmcnt(14)
v_mul_f32 v164, v160, v92
v_mul_f32 v166, v161, v92
v_fmac_f32 v164, v161, v93
v_fma_f32 v166, -v160, v93, v166
v_add_f32 v162, v14, v31
v_sub_f32 v163, v15, v30
v_mul_f32 v165, v162, v94
v_mul_f32 v167, v163, v94
v_fmac_f32 v165, v163, v95
v_fma_f32 v167, -v162, v95, v167
v_cvt_pk_bf16_f32 v123, v164, v165
v_cvt_pk_bf16_f32 v127, v166, v167
s_nop 0
v_mfma_f32_16x16x32_bf16 v[0:3], a[16:19], v[120:123], 0
buffer_load_dwordx4 a[104:107], v200, s[40:43], s94 offen offset:1024 sc1
v_mfma_f32_16x16x32_bf16 v[16:19], a[16:19], v[124:127], 0
buffer_load_dwordx4 a[108:111], v201, s[40:43], s94 offen offset:1024 sc1
v_mfma_f32_16x16x32_bf16 v[4:7], a[28:31], v[120:123], 0
s_mov_b32 s94, 24576
v_mfma_f32_16x16x32_bf16 v[20:23], a[28:31], v[124:127], 0
buffer_load_dwordx4 a[112:115], v200, s[40:43], s94 offen sc1
v_mfma_f32_16x16x32_bf16 v[0:3], a[20:23], v[124:127], v[0:3]
buffer_load_dwordx4 a[116:119], v201, s[40:43], s94 offen sc1
v_mfma_f32_16x16x32_bf16 v[16:19], a[24:27], v[120:123], v[16:19]
buffer_load_dwordx4 a[120:123], v200, s[40:43], s94 offen offset:1024 sc1
v_mfma_f32_16x16x32_bf16 v[4:7], a[32:35], v[124:127], v[4:7]
buffer_load_dwordx4 a[124:127], v201, s[40:43], s94 offen offset:1024 sc1
s_mov_b32 s94, 73728
v_mfma_f32_16x16x32_bf16 v[20:23], a[36:39], v[120:123], v[20:23]
s_waitcnt vmcnt(18)
s_lshl_b32 s66, s56, 4
s_add_u32 s66, s66, s57
s_lshl_b32 s66, s66, 2
s_add_u32 s66, s66, s58
s_lshl_b32 s66, s66, 3
s_add_u32 s66, s66, 25600
s_mov_b64 exec, 1
v_mov_b32 v160, s62
v_mov_b32 v161, s63
v_mov_b32 v162, s66
buffer_store_dwordx2 v[160:161], v162, s[40:43], 0 offen sc1
s_mov_b64 exec, -1
v_lshlrev_b32 v160, 16, v214
v_and_b32 v161, 4294901760, v214
v_mul_f32 v164, v0, v160
v_mul_f32 v166, v0, v161
v_fma_f32 v164, -v16, v161, v164
v_fmac_f32 v166, v16, v160
v_lshlrev_b32 v160, 16, v215
v_and_b32 v161, 4294901760, v215
v_mul_f32 v165, v1, v160
v_mul_f32 v167, v1, v161
v_fma_f32 v165, -v17, v161, v165
v_fmac_f32 v167, v17, v160
v_cvt_pk_bf16_f32 v120, v164, v165
v_cvt_pk_bf16_f32 v124, v166, v167
v_lshlrev_b32 v160, 16, v216
v_and_b32 v161, 4294901760, v216
v_mul_f32 v164, v2, v160
v_mul_f32 v166, v2, v161
v_fma_f32 v164, -v18, v161, v164
v_fmac_f32 v166, v18, v160
v_lshlrev_b32 v160, 16, v217
v_and_b32 v161, 4294901760, v217
v_mul_f32 v165, v3, v160
v_mul_f32 v167, v3, v161
v_fma_f32 v165, -v19, v161, v165
v_fmac_f32 v167, v19, v160
v_cvt_pk_bf16_f32 v121, v164, v165
v_cvt_pk_bf16_f32 v125, v166, v167
v_lshlrev_b32 v160, 16, v218
v_and_b32 v161, 4294901760, v218
v_mul_f32 v164, v4, v160
v_mul_f32 v166, v4, v161
v_fma_f32 v164, -v20, v161, v164
v_fmac_f32 v166, v20, v160
v_lshlrev_b32 v160, 16, v219
v_and_b32 v161, 4294901760, v219
v_mul_f32 v165, v5, v160
v_mul_f32 v167, v5, v161
v_fma_f32 v165, -v21, v161, v165
v_fmac_f32 v167, v21, v160
v_cvt_pk_bf16_f32 v122, v164, v165
v_cvt_pk_bf16_f32 v126, v166, v167
v_lshlrev_b32 v160, 16, v220
v_and_b32 v161, 4294901760, v220
v_mul_f32 v164, v6, v160
v_mul_f32 v166, v6, v161
v_fma_f32 v164, -v22, v161, v164
v_fmac_f32 v166, v22, v160
v_lshlrev_b32 v160, 16, v221
v_and_b32 v161, 4294901760, v221
v_mul_f32 v165, v7, v160
v_mul_f32 v167, v7, v161
v_fma_f32 v165, -v23, v161, v165
v_fmac_f32 v167, v23, v160
v_cvt_pk_bf16_f32 v123, v164, v165
v_cvt_pk_bf16_f32 v127, v166, v167
s_nop 0
v_mfma_f32_16x16x32_bf16 v[8:11], a[40:43], v[120:123], 0
buffer_load_dwordx4 a[128:131], v200, s[40:43], s94 offen sc1
v_mfma_f32_16x16x32_bf16 v[24:27], a[40:43], v[124:127], 0
buffer_load_dwordx4 a[132:135], v201, s[40:43], s94 offen sc1
v_mfma_f32_16x16x32_bf16 v[12:15], a[52:55], v[120:123], 0
buffer_load_dwordx4 a[136:139], v200, s[40:43], s94 offen offset:1024 sc1
v_mfma_f32_16x16x32_bf16 v[28:31], a[52:55], v[124:127], 0
buffer_load_dwordx4 a[140:143], v201, s[40:43], s94 offen offset:1024 sc1
v_mfma_f32_16x16x32_bf16 v[8:11], a[48:51], v[124:127], v[8:11]
s_mov_b32 s94, 81920
buffer_load_dwordx4 a[144:147], v200, s[40:43], s94 offen sc1
v_mfma_f32_16x16x32_bf16 v[24:27], a[44:47], v[120:123], v[24:27]
buffer_load_dwordx4 a[148:151], v201, s[40:43], s94 offen sc1
v_mfma_f32_16x16x32_bf16 v[12:15], a[60:63], v[124:127], v[12:15]
buffer_load_dwordx4 a[152:155], v200, s[40:43], s94 offen offset:1024 sc1
v_mfma_f32_16x16x32_bf16 v[28:31], a[56:59], v[120:123], v[28:31]
s_lshl_b32 s69, s68, 13
buffer_load_dword v214, v189, s[40:43], s69 offen sc1
buffer_load_dword v215, v189, s[40:43], s69 offen offset:256 sc1
buffer_load_dword v216, v189, s[40:43], s69 offen offset:512 sc1
buffer_load_dword v217, v189, s[40:43], s69 offen offset:768 sc1
s_lshl_b32 s69, s68, 13
s_add_u32 s69, s69, 4096
buffer_load_dword v218, v189, s[40:43], s69 offen sc1
buffer_load_dword v219, v189, s[40:43], s69 offen offset:256 sc1
buffer_load_dword v220, v189, s[40:43], s69 offen offset:512 sc1
buffer_load_dword v221, v189, s[40:43], s69 offen offset:768 sc1
v_mul_f32 v160, v8, v80
v_mul_f32 v161, v24, v80
v_fma_f32 v160, -v24, v81, v160
v_fmac_f32 v161, v8, v81
v_cvt_pk_bf16_f32 v120, v160, v161
v_mul_f32 v162, v9, v82
v_mul_f32 v163, v25, v82
v_fma_f32 v162, -v25, v83, v162
v_fmac_f32 v163, v9, v83
v_cvt_pk_bf16_f32 v121, v162, v163
v_xor_b32 v168, 0, v175
ds_write_b64 v168, v[120:121]
v_mul_f32 v160, v10, v84
v_mul_f32 v161, v26, v84
v_fma_f32 v160, -v26, v85, v160
v_fmac_f32 v161, v10, v85
v_cvt_pk_bf16_f32 v122, v160, v161
v_mul_f32 v162, v11, v86
v_mul_f32 v163, v27, v86
v_fma_f32 v162, -v27, v87, v162
v_fmac_f32 v163, v11, v87
v_cvt_pk_bf16_f32 v123, v162, v163
v_xor_b32 v168, 32, v175
ds_write_b64 v168, v[122:123]
v_mul_f32 v160, v12, v88
v_mul_f32 v161, v28, v88
v_fma_f32 v160, -v28, v89, v160
v_fmac_f32 v161, v12, v89
v_cvt_pk_bf16_f32 v120, v160, v161
v_mul_f32 v162, v13, v90
v_mul_f32 v163, v29, v90
v_fma_f32 v162, -v29, v91, v162
v_fmac_f32 v163, v13, v91
v_cvt_pk_bf16_f32 v121, v162, v163
v_xor_b32 v168, 64, v175
ds_write_b64 v168, v[120:121]
v_mul_f32 v160, v14, v92
v_mul_f32 v161, v30, v92
v_fma_f32 v160, -v30, v93, v160
v_fmac_f32 v161, v14, v93
v_cvt_pk_bf16_f32 v122, v160, v161
v_mul_f32 v162, v15, v94
v_mul_f32 v163, v31, v94
v_fma_f32 v162, -v31, v95, v162
v_fmac_f32 v163, v15, v95
v_cvt_pk_bf16_f32 v123, v162, v163
v_xor_b32 v168, 96, v175
ds_write_b64 v168, v[122:123]
s_waitcnt lgkmcnt(0)
s_barrier
s_lshl_b32 s86, s56, 2
s_add_u32 s86, s86, s82
s_lshl_b32 s86, s86, 7
s_add_u32 s86, s86, 25600
v_and_b32 v205, 15, v178
v_lshlrev_b32 v205, 3, v205
v_add_u32 v205, s86, v205
buffer_load_dwordx2 v[222:223], v205, s[40:43], 0 offen sc1
v_xor_b32 v160, 0, v176
v_xor_b32 v161, 64, v176
ds_read_b64_tr_b16 v[56:57], v160
ds_read_b64_tr_b16 v[58:59], v161 offset:512
v_xor_b32 v162, 0, v176
v_xor_b32 v163, 64, v176
ds_read_b64_tr_b16 v[60:61], v162 offset:4096
ds_read_b64_tr_b16 v[62:63], v163 offset:4608
v_xor_b32 v160, 8, v176
v_xor_b32 v161, 72, v176
ds_read_b64_tr_b16 v[152:153], v160
ds_read_b64_tr_b16 v[154:155], v161 offset:512
s_waitcnt lgkmcnt(4)
v_mfma_f32_16x16x32_bf16 v[0:3], v[56:59], a[0:3], 0
buffer_load_dwordx4 a[156:159], v201, s[40:43], s94 offen offset:1024 sc1
v_mfma_f32_16x16x32_bf16 v[16:19], v[56:59], a[8:11], 0
v_xor_b32 v162, 8, v176
v_xor_b32 v163, 72, v176
ds_read_b64_tr_b16 v[156:157], v162 offset:4096
ds_read_b64_tr_b16 v[158:159], v163 offset:4608
s_waitcnt lgkmcnt(4)
v_mfma_f32_16x16x32_bf16 v[0:3], v[60:63], a[4:7], v[0:3]
s_mov_b32 s94, 90112
buffer_load_dwordx4 a[160:163], v200, s[40:43], s94 offen sc1
v_mfma_f32_16x16x32_bf16 v[16:19], v[60:63], a[12:15], v[16:19]
v_xor_b32 v160, 16, v176
v_xor_b32 v161, 80, v176
ds_read_b64_tr_b16 v[56:57], v160
ds_read_b64_tr_b16 v[58:59], v161 offset:512
s_waitcnt lgkmcnt(4)
v_mfma_f32_16x16x32_bf16 v[4:7], v[152:155], a[0:3], 0
buffer_load_dwordx4 a[164:167], v201, s[40:43], s94 offen sc1
v_mfma_f32_16x16x32_bf16 v[20:23], v[152:155], a[8:11], 0
v_xor_b32 v162, 16, v176
v_xor_b32 v163, 80, v176
ds_read_b64_tr_b16 v[60:61], v162 offset:4096
ds_read_b64_tr_b16 v[62:63], v163 offset:4608
s_waitcnt lgkmcnt(4)
v_mfma_f32_16x16x32_bf16 v[4:7], v[156:159], a[4:7], v[4:7]
buffer_load_dwordx4 a[168:171], v200, s[40:43], s94 offen offset:1024 sc1
v_mfma_f32_16x16x32_bf16 v[20:23], v[156:159], a[12:15], v[20:23]
v_xor_b32 v160, 24, v176
v_xor_b32 v161, 88, v176
ds_read_b64_tr_b16 v[152:153], v160
ds_read_b64_tr_b16 v[154:155], v161 offset:512
s_waitcnt lgkmcnt(4)
v_mfma_f32_16x16x32_bf16 v[8:11], v[56:59], a[0:3], 0
buffer_load_dwordx4 a[172:175], v201, s[40:43], s94 offen offset:1024 sc1
s_mov_b32 s94, 98304
v_mfma_f32_16x16x32_bf16 v[24:27], v[56:59], a[8:11], 0
v_xor_b32 v162, 24, v176
v_xor_b32 v163, 88, v176
ds_read_b64_tr_b16 v[156:157], v162 offset:4096
ds_read_b64_tr_b16 v[158:159], v163 offset:4608
s_waitcnt lgkmcnt(4)
v_mfma_f32_16x16x32_bf16 v[8:11], v[60:63], a[4:7], v[8:11]
buffer_load_dwordx4 a[176:179], v200, s[40:43], s94 offen sc1
v_mfma_f32_16x16x32_bf16 v[24:27], v[60:63], a[12:15], v[24:27]
buffer_load_dwordx4 a[180:183], v201, s[40:43], s94 offen sc1
s_waitcnt lgkmcnt(2)
v_mfma_f32_16x16x32_bf16 v[12:15], v[152:155], a[0:3], 0
v_mfma_f32_16x16x32_bf16 v[28:31], v[152:155], a[8:11], 0
s_waitcnt lgkmcnt(0)
v_mfma_f32_16x16x32_bf16 v[12:15], v[156:159], a[4:7], v[12:15]
v_mfma_f32_16x16x32_bf16 v[28:31], v[156:159], a[12:15], v[28:31]
v_sub_f32 v160, v0, v17
v_add_f32 v162, v1, v16
v_sub_f32 v161, v2, v19
v_add_f32 v163, v3, v18
v_cvt_pk_bf16_f32 v56, v160, v161
v_cvt_pk_bf16_f32 v152, v162, v163
v_sub_f32 v160, v4, v21
v_add_f32 v162, v5, v20
v_sub_f32 v161, v6, v23
v_add_f32 v163, v7, v22
v_cvt_pk_bf16_f32 v57, v160, v161
v_cvt_pk_bf16_f32 v153, v162, v163
v_sub_f32 v160, v8, v25
v_add_f32 v162, v9, v24
v_sub_f32 v161, v10, v27
v_add_f32 v163, v11, v26
v_cvt_pk_bf16_f32 v58, v160, v161
v_cvt_pk_bf16_f32 v154, v162, v163
v_sub_f32 v160, v12, v29
v_add_f32 v162, v13, v28
v_sub_f32 v161, v14, v31
v_add_f32 v163, v15, v30
v_cvt_pk_bf16_f32 v59, v160, v161
v_cvt_pk_bf16_f32 v155, v162, v163
s_lshl_b32 s66, s23, 13
s_add_u32 s67, s66, 4096
s_cmp_eq_u32 s23, 288
s_cselect_b32 s66, 4096, s66
s_cselect_b32 s67, 2113929216, s67
s_cmp_eq_u32 s23, 0
s_cselect_b32 s67, 2113929216, s67
buffer_store_dwordx4 v[56:59], v169, s[40:43], s66 offen sc1
buffer_store_dwordx4 v[152:155], v169, s[40:43], s67 offen sc1
; ==== s2sel (1 iteration(s))
s_mov_b32 s23, s61
s_cmp_gt_u32 s23, 288
s_cbranch_scc1 L_notask
; ==== s2b (1 iteration(s))
v_xor_b32 v160, 64, v170
s_waitcnt vmcnt(33)
ds_write_b128 v170, v[206:209]
s_waitcnt vmcnt(32)
ds_write_b128 v160, v[210:213] offset:1024
s_waitcnt lgkmcnt(0)
s_barrier
ds_read_b64_tr_b16 v[56:57], v171
ds_read_b64_tr_b16 v[58:59], v171 offset:512
ds_read_b64_tr_b16 v[60:61], v171 offset:4096
ds_read_b64_tr_b16 v[62:63], v171 offset:4608
ds_read_b64_tr_b16 v[152:153], v172
ds_read_b64_tr_b16 v[154:155], v172 offset:512
s_waitcnt lgkmcnt(4)
v_mfma_f32_16x16x32_bf16 v[0:3], v[56:59], a[0:3], 0
v_mfma_f32_16x16x32_bf16 v[16:19], v[56:59], a[8:11], 0
ds_read_b64_tr_b16 v[156:157], v172 offset:4096
ds_read_b64_tr_b16 v[158:159], v172 offset:4608
s_waitcnt lgkmcnt(4)
v_mfma_f32_16x16x32_bf16 v[0:3], v[60:63], a[4:7], v[0:3]
v_mfma_f32_16x16x32_bf16 v[16:19], v[60:63], a[12:15], v[16:19]
ds_read_b64_tr_b16 v[56:57], v173
ds_read_b64_tr_b16 v[58:59], v173 offset:512
s_waitcnt lgkmcnt(4)
v_mfma_f32_16x16x32_bf16 v[4:7], v[152:155], a[0:3], 0
v_mfma_f32_16x16x32_bf16 v[20:23], v[152:155], a[8:11], 0
ds_read_b64_tr_b16 v[60:61], v173 offset:4096
ds_read_b64_tr_b16 v[62:63], v173 offset:4608
s_waitcnt lgkmcnt(4)
v_mfma_f32_16x16x32_bf16 v[4:7], v[156:159], a[4:7], v[4:7]
v_mfma_f32_16x16x32_bf16 v[20:23], v[156:159], a[12:15], v[20:23]
ds_read_b64_tr_b16 v[152:153], v174
ds_read_b64_tr_b16 v[154:155], v174 offset:512
s_waitcnt lgkmcnt(4)
v_mfma_f32_16x16x32_bf16 v[8:11], v[56:59], a[0:3], 0
v_mfma_f32_16x16x32_bf16 v[24:27], v[56:59], a[8:11], 0
ds_read_b64_tr_b16 v[156:157], v174 offset:4096
ds_read_b64_tr_b16 v[158:159], v174 offset:4608
s_waitcnt lgkmcnt(4)
v_mfma_f32_16x16x32_bf16 v[8:11], v[60:63], a[4:7], v[8:11]
v_mfma_f32_16x16x32_bf16 v[24:27], v[60:63], a[12:15], v[24:27]
s_waitcnt lgkmcnt(2)
v_mfma_f32_16x16x32_bf16 v[12:15], v[152:155], a[0:3], 0
v_mfma_f32_16x16x32_bf16 v[28:31], v[152:155], a[8:11], 0
s_waitcnt lgkmcnt(0)
v_mfma_f32_16x16x32_bf16 v[12:15], v[156:159], a[4:7], v[12:15]
v_mfma_f32_16x16x32_bf16 v[28:31], v[156:159], a[12:15], v[28:31]
v_add_f32 v160, v0, v17
v_sub_f32 v161, v1, v16
v_mul_f32 v164, v160, v80
v_mul_f32 v166, v161, v80
v_fmac_f32 v164, v161, v81
v_fma_f32 v166, -v160, v81, v166
v_add_f32 v162, v2, v19
v_sub_f32 v163, v3, v18
v_mul_f32 v165, v162, v82
v_mul_f32 v167, v163, v82
v_fmac_f32 v165, v163, v83
v_fma_f32 v167, -v162, v83, v167
v_cvt_pk_bf16_f32 v120, v164, v165
v_cvt_pk_bf16_f32 v124, v166, v167
v_add_f32 v160, v4, v21
v_sub_f32 v161, v5, v20
v_mul_f32 v164, v160, v84
v_mul_f32 v166, v161, v84
v_fmac_f32 v164, v161, v85
v_fma_f32 v166, -v160, v85, v166
v_add_f32 v162, v6, v23
v_sub_f32 v163, v7, v22
v_mul_f32 v165, v162, v86
v_mul_f32 v167, v163, v86
v_fmac_f32 v165, v163, v87
v_fma_f32 v167, -v162, v87, v167
v_cvt_pk_bf16_f32 v121, v164, v165
v_cvt_pk_bf16_f32 v125, v166, v167
v_add_f32 v160, v8, v25
v_sub_f32 v161, v9, v24
v_mul_f32 v164, v160, v88
v_mul_f32 v166, v161, v88
v_fmac_f32 v164, v161, v89
v_fma_f32 v166, -v160, v89, v166
v_add_f32 v162, v10, v27
v_sub_f32 v163, v11, v26
v_mul_f32 v165, v162, v90
v_mul_f32 v167, v163, v90
v_fmac_f32 v165, v163, v91
v_fma_f32 v167, -v162, v91, v167
v_cvt_pk_bf16_f32 v122, v164, v165
v_cvt_pk_bf16_f32 v126, v166, v167
v_add_f32 v160, v12, v29
v_sub_f32 v161, v13, v28
v_mul_f32 v164, v160, v92
v_mul_f32 v166, v161, v92
v_fmac_f32 v164, v161, v93
v_fma_f32 v166, -v160, v93, v166
v_add_f32 v162, v14, v31
v_sub_f32 v163, v15, v30
v_mul_f32 v165, v162, v94
v_mul_f32 v167, v163, v94
v_fmac_f32 v165, v163, v95
v_fma_f32 v167, -v162, v95, v167
v_cvt_pk_bf16_f32 v123, v164, v165
v_cvt_pk_bf16_f32 v127, v166, v167
s_nop 0
v_mfma_f32_16x16x32_bf16 v[0:3], a[16:19], v[120:123], 0
v_mfma_f32_16x16x32_bf16 v[16:19], a[16:19], v[124:127], 0
v_mfma_f32_16x16x32_bf16 v[4:7], a[28:31], v[120:123], 0
v_mfma_f32_16x16x32_bf16 v[20:23], a[28:31], v[124:127], 0
v_mfma_f32_16x16x32_bf16 v[0:3], a[20:23], v[124:127], v[0:3]
v_mfma_f32_16x16x32_bf16 v[16:19], a[24:27], v[120:123], v[16:19]
v_mfma_f32_16x16x32_bf16 v[4:7], a[32:35], v[124:127], v[4:7]
v_mfma_f32_16x16x32_bf16 v[20:23], a[36:39], v[120:123], v[20:23]
s_waitcnt vmcnt(17)
v_lshlrev_b32 v160, 16, v214
v_and_b32 v161, 4294901760, v214
s_nop 1
v_mul_f32 v164, v0, v160
v_mul_f32 v166, v0, v161
v_fma_f32 v164, -v16, v161, v164
v_fmac_f32 v166, v16, v160
s_waitcnt vmcnt(16)
v_lshlrev_b32 v160, 16, v215
v_and_b32 v161, 4294901760, v215
v_mul_f32 v165, v1, v160
v_mul_f32 v167, v1, v161
v_fma_f32 v165, -v17, v161, v165
v_fmac_f32 v167, v17, v160
v_cvt_pk_bf16_f32 v120, v164, v165
v_cvt_pk_bf16_f32 v124, v166, v167
s_waitcnt vmcnt(15)
v_lshlrev_b32 v160, 16, v216
v_and_b32 v161, 4294901760, v216
v_mul_f32 v164, v2, v160
v_mul_f32 v166, v2, v161
v_fma_f32 v164, -v18, v161, v164
v_fmac_f32 v166, v18, v160
s_waitcnt vmcnt(14)
v_lshlrev_b32 v160, 16, v217
v_and_b32 v161, 4294901760, v217
v_mul_f32 v165, v3, v160
v_mul_f32 v167, v3, v161
v_fma_f32 v165, -v19, v161, v165
v_fmac_f32 v167, v19, v160
v_cvt_pk_bf16_f32 v121, v164, v165
v_cvt_pk_bf16_f32 v125, v166, v167
s_waitcnt vmcnt(13)
v_lshlrev_b32 v160, 16, v218
v_and_b32 v161, 4294901760, v218
v_mul_f32 v164, v4, v160
v_mul_f32 v166, v4, v161
v_fma_f32 v164, -v20, v161, v164
v_fmac_f32 v166, v20, v160
s_waitcnt vmcnt(12)
v_lshlrev_b32 v160, 16, v219
v_and_b32 v161, 4294901760, v219
v_mul_f32 v165, v5, v160
v_mul_f32 v167, v5, v161
v_fma_f32 v165, -v21, v161, v165
v_fmac_f32 v167, v21, v160
v_cvt_pk_bf16_f32 v122, v164, v165
v_cvt_pk_bf16_f32 v126, v166, v167
s_waitcnt vmcnt(11)
v_lshlrev_b32 v160, 16, v220
v_and_b32 v161, 4294901760, v220
v_mul_f32 v164, v6, v160
v_mul_f32 v166, v6, v161
v_fma_f32 v164, -v22, v161, v164
v_fmac_f32 v166, v22, v160
s_waitcnt vmcnt(10)
v_lshlrev_b32 v160, 16, v221
v_and_b32 v161, 4294901760, v221
v_mul_f32 v165, v7, v160
v_mul_f32 v167, v7, v161
v_fma_f32 v165, -v23, v161, v165
v_fmac_f32 v167, v23, v160
v_cvt_pk_bf16_f32 v123, v164, v165
v_cvt_pk_bf16_f32 v127, v166, v167
s_nop 0
v_mfma_f32_16x16x32_bf16 v[8:11], a[40:43], v[120:123], 0
v_mfma_f32_16x16x32_bf16 v[24:27], a[40:43], v[124:127], 0
v_mfma_f32_16x16x32_bf16 v[12:15], a[52:55], v[120:123], 0
v_mfma_f32_16x16x32_bf16 v[28:31], a[52:55], v[124:127], 0
v_mfma_f32_16x16x32_bf16 v[8:11], a[48:51], v[124:127], v[8:11]
v_mfma_f32_16x16x32_bf16 v[24:27], a[44:47], v[120:123], v[24:27]
v_mfma_f32_16x16x32_bf16 v[12:15], a[60:63], v[124:127], v[12:15]
v_mfma_f32_16x16x32_bf16 v[28:31], a[56:59], v[120:123], v[28:31]
s_nop 4
v_mul_f32 v160, v8, v80
v_mul_f32 v161, v24, v80
v_fma_f32 v160, -v24, v81, v160
v_fmac_f32 v161, v8, v81
v_cvt_pk_bf16_f32 v120, v160, v161
v_mul_f32 v162, v9, v82
v_mul_f32 v163, v25, v82
v_fma_f32 v162, -v25, v83, v162
v_fmac_f32 v163, v9, v83
v_cvt_pk_bf16_f32 v121, v162, v163
v_xor_b32 v168, 0, v175
ds_write_b64 v168, v[120:121]
v_mul_f32 v160, v10, v84
v_mul_f32 v161, v26, v84
v_fma_f32 v160, -v26, v85, v160
v_fmac_f32 v161, v10, v85
v_cvt_pk_bf16_f32 v122, v160, v161
v_mul_f32 v162, v11, v86
v_mul_f32 v163, v27, v86
v_fma_f32 v162, -v27, v87, v162
v_fmac_f32 v163, v11, v87
v_cvt_pk_bf16_f32 v123, v162, v163
v_xor_b32 v168, 32, v175
ds_write_b64 v168, v[122:123]
v_mul_f32 v160, v12, v88
v_mul_f32 v161, v28, v88
v_fma_f32 v160, -v28, v89, v160
v_fmac_f32 v161, v12, v89
v_cvt_pk_bf16_f32 v120, v160, v161
v_mul_f32 v162, v13, v90
v_mul_f32 v163, v29, v90
v_fma_f32 v162, -v29, v91, v162
v_fmac_f32 v163, v13, v91
v_cvt_pk_bf16_f32 v121, v162, v163
v_xor_b32 v168, 64, v175
ds_write_b64 v168, v[120:121]
v_mul_f32 v160, v14, v92
v_mul_f32 v161, v30, v92
v_fma_f32 v160, -v30, v93, v160
v_fmac_f32 v161, v14, v93
v_cvt_pk_bf16_f32 v122, v160, v161
v_mul_f32 v162, v15, v94
v_mul_f32 v163, v31, v94
v_fma_f32 v162, -v31, v95, v162
v_fmac_f32 v163, v15, v95
v_cvt_pk_bf16_f32 v123, v162, v163
v_xor_b32 v168, 96, v175
ds_write_b64 v168, v[122:123]
s_waitcnt lgkmcnt(0)
s_barrier
v_xor_b32 v160, 0, v176
v_xor_b32 v161, 64, v176
ds_read_b64_tr_b16 v[56:57], v160
ds_read_b64_tr_b16 v[58:59], v161 offset:512
v_xor_b32 v162, 0, v176
v_xor_b32 v163, 64, v176
ds_read_b64_tr_b16 v[60:61], v162 offset:4096
ds_read_b64_tr_b16 v[62:63], v163 offset:4608
v_xor_b32 v160, 8, v176
v_xor_b32 v161, 72, v176
ds_read_b64_tr_b16 v[152:153], v160
ds_read_b64_tr_b16 v[154:155], v161 offset:512
s_waitcnt lgkmcnt(4)
v_mfma_f32_16x16x32_bf16 v[0:3], v[56:59], a[0:3], 0
v_mfma_f32_16x16x32_bf16 v[16:19], v[56:59], a[8:11], 0
v_xor_b32 v162, 8, v176
v_xor_b32 v163, 72, v176
ds_read_b64_tr_b16 v[156:157], v162 offset:4096
ds_read_b64_tr_b16 v[158:159], v163 offset:4608
s_waitcnt lgkmcnt(4)
v_mfma_f32_16x16x32_bf16 v[0:3], v[60:63], a[4:7], v[0:3]
v_mfma_f32_16x16x32_bf16 v[16:19], v[60:63], a[12:15], v[16:19]
v_xor_b32 v160, 16, v176
v_xor_b32 v161, 80, v176
ds_read_b64_tr_b16 v[56:57], v160
ds_read_b64_tr_b16 v[58:59], v161 offset:512
s_waitcnt lgkmcnt(4)
v_mfma_f32_16x16x32_bf16 v[4:7], v[152:155], a[0:3], 0
v_mfma_f32_16x16x32_bf16 v[20:23], v[152:155], a[8:11], 0
v_xor_b32 v162, 16, v176
v_xor_b32 v163, 80, v176
ds_read_b64_tr_b16 v[60:61], v162 offset:4096
ds_read_b64_tr_b16 v[62:63], v163 offset:4608
s_waitcnt lgkmcnt(4)
v_mfma_f32_16x16x32_bf16 v[4:7], v[156:159], a[4:7], v[4:7]
v_mfma_f32_16x16x32_bf16 v[20:23], v[156:159], a[12:15], v[20:23]
v_xor_b32 v160, 24, v176
v_xor_b32 v161, 88, v176
ds_read_b64_tr_b16 v[152:153], v160
ds_read_b64_tr_b16 v[154:155], v161 offset:512
s_waitcnt lgkmcnt(4)
v_mfma_f32_16x16x32_bf16 v[8:11], v[56:59], a[0:3], 0
v_mfma_f32_16x16x32_bf16 v[24:27], v[56:59], a[8:11], 0
v_xor_b32 v162, 24, v176
v_xor_b32 v163, 88, v176
ds_read_b64_tr_b16 v[156:157], v162 offset:4096
ds_read_b64_tr_b16 v[158:159], v163 offset:4608
s_waitcnt lgkmcnt(4)
v_mfma_f32_16x16x32_bf16 v[8:11], v[60:63], a[4:7], v[8:11]
v_mfma_f32_16x16x32_bf16 v[24:27], v[60:63], a[12:15], v[24:27]
s_waitcnt lgkmcnt(2)
v_mfma_f32_16x16x32_bf16 v[12:15], v[152:155], a[0:3], 0
v_mfma_f32_16x16x32_bf16 v[28:31], v[152:155], a[8:11], 0
s_waitcnt lgkmcnt(0)
v_mfma_f32_16x16x32_bf16 v[12:15], v[156:159], a[4:7], v[12:15]
v_mfma_f32_16x16x32_bf16 v[28:31], v[156:159], a[12:15], v[28:31]
v_sub_f32 v160, v0, v17
v_add_f32 v162, v1, v16
v_sub_f32 v161, v2, v19
v_add_f32 v163, v3, v18
v_cvt_pk_bf16_f32 v56, v160, v161
v_cvt_pk_bf16_f32 v152, v162, v163
v_sub_f32 v160, v4, v21
v_add_f32 v162, v5, v20
v_sub_f32 v161, v6, v23
v_add_f32 v163, v7, v22
v_cvt_pk_bf16_f32 v57, v160, v161
v_cvt_pk_bf16_f32 v153, v162, v163
v_sub_f32 v160, v8, v25
v_add_f32 v162, v9, v24
v_sub_f32 v161, v10, v27
v_add_f32 v163, v11, v26
v_cvt_pk_bf16_f32 v58, v160, v161
v_cvt_pk_bf16_f32 v154, v162, v163
v_sub_f32 v160, v12, v29
v_add_f32 v162, v13, v28
v_sub_f32 v161, v14, v31
v_add_f32 v163, v15, v30
v_cvt_pk_bf16_f32 v59, v160, v161
v_cvt_pk_bf16_f32 v155, v162, v163
s_lshl_b32 s66, s23, 13
s_add_u32 s67, s66, 4096
s_cmp_eq_u32 s23, 288
s_cselect_b32 s66, 4096, s66
s_cselect_b32 s67, 2113929216, s67
s_cmp_eq_u32 s23, 0
s_cselect_b32 s67, 2113929216, s67
buffer_store_dwordx4 v[56:59], v169, s[40:43], s66 offen sc1
buffer_store_dwordx4 v[152:155], v169, s[40:43], s67 offen sc1
L_notask:
; ==== b0 (1 iteration(s))
s_waitcnt vmcnt(42)
v_mfma_f32_32x32x16_bf16 v[64:79], v[128:131], a[64:67], 0
ds_read_b64_tr_b16 v[144:145], v196 offset:40960
ds_read_b64_tr_b16 v[146:147], v196 offset:43008
v_mfma_f32_32x32x16_bf16 v[80:95], v[128:131], a[68:71], 0
ds_read_b64_tr_b16 v[148:149], v197 offset:40960
ds_read_b64_tr_b16 v[150:151], v197 offset:43008
s_mov_b32 s94, 98304
v_mfma_f32_32x32x16_bf16 v[112:127], v[132:135], a[68:71], 0
buffer_load_dwordx4 a[184:187], v200, s[40:43], s94 offen offset:1024 sc1
v_mfma_f32_32x32x16_bf16 v[96:111], v[132:135], a[64:67], 0
buffer_load_dwordx4 a[188:191], v201, s[40:43], s94 offen offset:1024 sc1
s_waitcnt vmcnt(42)
v_mfma_f32_32x32x16_bf16 v[64:79], v[136:139], a[72:75], v[64:79]
ds_read_b64_tr_b16 v[128:129], v196 offset:45056
ds_read_b64_tr_b16 v[130:131], v196 offset:47104
v_mfma_f32_32x32x16_bf16 v[80:95], v[136:139], a[76:79], v[80:95]
ds_read_b64_tr_b16 v[132:133], v197 offset:45056
ds_read_b64_tr_b16 v[134:135], v197 offset:47104
s_mov_b32 s94, 32768
v_mfma_f32_32x32x16_bf16 v[112:127], v[140:143], a[76:79], v[112:127]
buffer_load_dwordx4 a[192:195], v200, s[40:43], s94 offen sc1
v_mfma_f32_32x32x16_bf16 v[96:111], v[140:143], a[72:75], v[96:111]
buffer_load_dwordx4 a[196:199], v201, s[40:43], s94 offen sc1
s_waitcnt vmcnt(42) lgkmcnt(4)
v_mfma_f32_32x32x16_bf16 v[64:79], v[144:147], a[80:83], v[64:79]
s_barrier
ds_read_b64_tr_b16 v[136:137], v194
ds_read_b64_tr_b16 v[138:139], v194 offset:2048
v_mfma_f32_32x32x16_bf16 v[80:95], v[144:147], a[84:87], v[80:95]
ds_read_b64_tr_b16 v[140:141], v195
ds_read_b64_tr_b16 v[142:143], v195 offset:2048
v_mfma_f32_32x32x16_bf16 v[112:127], v[148:151], a[84:87], v[112:127]
s_add_u32 s64, s4, 516096
s_add_u32 m0, s72, 49152
s_add_u32 s65, s64, 9216
buffer_load_dwordx4 a[200:203], v200, s[40:43], s94 offen offset:1024 sc1
v_mfma_f32_32x32x16_bf16 v[96:111], v[148:151], a[80:83], v[96:111]
buffer_load_dwordx4 v192, s[24:27], s64 offen lds
s_waitcnt vmcnt(42) lgkmcnt(4)
v_mfma_f32_32x32x16_bf16 v[64:79], v[128:131], a[88:91], v[64:79]
ds_read_b64_tr_b16 v[144:145], v194 offset:4096
ds_read_b64_tr_b16 v[146:147], v194 offset:6144
v_mfma_f32_32x32x16_bf16 v[80:95], v[128:131], a[92:95], v[80:95]
ds_read_b64_tr_b16 v[148:149], v195 offset:4096
ds_read_b64_tr_b16 v[150:151], v195 offset:6144
buffer_load_dwordx4 a[204:207], v201, s[40:43], s94 offen offset:1024 sc1
v_mfma_f32_32x32x16_bf16 v[112:127], v[132:135], a[92:95], v[112:127]
buffer_load_dwordx4 v192, s[12:15], s64 offen offset:1024 lds
s_add_u32 m0, s72, 51200
s_mov_b32 s94, 40960
v_mfma_f32_32x32x16_bf16 v[96:111], v[132:135], a[88:91], v[96:111]
buffer_load_dwordx4 v192, s[24:27], s65 offen lds
s_waitcnt vmcnt(43) lgkmcnt(4)
v_mfma_f32_32x32x16_bf16 v[64:79], v[136:139], a[96:99], v[64:79]
ds_read_b64_tr_b16 v[128:129], v194 offset:8192
ds_read_b64_tr_b16 v[130:131], v194 offset:10240
buffer_load_dwordx4 a[208:211], v200, s[40:43], s94 offen sc1
v_mfma_f32_32x32x16_bf16 v[80:95], v[136:139], a[100:103], v[80:95]
ds_read_b64_tr_b16 v[132:133], v195 offset:8192
ds_read_b64_tr_b16 v[134:135], v195 offset:10240
v_mfma_f32_32x32x16_bf16 v[112:127], v[140:143], a[100:103], v[112:127]
buffer_load_dwordx4 v192, s[12:15], s65 offen offset:1024 lds
buffer_load_dwordx4 a[212:215], v201, s[40:43], s94 offen sc1
v_mfma_f32_32x32x16_bf16 v[96:111], v[140:143], a[96:99], v[96:111]
s_waitcnt vmcnt(42) lgkmcnt(4)
v_mfma_f32_32x32x16_bf16 v[64:79], v[144:147], a[104:107], v[64:79]
ds_read_b64_tr_b16 v[136:137], v194 offset:12288
ds_read_b64_tr_b16 v[138:139], v194 offset:14336
buffer_load_dwordx4 a[216:219], v200, s[40:43], s94 offen offset:1024 sc1
v_mfma_f32_32x32x16_bf16 v[80:95], v[144:147], a[108:111], v[80:95]
ds_read_b64_tr_b16 v[140:141], v195 offset:12288
ds_read_b64_tr_b16 v[142:143], v195 offset:14336
v_mfma_f32_32x32x16_bf16 v[112:127], v[148:151], a[108:111], v[112:127]
buffer_load_dwordx4 a[220:223], v201, s[40:43], s94 offen offset:1024 sc1
v_mfma_f32_32x32x16_bf16 v[96:111], v[148:151], a[104:107], v[96:111]
s_mov_b32 s94, 106496
s_waitcnt vmcnt(42) lgkmcnt(4)
v_mfma_f32_32x32x16_bf16 v[64:79], v[128:131], a[112:115], v[64:79]
s_waitcnt vmcnt(14)
s_barrier
s_lshl_b32 s66, s56, 4
s_add_u32 s66, s66, s57
s_lshl_b32 s66, s66, 3
s_add_u32 s66, s66, 33792
s_cmp_lg_u32 s58, 0
s_cbranch_scc1 L_skip1
s_mov_b64 exec, 1
v_mov_b32 v186, s62
v_mov_b32 v187, s63
v_mov_b32 v188, s66
buffer_store_dwordx2 v[186:187], v188, s[40:43], 0 offen sc1
s_mov_b64 exec, -1
L_skip1:
ds_read_b64_tr_b16 v[144:145], v194 offset:16384
ds_read_b64_tr_b16 v[146:147], v194 offset:18432
v_mfma_f32_32x32x16_bf16 v[80:95], v[128:131], a[116:119], v[80:95]
ds_read_b64_tr_b16 v[148:149], v195 offset:16384
ds_read_b64_tr_b16 v[150:151], v195 offset:18432
buffer_load_dwordx4 a[224:227], v200, s[40:43], s94 offen sc1
v_mfma_f32_32x32x16_bf16 v[112:127], v[132:135], a[116:119], v[112:127]
s_add_u32 s64, s4, 221184
s_add_u32 m0, s72, 65536
s_add_u32 s65, s64, 9216
v_mfma_f32_32x32x16_bf16 v[96:111], v[132:135], a[112:115], v[96:111]
buffer_load_dwordx4 v192, s[24:27], s64 offen lds
buffer_load_dwordx4 a[228:231], v201, s[40:43], s94 offen sc1
s_waitcnt lgkmcnt(4)
v_mfma_f32_32x32x16_bf16 v[64:79], v[136:139], a[120:123], v[64:79]
ds_read_b64_tr_b16 v[128:129], v194 offset:20480
ds_read_b64_tr_b16 v[130:131], v194 offset:22528
v_mfma_f32_32x32x16_bf16 v[80:95], v[136:139], a[124:127], v[80:95]
ds_read_b64_tr_b16 v[132:133], v195 offset:20480
ds_read_b64_tr_b16 v[134:135], v195 offset:22528
buffer_load_dwordx4 a[232:235], v200, s[40:43], s94 offen offset:1024 sc1
v_mfma_f32_32x32x16_bf16 v[112:127], v[140:143], a[124:127], v[112:127]
buffer_load_dwordx4 v192, s[12:15], s64 offen offset:1024 lds
s_add_u32 m0, s72, 67584
v_mfma_f32_32x32x16_bf16 v[96:111], v[140:143], a[120:123], v[96:111]
buffer_load_dwordx4 v192, s[24:27], s65 offen lds
s_waitcnt lgkmcnt(4)
v_mfma_f32_32x32x16_bf16 v[64:79], v[144:147], a[128:131], v[64:79]
ds_read_b64_tr_b16 v[136:137], v194 offset:24576
ds_read_b64_tr_b16 v[138:139], v194 offset:26624
buffer_load_dwordx4 a[236:239], v201, s[40:43], s94 offen offset:1024 sc1
v_mfma_f32_32x32x16_bf16 v[80:95], v[144:147], a[132:135], v[80:95]
ds_read_b64_tr_b16 v[140:141], v195 offset:24576
ds_read_b64_tr_b16 v[142:143], v195 offset:26624
v_mfma_f32_32x32x16_bf16 v[112:127], v[148:151], a[132:135], v[112:127]
buffer_load_dwordx4 v192, s[12:15], s65 offen offset:1024 lds
s_mov_b32 s94, 114688
v_mfma_f32_32x32x16_bf16 v[96:111], v[148:151], a[128:131], v[96:111]
buffer_load_dwordx4 a[240:243], v200, s[40:43], s94 offen sc1
s_waitcnt lgkmcnt(4)
v_mfma_f32_32x32x16_bf16 v[64:79], v[128:131], a[136:139], v[64:79]
ds_read_b64_tr_b16 v[144:145], v194 offset:28672
ds_read_b64_tr_b16 v[146:147], v194 offset:30720
v_mfma_f32_32x32x16_bf16 v[80:95], v[128:131], a[140:143], v[80:95]
ds_read_b64_tr_b16 v[148:149], v195 offset:28672
ds_read_b64_tr_b16 v[150:151], v195 offset:30720
buffer_load_dwordx4 a[244:247], v201, s[40:43], s94 offen sc1
v_mfma_f32_32x32x16_bf16 v[112:127], v[132:135], a[140:143], v[112:127]
buffer_load_dwordx4 a[248:251], v200, s[40:43], s94 offen offset:1024 sc1
v_mfma_f32_32x32x16_bf16 v[96:111], v[132:135], a[136:139], v[96:111]
s_waitcnt lgkmcnt(4)
v_mfma_f32_32x32x16_bf16 v[64:79], v[136:139], a[144:147], v[64:79]
s_barrier
ds_read_b64_tr_b16 v[128:129], v194 offset:32768
ds_read_b64_tr_b16 v[130:131], v194 offset:34816
buffer_load_dwordx4 a[252:255], v201, s[40:43], s94 offen offset:1024 sc1
v_mfma_f32_32x32x16_bf16 v[80:95], v[136:139], a[148:151], v[80:95]
ds_read_b64_tr_b16 v[132:133], v195 offset:32768
ds_read_b64_tr_b16 v[134:135], v195 offset:34816
v_mfma_f32_32x32x16_bf16 v[112:127], v[140:143], a[148:151], v[112:127]
s_add_u32 s64, s4, 589824
s_add_u32 m0, s72, 0
s_add_u32 s65, s64, 9216
v_mfma_f32_32x32x16_bf16 v[96:111], v[140:143], a[144:147], v[96:111]
buffer_load_dwordx4 v192, s[24:27], s64 offen lds
s_waitcnt lgkmcnt(4)
v_mfma_f32_32x32x16_bf16 v[64:79], v[144:147], a[152:155], v[64:79]
ds_read_b64_tr_b16 v[136:137], v194 offset:36864
ds_read_b64_tr_b16 v[138:139], v194 offset:38912
v_cmp_eq_u64 vcc, s[62:63], v[222:223]
s_nop 3
s_cmp_eq_u64 vcc, -1
s_cbranch_scc1 L_ok9
s_mov_b32 s17, 0x20000
L_poll8:
s_sleep 1
buffer_load_dwordx2 v[222:223], v205, s[40:43], 0 offen sc1
s_waitcnt vmcnt(0)
v_cmp_eq_u64 vcc, s[62:63], v[222:223]
s_nop 4
s_cmp_eq_u64 vcc, -1
s_cbranch_scc1 L_ok9
s_sub_u32 s17, s17, 1
s_cmp_lg_u32 s17, 0
s_cbranch_scc1 L_poll8
L_ok9:
v_mfma_f32_32x32x16_bf16 v[80:95], v[144:147], a[156:159], v[80:95]
ds_read_b64_tr_b16 v[140:141], v195 offset:36864
ds_read_b64_tr_b16 v[142:143], v195 offset:38912
v_mfma_f32_32x32x16_bf16 v[112:127], v[148:151], a[156:159], v[112:127]
buffer_load_dwordx4 v192, s[12:15], s64 offen offset:1024 lds
s_add_u32 m0, s72, 2048
s_mov_b32 s94, 49152
v_mfma_f32_32x32x16_bf16 v[96:111], v[148:151], a[152:155], v[96:111]
buffer_load_dwordx4 v192, s[24:27], s65 offen lds
s_waitcnt lgkmcnt(4)
v_mfma_f32_32x32x16_bf16 v[64:79], v[128:131], a[160:163], v[64:79]
ds_read_b64_tr_b16 v[144:145], v194 offset:40960
ds_read_b64_tr_b16 v[146:147], v194 offset:43008
buffer_load_dwordx4 v[224:227], v200, s[40:43], s94 offen sc1
v_mfma_f32_32x32x16_bf16 v[80:95], v[128:131], a[164:167], v[80:95]
ds_read_b64_tr_b16 v[148:149], v195 offset:40960
ds_read_b64_tr_b16 v[150:151], v195 offset:43008
v_mfma_f32_32x32x16_bf16 v[112:127], v[132:135], a[164:167], v[112:127]
buffer_load_dwordx4 v192, s[12:15], s65 offen offset:1024 lds
buffer_load_dwordx4 v[228:231], v201, s[40:43], s94 offen sc1
v_mfma_f32_32x32x16_bf16 v[96:111], v[132:135], a[160:163], v[96:111]
buffer_load_dwordx4 v[232:235], v200, s[40:43], s94 offen offset:1024 sc1
s_waitcnt lgkmcnt(4)
v_mfma_f32_32x32x16_bf16 v[64:79], v[136:139], a[168:171], v[64:79]
ds_read_b64_tr_b16 v[128:129], v194 offset:45056
ds_read_b64_tr_b16 v[130:131], v194 offset:47104
v_mfma_f32_32x32x16_bf16 v[80:95], v[136:139], a[172:175], v[80:95]
ds_read_b64_tr_b16 v[132:133], v195 offset:45056
ds_read_b64_tr_b16 v[134:135], v195 offset:47104
buffer_load_dwordx4 v[236:239], v201, s[40:43], s94 offen offset:1024 sc1
v_mfma_f32_32x32x16_bf16 v[112:127], v[140:143], a[172:175], v[112:127]
s_mov_b32 s94, 57344
v_mfma_f32_32x32x16_bf16 v[96:111], v[140:143], a[168:171], v[96:111]
buffer_load_dwordx4 v[240:243], v200, s[40:43], s94 offen sc1
s_waitcnt lgkmcnt(4)
v_mfma_f32_32x32x16_bf16 v[64:79], v[144:147], a[176:179], v[64:79]
s_barrier
ds_read_b64_tr_b16 v[136:137], v196 offset:49152
ds_read_b64_tr_b16 v[138:139], v196 offset:51200
v_mfma_f32_32x32x16_bf16 v[80:95], v[144:147], a[180:183], v[80:95]
ds_read_b64_tr_b16 v[140:141], v197 offset:49152
ds_read_b64_tr_b16 v[142:143], v197 offset:51200
v_mfma_f32_32x32x16_bf16 v[112:127], v[148:151], a[180:183], v[112:127]
s_add_u32 s64, s4, 294912
s_add_u32 m0, s72, 16384
s_add_u32 s65, s64, 9216
buffer_load_dwordx4 v[244:247], v201, s[40:43], s94 offen sc1
v_mfma_f32_32x32x16_bf16 v[96:111], v[148:151], a[176:179], v[96:111]
buffer_load_dwordx4 v192, s[24:27], s64 offen lds
s_waitcnt vmcnt(35) lgkmcnt(4)
v_mfma_f32_32x32x16_bf16 v[64:79], v[128:131], a[184:187], v[64:79]
ds_read_b64_tr_b16 v[144:145], v196 offset:53248
ds_read_b64_tr_b16 v[146:147], v196 offset:55296
buffer_load_dwordx4 v[248:251], v200, s[40:43], s94 offen offset:1024 sc1
v_mfma_f32_32x32x16_bf16 v[80:95], v[128:131], a[188:191], v[80:95]
ds_read_b64_tr_b16 v[148:149], v197 offset:53248
ds_read_b64_tr_b16 v[150:151], v197 offset:55296
v_mfma_f32_32x32x16_bf16 v[112:127], v[132:135], a[188:191], v[112:127]
buffer_load_dwordx4 v192, s[12:15], s64 offen offset:1024 lds
s_add_u32 m0, s72, 18432
v_mfma_f32_32x32x16_bf16 v[96:111], v[132:135], a[184:187], v[96:111]
buffer_load_dwordx4 v192, s[24:27], s65 offen lds
buffer_load_dwordx4 v[252:255], v201, s[40:43], s94 offen offset:1024 sc1
s_waitcnt vmcnt(37) lgkmcnt(4)
v_mfma_f32_32x32x16_bf16 v[64:79], v[136:139], a[192:195], v[64:79]
ds_read_b64_tr_b16 v[128:129], v196 offset:57344
ds_read_b64_tr_b16 v[130:131], v196 offset:59392
s_mov_b32 s94, 122880
v_mfma_f32_32x32x16_bf16 v[80:95], v[136:139], a[196:199], v[80:95]
ds_read_b64_tr_b16 v[132:133], v197 offset:57344
ds_read_b64_tr_b16 v[134:135], v197 offset:59392
v_mfma_f32_32x32x16_bf16 v[112:127], v[140:143], a[196:199], v[112:127]
buffer_load_dwordx4 v192, s[12:15], s65 offen offset:1024 lds
buffer_load_dwordx4 a[0:3], v200, s[40:43], s94 offen sc1
v_mfma_f32_32x32x16_bf16 v[96:111], v[140:143], a[192:195], v[96:111]
buffer_load_dwordx4 a[4:7], v201, s[40:43], s94 offen sc1
s_waitcnt vmcnt(37) lgkmcnt(4)
v_mfma_f32_32x32x16_bf16 v[64:79], v[144:147], a[200:203], v[64:79]
ds_read_b64_tr_b16 v[136:137], v196 offset:61440
ds_read_b64_tr_b16 v[138:139], v196 offset:63488
v_mfma_f32_32x32x16_bf16 v[80:95], v[144:147], a[204:207], v[80:95]
ds_read_b64_tr_b16 v[140:141], v197 offset:61440
ds_read_b64_tr_b16 v[142:143], v197 offset:63488
buffer_load_dwordx4 a[8:11], v200, s[40:43], s94 offen offset:1024 sc1
v_mfma_f32_32x32x16_bf16 v[112:127], v[148:151], a[204:207], v[112:127]
v_mfma_f32_32x32x16_bf16 v[96:111], v[148:151], a[200:203], v[96:111]
buffer_load_dwordx4 a[12:15], v201, s[40:43], s94 offen offset:1024 sc1
s_waitcnt vmcnt(34) lgkmcnt(4)
v_mfma_f32_32x32x16_bf16 v[64:79], v[128:131], a[208:211], v[64:79]
s_barrier
ds_read_b64_tr_b16 v[144:145], v194 offset:49152
ds_read_b64_tr_b16 v[146:147], v194 offset:51200
s_mov_b32 s94, 131072
v_mfma_f32_32x32x16_bf16 v[80:95], v[128:131], a[212:215], v[80:95]
ds_read_b64_tr_b16 v[148:149], v195 offset:49152
ds_read_b64_tr_b16 v[150:151], v195 offset:51200
v_mfma_f32_32x32x16_bf16 v[112:127], v[132:135], a[212:215], v[112:127]
s_add_u32 s64, s4, 128
s_add_u32 m0, s72, 32768
s_add_u32 s65, s64, 9216
buffer_load_dwordx4 a[16:19], v200, s[40:43], s94 offen sc1
v_mfma_f32_32x32x16_bf16 v[96:111], v[132:135], a[208:211], v[96:111]
buffer_load_dwordx4 v192, s[24:27], s64 offen lds
s_waitcnt vmcnt(34) lgkmcnt(4)
v_mfma_f32_32x32x16_bf16 v[64:79], v[136:139], a[216:219], v[64:79]
ds_read_b64_tr_b16 v[128:129], v194 offset:53248
ds_read_b64_tr_b16 v[130:131], v194 offset:55296
buffer_load_dwordx4 a[20:23], v201, s[40:43], s94 offen sc1
v_mfma_f32_32x32x16_bf16 v[80:95], v[136:139], a[220:223], v[80:95]
ds_read_b64_tr_b16 v[132:133], v195 offset:53248
ds_read_b64_tr_b16 v[134:135], v195 offset:55296
v_mfma_f32_32x32x16_bf16 v[112:127], v[140:143], a[220:223], v[112:127]
buffer_load_dwordx4 v192, s[12:15], s64 offen offset:1024 lds
s_add_u32 m0, s72, 34816
v_mfma_f32_32x32x16_bf16 v[96:111], v[140:143], a[216:219], v[96:111]
buffer_load_dwordx4 v192, s[24:27], s65 offen lds
buffer_load_dwordx4 a[24:27], v200, s[40:43], s94 offen offset:1024 sc1
s_waitcnt vmcnt(35) lgkmcnt(4)
v_mfma_f32_32x32x16_bf16 v[64:79], v[144:147], a[224:227], v[64:79]
ds_read_b64_tr_b16 v[136:137], v194 offset:57344
ds_read_b64_tr_b16 v[138:139], v194 offset:59392
v_mfma_f32_32x32x16_bf16 v[80:95], v[144:147], a[228:231], v[80:95]
ds_read_b64_tr_b16 v[140:141], v195 offset:57344
ds_read_b64_tr_b16 v[142:143], v195 offset:59392
buffer_load_dwordx4 a[28:31], v201, s[40:43], s94 offen offset:1024 sc1
v_mfma_f32_32x32x16_bf16 v[112:127], v[148:151], a[228:231], v[112:127]
buffer_load_dwordx4 v192, s[12:15], s65 offen offset:1024 lds
s_mov_b32 s94, 65536
v_mfma_f32_32x32x16_bf16 v[96:111], v[148:151], a[224:227], v[96:111]
buffer_load_dwordx4 a[32:35], v200, s[40:43], s94 offen sc1
s_waitcnt vmcnt(34) lgkmcnt(4)
v_mfma_f32_32x32x16_bf16 v[64:79], v[128:131], a[232:235], v[64:79]
ds_read_b64_tr_b16 v[144:145], v194 offset:61440
ds_read_b64_tr_b16 v[146:147], v194 offset:63488
v_mfma_f32_32x32x16_bf16 v[80:95], v[128:131], a[236:239], v[80:95]
ds_read_b64_tr_b16 v[148:149], v195 offset:61440
ds_read_b64_tr_b16 v[150:151], v195 offset:63488
buffer_load_dwordx4 a[36:39], v201, s[40:43], s94 offen sc1
v_mfma_f32_32x32x16_bf16 v[112:127], v[132:135], a[236:239], v[112:127]
v_mfma_f32_32x32x16_bf16 v[96:111], v[132:135], a[232:235], v[96:111]
buffer_load_dwordx4 a[40:43], v200, s[40:43], s94 offen offset:1024 sc1
s_waitcnt vmcnt(33) lgkmcnt(4)
v_mfma_f32_32x32x16_bf16 v[64:79], v[136:139], a[240:243], v[64:79]
s_barrier
ds_read_b64_tr_b16 v[128:129], v196 offset:32768
ds_read_b64_tr_b16 v[130:131], v196 offset:34816
v_mfma_f32_32x32x16_bf16 v[80:95], v[136:139], a[244:247], v[80:95]
ds_read_b64_tr_b16 v[132:133], v197 offset:32768
ds_read_b64_tr_b16 v[134:135], v197 offset:34816
buffer_load_dwordx4 a[44:47], v201, s[40:43], s94 offen offset:1024 sc1
v_mfma_f32_32x32x16_bf16 v[112:127], v[140:143], a[244:247], v[112:127]
s_add_u32 s64, s4, 73856
s_add_u32 m0, s72, 81920
s_add_u32 s65, s64, 9216
s_mov_b32 s94, 139264
v_mfma_f32_32x32x16_bf16 v[96:111], v[140:143], a[240:243], v[96:111]
buffer_load_dwordx4 v192, s[24:27], s64 offen lds
s_waitcnt vmcnt(33) lgkmcnt(4)
v_mfma_f32_32x32x16_bf16 v[64:79], v[144:147], a[248:251], v[64:79]
ds_read_b64_tr_b16 v[136:137], v196 offset:36864
ds_read_b64_tr_b16 v[138:139], v196 offset:38912
buffer_load_dwordx4 a[48:51], v200, s[40:43], s94 offen sc1
v_mfma_f32_32x32x16_bf16 v[80:95], v[144:147], a[252:255], v[80:95]
ds_read_b64_tr_b16 v[140:141], v197 offset:36864
ds_read_b64_tr_b16 v[142:143], v197 offset:38912
v_mfma_f32_32x32x16_bf16 v[112:127], v[148:151], a[252:255], v[112:127]
buffer_load_dwordx4 v192, s[12:15], s64 offen offset:1024 lds
s_add_u32 m0, s72, 83968
v_mfma_f32_32x32x16_bf16 v[96:111], v[148:151], a[248:251], v[96:111]
buffer_load_dwordx4 v192, s[24:27], s65 offen lds
buffer_load_dwordx4 a[52:55], v201, s[40:43], s94 offen sc1
s_waitcnt vmcnt(31) lgkmcnt(4)
v_mfma_f32_32x32x16_bf16 v[64:79], v[128:131], v[224:227], v[64:79]
ds_read_b64_tr_b16 v[144:145], v196 offset:40960
ds_read_b64_tr_b16 v[146:147], v196 offset:43008
v_mfma_f32_32x32x16_bf16 v[80:95], v[128:131], v[228:231], v[80:95]
ds_read_b64_tr_b16 v[148:149], v197 offset:40960
ds_read_b64_tr_b16 v[150:151], v197 offset:43008
buffer_load_dwordx4 a[56:59], v200, s[40:43], s94 offen offset:1024 sc1
v_mfma_f32_32x32x16_bf16 v[112:127], v[132:135], v[228:231], v[112:127]
buffer_load_dwordx4 v192, s[12:15], s65 offen offset:1024 lds
v_mfma_f32_32x32x16_bf16 v[96:111], v[132:135], v[224:227], v[96:111]
buffer_load_dwordx4 a[60:63], v201, s[40:43], s94 offen offset:1024 sc1
s_waitcnt vmcnt(32) lgkmcnt(4)
v_mfma_f32_32x32x16_bf16 v[64:79], v[136:139], v[232:235], v[64:79]
ds_read_b64_tr_b16 v[128:129], v196 offset:45056
ds_read_b64_tr_b16 v[130:131], v196 offset:47104
v_mfma_f32_32x32x16_bf16 v[80:95], v[136:139], v[236:239], v[80:95]
ds_read_b64_tr_b16 v[132:133], v197 offset:45056
ds_read_b64_tr_b16 v[134:135], v197 offset:47104
v_mfma_f32_32x32x16_bf16 v[112:127], v[140:143], v[236:239], v[112:127]
v_mfma_f32_32x32x16_bf16 v[96:111], v[140:143], v[232:235], v[96:111]
s_waitcnt vmcnt(30) lgkmcnt(4)
v_mfma_f32_32x32x16_bf16 v[64:79], v[144:147], v[240:243], v[64:79]
s_barrier
ds_read_b64_tr_b16 v[136:137], v194
ds_read_b64_tr_b16 v[138:139], v194 offset:2048
v_mfma_f32_32x32x16_bf16 v[80:95], v[144:147], v[244:247], v[80:95]
ds_read_b64_tr_b16 v[140:141], v195
ds_read_b64_tr_b16 v[142:143], v195 offset:2048
v_mfma_f32_32x32x16_bf16 v[112:127], v[148:151], v[244:247], v[112:127]
s_add_u32 s64, s4, 368768
s_add_u32 m0, s72, 49152
s_add_u32 s65, s64, 9216
v_mfma_f32_32x32x16_bf16 v[96:111], v[148:151], v[240:243], v[96:111]
buffer_load_dwordx4 v192, s[24:27], s64 offen lds
s_waitcnt vmcnt(26) lgkmcnt(4)
v_mfma_f32_32x32x16_bf16 v[64:79], v[128:131], v[248:251], v[64:79]
ds_read_b64_tr_b16 v[144:145], v194 offset:4096
ds_read_b64_tr_b16 v[146:147], v194 offset:6144
v_mfma_f32_32x32x16_bf16 v[80:95], v[128:131], v[252:255], v[80:95]
ds_read_b64_tr_b16 v[148:149], v195 offset:4096
ds_read_b64_tr_b16 v[150:151], v195 offset:6144
v_mfma_f32_32x32x16_bf16 v[112:127], v[132:135], v[252:255], v[112:127]
buffer_load_dwordx4 v192, s[12:15], s64 offen offset:1024 lds
s_add_u32 m0, s72, 51200
v_mfma_f32_32x32x16_bf16 v[96:111], v[132:135], v[248:251], v[96:111]
buffer_load_dwordx4 v192, s[24:27], s65 offen lds
s_waitcnt vmcnt(25) lgkmcnt(4)
v_mfma_f32_32x32x16_bf16 v[64:79], v[136:139], a[0:3], v[64:79]
ds_read_b64_tr_b16 v[128:129], v194 offset:8192
ds_read_b64_tr_b16 v[130:131], v194 offset:10240
v_mfma_f32_32x32x16_bf16 v[80:95], v[136:139], a[4:7], v[80:95]
ds_read_b64_tr_b16 v[132:133], v195 offset:8192
ds_read_b64_tr_b16 v[134:135], v195 offset:10240
v_mfma_f32_32x32x16_bf16 v[112:127], v[140:143], a[4:7], v[112:127]
buffer_load_dwordx4 v192, s[12:15], s65 offen offset:1024 lds
v_mfma_f32_32x32x16_bf16 v[96:111], v[140:143], a[0:3], v[96:111]
s_waitcnt vmcnt(24) lgkmcnt(4)
v_mfma_f32_32x32x16_bf16 v[64:79], v[144:147], a[8:11], v[64:79]
ds_read_b64_tr_b16 v[136:137], v194 offset:12288
ds_read_b64_tr_b16 v[138:139], v194 offset:14336
v_mfma_f32_32x32x16_bf16 v[80:95], v[144:147], a[12:15], v[80:95]
ds_read_b64_tr_b16 v[140:141], v195 offset:12288
ds_read_b64_tr_b16 v[142:143], v195 offset:14336
v_mfma_f32_32x32x16_bf16 v[112:127], v[148:151], a[12:15], v[112:127]
v_mfma_f32_32x32x16_bf16 v[96:111], v[148:151], a[8:11], v[96:111]
s_waitcnt vmcnt(21) lgkmcnt(4)
v_mfma_f32_32x32x16_bf16 v[64:79], v[128:131], a[16:19], v[64:79]
s_barrier
ds_read_b64_tr_b16 v[144:145], v194 offset:16384
ds_read_b64_tr_b16 v[146:147], v194 offset:18432
v_mfma_f32_32x32x16_bf16 v[80:95], v[128:131], a[20:23], v[80:95]
ds_read_b64_tr_b16 v[148:149], v195 offset:16384
ds_read_b64_tr_b16 v[150:151], v195 offset:18432
v_mfma_f32_32x32x16_bf16 v[112:127], v[132:135], a[20:23], v[112:127]
s_add_u32 s64, s4, 442496
s_add_u32 m0, s72, 65536
s_add_u32 s65, s64, 9216
v_mfma_f32_32x32x16_bf16 v[96:111], v[132:135], a[16:19], v[96:111]
buffer_load_dwordx4 v192, s[24:27], s64 offen lds
s_waitcnt vmcnt(18) lgkmcnt(4)
v_mfma_f32_32x32x16_bf16 v[64:79], v[136:139], a[24:27], v[64:79]
ds_read_b64_tr_b16 v[128:129], v194 offset:20480
ds_read_b64_tr_b16 v[130:131], v194 offset:22528
v_mfma_f32_32x32x16_bf16 v[80:95], v[136:139], a[28:31], v[80:95]
ds_read_b64_tr_b16 v[132:133], v195 offset:20480
ds_read_b64_tr_b16 v[134:135], v195 offset:22528
v_mfma_f32_32x32x16_bf16 v[112:127], v[140:143], a[28:31], v[112:127]
buffer_load_dwordx4 v192, s[12:15], s64 offen offset:1024 lds
s_add_u32 m0, s72, 67584
v_mfma_f32_32x32x16_bf16 v[96:111], v[140:143], a[24:27], v[96:111]
buffer_load_dwordx4 v192, s[24:27], s65 offen lds
s_waitcnt vmcnt(17) lgkmcnt(4)
v_mfma_f32_32x32x16_bf16 v[64:79], v[144:147], a[32:35], v[64:79]
ds_read_b64_tr_b16 v[136:137], v194 offset:24576
ds_read_b64_tr_b16 v[138:139], v194 offset:26624
v_mfma_f32_32x32x16_bf16 v[80:95], v[144:147], a[36:39], v[80:95]
ds_read_b64_tr_b16 v[140:141], v195 offset:24576
ds_read_b64_tr_b16 v[142:143], v195 offset:26624
v_mfma_f32_32x32x16_bf16 v[112:127], v[148:151], a[36:39], v[112:127]
buffer_load_dwordx4 v192, s[12:15], s65 offen offset:1024 lds
v_mfma_f32_32x32x16_bf16 v[96:111], v[148:151], a[32:35], v[96:111]
s_waitcnt vmcnt(16) lgkmcnt(4)
v_mfma_f32_32x32x16_bf16 v[64:79], v[128:131], a[40:43], v[64:79]
ds_read_b64_tr_b16 v[144:145], v194 offset:28672
ds_read_b64_tr_b16 v[146:147], v194 offset:30720
v_mfma_f32_32x32x16_bf16 v[80:95], v[128:131], a[44:47], v[80:95]
ds_read_b64_tr_b16 v[148:149], v195 offset:28672
ds_read_b64_tr_b16 v[150:151], v195 offset:30720
v_mfma_f32_32x32x16_bf16 v[112:127], v[132:135], a[44:47], v[112:127]
v_mfma_f32_32x32x16_bf16 v[96:111], v[132:135], a[40:43], v[96:111]
s_waitcnt vmcnt(11) lgkmcnt(4)
v_mfma_f32_32x32x16_bf16 v[64:79], v[136:139], a[48:51], v[64:79]
s_barrier
ds_read_b64_tr_b16 v[128:129], v194 offset:32768
ds_read_b64_tr_b16 v[130:131], v194 offset:34816
v_mfma_f32_32x32x16_bf16 v[80:95], v[136:139], a[52:55], v[80:95]
ds_read_b64_tr_b16 v[132:133], v195 offset:32768
ds_read_b64_tr_b16 v[134:135], v195 offset:34816
v_mfma_f32_32x32x16_bf16 v[112:127], v[140:143], a[52:55], v[112:127]
s_add_u32 s64, s4, 147584
s_add_u32 m0, s72, 0
s_add_u32 s65, s64, 9216
v_mfma_f32_32x32x16_bf16 v[96:111], v[140:143], a[48:51], v[96:111]
buffer_load_dwordx4 v192, s[24:27], s64 offen lds
s_waitcnt vmcnt(9) lgkmcnt(4)
v_mfma_f32_32x32x16_bf16 v[64:79], v[144:147], a[56:59], v[64:79]
ds_read_b64_tr_b16 v[136:137], v194 offset:36864
ds_read_b64_tr_b16 v[138:139], v194 offset:38912
v_mfma_f32_32x32x16_bf16 v[80:95], v[144:147], a[60:63], v[80:95]
ds_read_b64_tr_b16 v[140:141], v195 offset:36864
ds_read_b64_tr_b16 v[142:143], v195 offset:38912
v_mfma_f32_32x32x16_bf16 v[112:127], v[148:151], a[60:63], v[112:127]
buffer_load_dwordx4 v192, s[12:15], s64 offen offset:1024 lds
s_add_u32 m0, s72, 2048
v_mfma_f32_32x32x16_bf16 v[96:111], v[148:151], a[56:59], v[96:111]
buffer_load_dwordx4 v192, s[24:27], s65 offen lds
; ==== b1 (1 iteration(s))
s_waitcnt lgkmcnt(4)
v_mfma_f32_32x32x16_bf16 v[0:15], v[128:131], a[64:67], 0
ds_read_b64_tr_b16 v[144:145], v194 offset:40960
ds_read_b64_tr_b16 v[146:147], v194 offset:43008
v_mfma_f32_32x32x16_bf16 v[16:31], v[128:131], a[68:71], 0
ds_read_b64_tr_b16 v[148:149], v195 offset:40960
ds_read_b64_tr_b16 v[150:151], v195 offset:43008
v_mfma_f32_32x32x16_bf16 v[48:63], v[132:135], a[68:71], 0
buffer_load_dwordx4 v192, s[12:15], s65 offen offset:1024 lds
v_mfma_f32_32x32x16_bf16 v[32:47], v[132:135], a[64:67], 0
ds_write_b128 v203, v[80:83]
s_waitcnt lgkmcnt(5)
v_mfma_f32_32x32x16_bf16 v[0:15], v[136:139], a[72:75], v[0:15]
ds_read_b64_tr_b16 v[128:129], v194 offset:45056
ds_read_b64_tr_b16 v[130:131], v194 offset:47104
v_mfma_f32_32x32x16_bf16 v[16:31], v[136:139], a[76:79], v[16:31]
ds_read_b64_tr_b16 v[132:133], v195 offset:45056
ds_read_b64_tr_b16 v[134:135], v195 offset:47104
v_mfma_f32_32x32x16_bf16 v[48:63], v[140:143], a[76:79], v[48:63]
v_mfma_f32_32x32x16_bf16 v[32:47], v[140:143], a[72:75], v[32:47]
ds_write_b128 v203, v[84:87] offset:1024
s_waitcnt lgkmcnt(6)
v_mfma_f32_32x32x16_bf16 v[0:15], v[144:147], a[80:83], v[0:15]
s_barrier
ds_read_b64_tr_b16 v[136:137], v196 offset:49152
ds_read_b64_tr_b16 v[138:139], v196 offset:51200
v_mfma_f32_32x32x16_bf16 v[16:31], v[144:147], a[84:87], v[16:31]
ds_read_b64_tr_b16 v[140:141], v197 offset:49152
ds_read_b64_tr_b16 v[142:143], v197 offset:51200
v_mfma_f32_32x32x16_bf16 v[48:63], v[148:151], a[84:87], v[48:63]
s_add_u32 s64, s4, 516224
s_add_u32 m0, s72, 16384
s_add_u32 s65, s64, 9216
v_mfma_f32_32x32x16_bf16 v[32:47], v[148:151], a[80:83], v[32:47]
buffer_load_dwordx4 v192, s[24:27], s64 offen lds
s_waitcnt lgkmcnt(5)
v_mfma_f32_32x32x16_bf16 v[0:15], v[128:131], a[88:91], v[0:15]
ds_read_b64_tr_b16 v[144:145], v196 offset:53248
ds_read_b64_tr_b16 v[146:147], v196 offset:55296
ds_write_b128 v203, v[88:91] offset:2048
v_mfma_f32_32x32x16_bf16 v[16:31], v[128:131], a[92:95], v[16:31]
ds_read_b64_tr_b16 v[148:149], v197 offset:53248
ds_read_b64_tr_b16 v[150:151], v197 offset:55296
v_mfma_f32_32x32x16_bf16 v[48:63], v[132:135], a[92:95], v[48:63]
buffer_load_dwordx4 v192, s[12:15], s64 offen offset:1024 lds
s_add_u32 m0, s72, 18432
v_mfma_f32_32x32x16_bf16 v[32:47], v[132:135], a[88:91], v[32:47]
buffer_load_dwordx4 v192, s[24:27], s65 offen lds
s_waitcnt lgkmcnt(5)
v_mfma_f32_32x32x16_bf16 v[0:15], v[136:139], a[96:99], v[0:15]
ds_read_b64_tr_b16 v[128:129], v196 offset:57344
ds_read_b64_tr_b16 v[130:131], v196 offset:59392
v_mfma_f32_32x32x16_bf16 v[16:31], v[136:139], a[100:103], v[16:31]
ds_read_b64_tr_b16 v[132:133], v197 offset:57344
ds_read_b64_tr_b16 v[134:135], v197 offset:59392
ds_write_b128 v203, v[92:95] offset:3072
v_mfma_f32_32x32x16_bf16 v[48:63], v[140:143], a[100:103], v[48:63]
buffer_load_dwordx4 v192, s[12:15], s65 offen offset:1024 lds
v_mfma_f32_32x32x16_bf16 v[32:47], v[140:143], a[96:99], v[32:47]
s_waitcnt lgkmcnt(5)
v_mfma_f32_32x32x16_bf16 v[0:15], v[144:147], a[104:107], v[0:15]
ds_read_b64_tr_b16 v[136:137], v196 offset:61440
ds_read_b64_tr_b16 v[138:139], v196 offset:63488
v_mfma_f32_32x32x16_bf16 v[16:31], v[144:147], a[108:111], v[16:31]
ds_read_b64_tr_b16 v[140:141], v197 offset:61440
ds_read_b64_tr_b16 v[142:143], v197 offset:63488
v_mfma_f32_32x32x16_bf16 v[48:63], v[148:151], a[108:111], v[48:63]
ds_write_b128 v203, v[112:115] offset:4096
v_mfma_f32_32x32x16_bf16 v[32:47], v[148:151], a[104:107], v[32:47]
s_waitcnt lgkmcnt(6)
v_mfma_f32_32x32x16_bf16 v[0:15], v[128:131], a[112:115], v[0:15]
s_waitcnt vmcnt(12)
s_barrier
ds_read_b64_tr_b16 v[144:145], v194 offset:49152
ds_read_b64_tr_b16 v[146:147], v194 offset:51200
v_mfma_f32_32x32x16_bf16 v[16:31], v[128:131], a[116:119], v[16:31]
ds_read_b64_tr_b16 v[148:149], v195 offset:49152
ds_read_b64_tr_b16 v[150:151], v195 offset:51200
v_mfma_f32_32x32x16_bf16 v[48:63], v[132:135], a[116:119], v[48:63]
s_add_u32 s64, s4, 221312
s_add_u32 m0, s72, 32768
s_add_u32 s65, s64, 9216
ds_write_b128 v203, v[116:119] offset:5120
v_mfma_f32_32x32x16_bf16 v[32:47], v[132:135], a[112:115], v[32:47]
buffer_load_dwordx4 v192, s[24:27], s64 offen lds
s_waitcnt lgkmcnt(6)
v_mfma_f32_32x32x16_bf16 v[0:15], v[136:139], a[120:123], v[0:15]
ds_read_b64_tr_b16 v[128:129], v194 offset:53248
ds_read_b64_tr_b16 v[130:131], v194 offset:55296
v_mfma_f32_32x32x16_bf16 v[16:31], v[136:139], a[124:127], v[16:31]
ds_read_b64_tr_b16 v[132:133], v195 offset:53248
ds_read_b64_tr_b16 v[134:135], v195 offset:55296
v_mfma_f32_32x32x16_bf16 v[48:63], v[140:143], a[124:127], v[48:63]
buffer_load_dwordx4 v192, s[12:15], s64 offen offset:1024 lds
s_add_u32 m0, s72, 34816
v_mfma_f32_32x32x16_bf16 v[32:47], v[140:143], a[120:123], v[32:47]
buffer_load_dwordx4 v192, s[24:27], s65 offen lds
ds_write_b128 v203, v[120:123] offset:6144
s_waitcnt lgkmcnt(6)
v_mfma_f32_32x32x16_bf16 v[0:15], v[144:147], a[128:131], v[0:15]
ds_read_b64_tr_b16 v[136:137], v194 offset:57344
ds_read_b64_tr_b16 v[138:139], v194 offset:59392
v_mfma_f32_32x32x16_bf16 v[16:31], v[144:147], a[132:135], v[16:31]
ds_read_b64_tr_b16 v[140:141], v195 offset:57344
ds_read_b64_tr_b16 v[142:143], v195 offset:59392
v_mfma_f32_32x32x16_bf16 v[48:63], v[148:151], a[132:135], v[48:63]
buffer_load_dwordx4 v192, s[12:15], s65 offen offset:1024 lds
v_mfma_f32_32x32x16_bf16 v[32:47], v[148:151], a[128:131], v[32:47]
ds_write_b128 v203, v[124:127] offset:7168
s_waitcnt lgkmcnt(6)
v_mfma_f32_32x32x16_bf16 v[0:15], v[128:131], a[136:139], v[0:15]
ds_read_b64_tr_b16 v[144:145], v194 offset:61440
ds_read_b64_tr_b16 v[146:147], v194 offset:63488
v_mfma_f32_32x32x16_bf16 v[16:31], v[128:131], a[140:143], v[16:31]
ds_read_b64_tr_b16 v[148:149], v195 offset:61440
ds_read_b64_tr_b16 v[150:151], v195 offset:63488
v_mfma_f32_32x32x16_bf16 v[48:63], v[132:135], a[140:143], v[48:63]
v_mfma_f32_32x32x16_bf16 v[32:47], v[132:135], a[136:139], v[32:47]
s_waitcnt lgkmcnt(5)
v_mfma_f32_32x32x16_bf16 v[0:15], v[136:139], a[144:147], v[0:15]
s_waitcnt vmcnt(12) lgkmcnt(4)
s_barrier
ds_read_b64_tr_b16 v[128:129], v196 offset:32768
ds_read_b64_tr_b16 v[130:131], v196 offset:34816
v_mfma_f32_32x32x16_bf16 v[16:31], v[136:139], a[148:151], v[16:31]
ds_read_b64_tr_b16 v[132:133], v197 offset:32768
ds_read_b64_tr_b16 v[134:135], v197 offset:34816
v_mfma_f32_32x32x16_bf16 v[48:63], v[140:143], a[148:151], v[48:63]
s_add_u32 s64, s4, 589952
s_add_u32 m0, s72, 81920
s_add_u32 s65, s64, 9216
v_mfma_f32_32x32x16_bf16 v[32:47], v[140:143], a[144:147], v[32:47]
buffer_load_dwordx4 v192, s[24:27], s64 offen lds
s_waitcnt lgkmcnt(4)
v_mfma_f32_32x32x16_bf16 v[0:15], v[144:147], a[152:155], v[0:15]
ds_read_b64_tr_b16 v[136:137], v196 offset:36864
ds_read_b64_tr_b16 v[138:139], v196 offset:38912
v_mfma_f32_32x32x16_bf16 v[16:31], v[144:147], a[156:159], v[16:31]
ds_read_b64_tr_b16 v[140:141], v197 offset:36864
ds_read_b64_tr_b16 v[142:143], v197 offset:38912
v_mfma_f32_32x32x16_bf16 v[48:63], v[148:151], a[156:159], v[48:63]
buffer_load_dwordx4 v192, s[12:15], s64 offen offset:1024 lds
s_add_u32 m0, s72, 83968
s_add_u32 s85, s8, 0
v_mfma_f32_32x32x16_bf16 v[32:47], v[148:151], a[152:155], v[32:47]
buffer_load_dwordx4 v192, s[24:27], s65 offen lds
s_lshl_b32 s85, s85, 13
s_waitcnt lgkmcnt(4)
v_mfma_f32_32x32x16_bf16 v[0:15], v[128:131], a[160:163], v[0:15]
ds_read_b64_tr_b16 v[144:145], v196 offset:40960
ds_read_b64_tr_b16 v[146:147], v196 offset:43008
v_mfma_f32_32x32x16_bf16 v[16:31], v[128:131], a[164:167], v[16:31]
ds_read_b64_tr_b16 v[148:149], v197 offset:40960
ds_read_b64_tr_b16 v[150:151], v197 offset:43008
ds_read_b128 v[152:155], v204
v_mfma_f32_32x32x16_bf16 v[48:63], v[132:135], a[164:167], v[48:63]
buffer_load_dwordx4 v192, s[12:15], s65 offen offset:1024 lds
ds_read_b128 v[156:159], v204 offset:1024
v_mfma_f32_32x32x16_bf16 v[32:47], v[132:135], a[160:163], v[32:47]
s_waitcnt lgkmcnt(1)
v_add_f32 v152, v152, v64
v_add_f32 v153, v153, v65
v_mfma_f32_32x32x16_bf16 v[0:15], v[136:139], a[168:171], v[0:15]
ds_read_b64_tr_b16 v[128:129], v196 offset:45056
ds_read_b64_tr_b16 v[130:131], v196 offset:47104
v_add_f32 v154, v154, v66
v_mfma_f32_32x32x16_bf16 v[16:31], v[136:139], a[172:175], v[16:31]
ds_read_b64_tr_b16 v[132:133], v197 offset:45056
ds_read_b64_tr_b16 v[134:135], v197 offset:47104
v_add_f32 v155, v155, v67
v_mfma_f32_32x32x16_bf16 v[48:63], v[140:143], a[172:175], v[48:63]
s_waitcnt lgkmcnt(4)
v_add_f32 v156, v156, v68
v_add_f32 v157, v157, v69
v_mfma_f32_32x32x16_bf16 v[32:47], v[140:143], a[168:171], v[32:47]
v_add_f32 v158, v158, v70
v_mfma_f32_32x32x16_bf16 v[0:15], v[144:147], a[176:179], v[0:15]
s_waitcnt vmcnt(12)
s_barrier
ds_read_b64_tr_b16 v[136:137], v194
ds_read_b64_tr_b16 v[138:139], v194 offset:2048
v_add_f32 v159, v159, v71
v_mfma_f32_32x32x16_bf16 v[16:31], v[144:147], a[180:183], v[16:31]
ds_read_b64_tr_b16 v[140:141], v195
ds_read_b64_tr_b16 v[142:143], v195 offset:2048
v_cvt_pk_bf16_f32 v170, v152, v153
v_mfma_f32_32x32x16_bf16 v[48:63], v[148:151], a[180:183], v[48:63]
s_add_u32 s64, s4, 295040
s_add_u32 m0, s72, 49152
s_add_u32 s65, s64, 9216
v_cvt_pk_bf16_f32 v171, v154, v155
v_mfma_f32_32x32x16_bf16 v[32:47], v[148:151], a[176:179], v[32:47]
buffer_load_dwordx4 v192, s[24:27], s64 offen lds
v_cvt_pk_bf16_f32 v172, v156, v157
s_waitcnt lgkmcnt(4)
v_mfma_f32_32x32x16_bf16 v[0:15], v[128:131], a[184:187], v[0:15]
ds_read_b64_tr_b16 v[144:145], v194 offset:4096
ds_read_b64_tr_b16 v[146:147], v194 offset:6144
v_cvt_pk_bf16_f32 v173, v158, v159
v_mfma_f32_32x32x16_bf16 v[16:31], v[128:131], a[188:191], v[16:31]
ds_read_b64_tr_b16 v[148:149], v195 offset:4096
ds_read_b64_tr_b16 v[150:151], v195 offset:6144
v_mfma_f32_32x32x16_bf16 v[48:63], v[132:135], a[188:191], v[48:63]
buffer_load_dwordx4 v192, s[12:15], s64 offen offset:1024 lds
s_add_u32 m0, s72, 51200
buffer_store_dwordx4 v[170:173], v202, s[40:43], s85 offen sc1
v_mfma_f32_32x32x16_bf16 v[32:47], v[132:135], a[184:187], v[32:47]
buffer_load_dwordx4 v192, s[24:27], s65 offen lds
s_waitcnt lgkmcnt(4)
v_mfma_f32_32x32x16_bf16 v[0:15], v[136:139], a[192:195], v[0:15]
ds_read_b64_tr_b16 v[128:129], v194 offset:8192
ds_read_b64_tr_b16 v[130:131], v194 offset:10240
ds_read_b128 v[160:163], v204 offset:2048
v_mfma_f32_32x32x16_bf16 v[16:31], v[136:139], a[196:199], v[16:31]
ds_read_b64_tr_b16 v[132:133], v195 offset:8192
ds_read_b64_tr_b16 v[134:135], v195 offset:10240
v_mfma_f32_32x32x16_bf16 v[48:63], v[140:143], a[196:199], v[48:63]
buffer_load_dwordx4 v192, s[12:15], s65 offen offset:1024 lds
ds_read_b128 v[164:167], v204 offset:3072
v_mfma_f32_32x32x16_bf16 v[32:47], v[140:143], a[192:195], v[32:47]
s_waitcnt lgkmcnt(3)
v_add_f32 v160, v160, v72
v_add_f32 v161, v161, v73
v_mfma_f32_32x32x16_bf16 v[0:15], v[144:147], a[200:203], v[0:15]
ds_read_b64_tr_b16 v[136:137], v194 offset:12288
ds_read_b64_tr_b16 v[138:139], v194 offset:14336
v_add_f32 v162, v162, v74
v_mfma_f32_32x32x16_bf16 v[16:31], v[144:147], a[204:207], v[16:31]
ds_read_b64_tr_b16 v[140:141], v195 offset:12288
ds_read_b64_tr_b16 v[142:143], v195 offset:14336
v_add_f32 v163, v163, v75
v_mfma_f32_32x32x16_bf16 v[48:63], v[148:151], a[204:207], v[48:63]
s_waitcnt lgkmcnt(4)
v_add_f32 v164, v164, v76
v_add_f32 v165, v165, v77
v_mfma_f32_32x32x16_bf16 v[32:47], v[148:151], a[200:203], v[32:47]
v_add_f32 v166, v166, v78
v_mfma_f32_32x32x16_bf16 v[0:15], v[128:131], a[208:211], v[0:15]
s_waitcnt vmcnt(13)
s_barrier
ds_read_b64_tr_b16 v[144:145], v194 offset:16384
ds_read_b64_tr_b16 v[146:147], v194 offset:18432
v_add_f32 v167, v167, v79
v_mfma_f32_32x32x16_bf16 v[16:31], v[128:131], a[212:215], v[16:31]
ds_read_b64_tr_b16 v[148:149], v195 offset:16384
ds_read_b64_tr_b16 v[150:151], v195 offset:18432
v_cvt_pk_bf16_f32 v174, v160, v161
v_mfma_f32_32x32x16_bf16 v[48:63], v[132:135], a[212:215], v[48:63]
s_add_u32 s64, s6, 0
s_add_u32 m0, s70, 65536
v_cvt_pk_bf16_f32 v175, v162, v163
v_mfma_f32_32x32x16_bf16 v[32:47], v[132:135], a[208:211], v[32:47]
buffer_load_dwordx4 v193, s[24:27], s64 offen lds
v_cvt_pk_bf16_f32 v176, v164, v165
s_waitcnt lgkmcnt(4)
v_mfma_f32_32x32x16_bf16 v[0:15], v[136:139], a[216:219], v[0:15]
ds_read_b64_tr_b16 v[128:129], v194 offset:20480
ds_read_b64_tr_b16 v[130:131], v194 offset:22528
v_cvt_pk_bf16_f32 v177, v166, v167
v_mfma_f32_32x32x16_bf16 v[16:31], v[136:139], a[220:223], v[16:31]
ds_read_b64_tr_b16 v[132:133], v195 offset:20480
ds_read_b64_tr_b16 v[134:135], v195 offset:22528
v_mfma_f32_32x32x16_bf16 v[48:63], v[140:143], a[220:223], v[48:63]
buffer_load_dwordx4 v193, s[12:15], s64 offen offset:1024 lds
buffer_store_dwordx4 v[174:177], v202, s[40:43], s85 offen offset:1024 sc1
v_mfma_f32_32x32x16_bf16 v[32:47], v[140:143], a[216:219], v[32:47]
s_add_u32 s85, s85, 8192
s_waitcnt lgkmcnt(4)
v_mfma_f32_32x32x16_bf16 v[0:15], v[144:147], a[224:227], v[0:15]
ds_read_b64_tr_b16 v[136:137], v194 offset:24576
ds_read_b64_tr_b16 v[138:139], v194 offset:26624
ds_read_b128 v[152:155], v204 offset:4096
v_mfma_f32_32x32x16_bf16 v[16:31], v[144:147], a[228:231], v[16:31]
ds_read_b64_tr_b16 v[140:141], v195 offset:24576
ds_read_b64_tr_b16 v[142:143], v195 offset:26624
ds_read_b128 v[156:159], v204 offset:5120
v_mfma_f32_32x32x16_bf16 v[48:63], v[148:151], a[228:231], v[48:63]
s_waitcnt lgkmcnt(3)
v_add_f32 v152, v152, v96
v_add_f32 v153, v153, v97
v_mfma_f32_32x32x16_bf16 v[32:47], v[148:151], a[224:227], v[32:47]
v_add_f32 v154, v154, v98
v_mfma_f32_32x32x16_bf16 v[0:15], v[128:131], a[232:235], v[0:15]
ds_read_b64_tr_b16 v[144:145], v194 offset:28672
ds_read_b64_tr_b16 v[146:147], v194 offset:30720
v_add_f32 v155, v155, v99
v_mfma_f32_32x32x16_bf16 v[16:31], v[128:131], a[236:239], v[16:31]
ds_read_b64_tr_b16 v[148:149], v195 offset:28672
ds_read_b64_tr_b16 v[150:151], v195 offset:30720
s_waitcnt lgkmcnt(4)
v_add_f32 v156, v156, v100
v_mfma_f32_32x32x16_bf16 v[48:63], v[132:135], a[236:239], v[48:63]
v_add_f32 v157, v157, v101
v_add_f32 v158, v158, v102
v_mfma_f32_32x32x16_bf16 v[32:47], v[132:135], a[232:235], v[32:47]
v_add_f32 v159, v159, v103
v_cvt_pk_bf16_f32 v170, v152, v153
v_mfma_f32_32x32x16_bf16 v[0:15], v[136:139], a[240:243], v[0:15]
s_waitcnt vmcnt(12)
s_barrier
ds_read_b64_tr_b16 v[128:129], v194 offset:32768
ds_read_b64_tr_b16 v[130:131], v194 offset:34816
v_mfma_f32_32x32x16_bf16 v[16:31], v[136:139], a[244:247], v[16:31]
ds_read_b64_tr_b16 v[132:133], v195 offset:32768
ds_read_b64_tr_b16 v[134:135], v195 offset:34816
v_cvt_pk_bf16_f32 v171, v154, v155
v_mfma_f32_32x32x16_bf16 v[48:63], v[140:143], a[244:247], v[48:63]
s_add_u32 s64, s6, 73728
s_add_u32 m0, s70, 0
v_cvt_pk_bf16_f32 v172, v156, v157
v_mfma_f32_32x32x16_bf16 v[32:47], v[140:143], a[240:243], v[32:47]
buffer_load_dwordx4 v193, s[24:27], s64 offen lds
v_cvt_pk_bf16_f32 v173, v158, v159
s_waitcnt lgkmcnt(4)
v_mfma_f32_32x32x16_bf16 v[0:15], v[144:147], a[248:251], v[0:15]
ds_read_b64_tr_b16 v[136:137], v194 offset:36864
ds_read_b64_tr_b16 v[138:139], v194 offset:38912
buffer_store_dwordx4 v[170:173], v202, s[40:43], s85 offen sc1
v_mfma_f32_32x32x16_bf16 v[16:31], v[144:147], a[252:255], v[16:31]
ds_read_b64_tr_b16 v[140:141], v195 offset:36864
ds_read_b64_tr_b16 v[142:143], v195 offset:38912
v_mfma_f32_32x32x16_bf16 v[48:63], v[148:151], a[252:255], v[48:63]
buffer_load_dwordx4 v193, s[12:15], s64 offen offset:1024 lds
ds_read_b128 v[160:163], v204 offset:6144
v_mfma_f32_32x32x16_bf16 v[32:47], v[148:151], a[248:251], v[32:47]
ds_read_b128 v[164:167], v204 offset:7168
s_waitcnt lgkmcnt(6)
v_mfma_f32_32x32x16_bf16 v[0:15], v[128:131], v[224:227], v[0:15]
ds_read_b64_tr_b16 v[144:145], v194 offset:40960
ds_read_b64_tr_b16 v[146:147], v194 offset:43008
s_waitcnt lgkmcnt(3)
v_add_f32 v160, v160, v104
v_mfma_f32_32x32x16_bf16 v[16:31], v[128:131], v[228:231], v[16:31]
ds_read_b64_tr_b16 v[148:149], v195 offset:40960
ds_read_b64_tr_b16 v[150:151], v195 offset:43008
v_add_f32 v161, v161, v105
v_mfma_f32_32x32x16_bf16 v[48:63], v[132:135], v[228:231], v[48:63]
v_add_f32 v162, v162, v106
v_add_f32 v163, v163, v107
v_mfma_f32_32x32x16_bf16 v[32:47], v[132:135], v[224:227], v[32:47]
s_waitcnt lgkmcnt(4)
v_add_f32 v164, v164, v108
v_mfma_f32_32x32x16_bf16 v[0:15], v[136:139], v[232:235], v[0:15]
ds_read_b64_tr_b16 v[128:129], v194 offset:45056
ds_read_b64_tr_b16 v[130:131], v194 offset:47104
v_add_f32 v165, v165, v109
v_add_f32 v166, v166, v110
v_mfma_f32_32x32x16_bf16 v[16:31], v[136:139], v[236:239], v[16:31]
ds_read_b64_tr_b16 v[132:133], v195 offset:45056
ds_read_b64_tr_b16 v[134:135], v195 offset:47104
v_add_f32 v167, v167, v111
v_mfma_f32_32x32x16_bf16 v[48:63], v[140:143], v[236:239], v[48:63]
v_cvt_pk_bf16_f32 v174, v160, v161
v_mfma_f32_32x32x16_bf16 v[32:47], v[140:143], v[232:235], v[32:47]
v_cvt_pk_bf16_f32 v175, v162, v163
v_cvt_pk_bf16_f32 v176, v164, v165
s_waitcnt lgkmcnt(4)
v_mfma_f32_32x32x16_bf16 v[0:15], v[144:147], v[240:243], v[0:15]
s_waitcnt vmcnt(11)
s_barrier
ds_read_b64_tr_b16 v[136:137], v196 offset:49152
ds_read_b64_tr_b16 v[138:139], v196 offset:51200
v_mfma_f32_32x32x16_bf16 v[16:31], v[144:147], v[244:247], v[16:31]
ds_read_b64_tr_b16 v[140:141], v197 offset:49152
ds_read_b64_tr_b16 v[142:143], v197 offset:51200
v_cvt_pk_bf16_f32 v177, v166, v167
v_mfma_f32_32x32x16_bf16 v[48:63], v[148:151], v[244:247], v[48:63]
s_add_u32 s64, s6, 368640
s_add_u32 m0, s70, 16384
buffer_store_dwordx4 v[174:177], v202, s[40:43], s85 offen offset:1024 sc1
v_mfma_f32_32x32x16_bf16 v[32:47], v[148:151], v[240:243], v[32:47]
buffer_load_dwordx4 v193, s[24:27], s64 offen lds
s_waitcnt lgkmcnt(4)
v_mfma_f32_32x32x16_bf16 v[0:15], v[128:131], v[248:251], v[0:15]
ds_read_b64_tr_b16 v[144:145], v196 offset:53248
ds_read_b64_tr_b16 v[146:147], v196 offset:55296
s_lshr_b32 s94, s22, 3
v_mfma_f32_32x32x16_bf16 v[16:31], v[128:131], v[252:255], v[16:31]
ds_read_b64_tr_b16 v[148:149], v197 offset:53248
ds_read_b64_tr_b16 v[150:151], v197 offset:55296
s_mul_i32 s94, s94, 81
v_mfma_f32_32x32x16_bf16 v[48:63], v[132:135], v[252:255], v[48:63]
buffer_load_dwordx4 v193, s[12:15], s64 offen offset:1024 lds
s_mul_i32 s87, s58, 21
v_mfma_f32_32x32x16_bf16 v[32:47], v[132:135], v[248:251], v[32:47]
s_add_u32 s94, s94, s87
v_add_u32 v222, s94, v178
s_waitcnt lgkmcnt(4)
v_mfma_f32_32x32x16_bf16 v[0:15], v[136:139], a[0:3], v[0:15]
ds_read_b64_tr_b16 v[128:129], v196 offset:57344
ds_read_b64_tr_b16 v[130:131], v196 offset:59392
v_lshlrev_b32 v222, 7, v222
v_mfma_f32_32x32x16_bf16 v[16:31], v[136:139], a[4:7], v[16:31]
ds_read_b64_tr_b16 v[132:133], v197 offset:57344
ds_read_b64_tr_b16 v[134:135], v197 offset:59392
v_mfma_f32_32x32x16_bf16 v[48:63], v[140:143], a[4:7], v[48:63]
v_cmp_gt_u32 vcc, 21, v178
v_mov_b32 v223, 2113929216
s_nop 0
v_cndmask_b32 v222, v223, v222, vcc
v_mfma_f32_32x32x16_bf16 v[32:47], v[140:143], a[0:3], v[32:47]
s_waitcnt lgkmcnt(4)
v_mfma_f32_32x32x16_bf16 v[0:15], v[144:147], a[8:11], v[0:15]
ds_read_b64_tr_b16 v[136:137], v196 offset:61440
ds_read_b64_tr_b16 v[138:139], v196 offset:63488
buffer_load_dword v191, v222, s[52:55], 0 offen
v_mfma_f32_32x32x16_bf16 v[16:31], v[144:147], a[12:15], v[16:31]
ds_read_b64_tr_b16 v[140:141], v197 offset:61440
ds_read_b64_tr_b16 v[142:143], v197 offset:63488
v_lshlrev_b32 v208, 3, v178
v_mfma_f32_32x32x16_bf16 v[48:63], v[148:151], a[12:15], v[48:63]
v_add_u32 v208, 33792, v208
v_mfma_f32_32x32x16_bf16 v[32:47], v[148:151], a[8:11], v[32:47]
buffer_load_dwordx2 v[206:207], v208, s[40:43], 0 offen sc1
s_waitcnt lgkmcnt(4)
v_mfma_f32_32x32x16_bf16 v[0:15], v[128:131], a[16:19], v[0:15]
s_waitcnt vmcnt(11)
s_barrier
ds_read_b64_tr_b16 v[144:145], v194 offset:49152
ds_read_b64_tr_b16 v[146:147], v194 offset:51200
v_mfma_f32_32x32x16_bf16 v[16:31], v[128:131], a[20:23], v[16:31]
ds_read_b64_tr_b16 v[148:149], v195 offset:49152
ds_read_b64_tr_b16 v[150:151], v195 offset:51200
v_lshlrev_b32 v212, 3, v178
v_mfma_f32_32x32x16_bf16 v[48:63], v[132:135], a[20:23], v[48:63]
s_add_u32 s64, s6, 442368
s_add_u32 m0, s70, 32768
v_add_u32 v212, 34304, v212
v_mfma_f32_32x32x16_bf16 v[32:47], v[132:135], a[16:19], v[32:47]
buffer_load_dwordx4 v193, s[24:27], s64 offen lds
buffer_load_dwordx2 v[210:211], v212, s[40:43], 0 offen sc1
s_waitcnt lgkmcnt(4)
v_mfma_f32_32x32x16_bf16 v[0:15], v[136:139], a[24:27], v[0:15]
ds_read_b64_tr_b16 v[128:129], v194 offset:53248
ds_read_b64_tr_b16 v[130:131], v194 offset:55296
v_mfma_f32_32x32x16_bf16 v[16:31], v[136:139], a[28:31], v[16:31]
ds_read_b64_tr_b16 v[132:133], v195 offset:53248
ds_read_b64_tr_b16 v[134:135], v195 offset:55296
v_lshlrev_b32 v216, 3, v178
v_mfma_f32_32x32x16_bf16 v[48:63], v[140:143], a[28:31], v[48:63]
buffer_load_dwordx4 v193, s[12:15], s64 offen offset:1024 lds
v_add_u32 v216, 34816, v216
v_mfma_f32_32x32x16_bf16 v[32:47], v[140:143], a[24:27], v[32:47]
buffer_load_dwordx2 v[214:215], v216, s[40:43], 0 offen sc1
s_waitcnt lgkmcnt(4)
v_mfma_f32_32x32x16_bf16 v[0:15], v[144:147], a[32:35], v[0:15]
ds_read_b64_tr_b16 v[136:137], v194 offset:57344
ds_read_b64_tr_b16 v[138:139], v194 offset:59392
v_mfma_f32_32x32x16_bf16 v[16:31], v[144:147], a[36:39], v[16:31]
ds_read_b64_tr_b16 v[140:141], v195 offset:57344
ds_read_b64_tr_b16 v[142:143], v195 offset:59392
v_lshlrev_b32 v220, 3, v178
v_mfma_f32_32x32x16_bf16 v[48:63], v[148:151], a[36:39], v[48:63]
v_add_u32 v220, 35328, v220
v_mfma_f32_32x32x16_bf16 v[32:47], v[148:151], a[32:35], v[32:47]
buffer_load_dwordx2 v[218:219], v220, s[40:43], 0 offen sc1
s_waitcnt lgkmcnt(4)
v_mfma_f32_32x32x16_bf16 v[0:15], v[128:131], a[40:43], v[0:15]
ds_read_b64_tr_b16 v[144:145], v194 offset:61440
ds_read_b64_tr_b16 v[146:147], v194 offset:63488
v_mfma_f32_32x32x16_bf16 v[16:31], v[128:131], a[44:47], v[16:31]
ds_read_b64_tr_b16 v[148:149], v195 offset:61440
ds_read_b64_tr_b16 v[150:151], v195 offset:63488
v_mfma_f32_32x32x16_bf16 v[48:63], v[132:135], a[44:47], v[48:63]
v_mfma_f32_32x32x16_bf16 v[32:47], v[132:135], a[40:43], v[32:47]
s_waitcnt lgkmcnt(4)
v_mfma_f32_32x32x16_bf16 v[0:15], v[136:139], a[48:51], v[0:15]
s_waitcnt vmcnt(14)
s_barrier
ds_read_b64_tr_b16 v[128:129], v199 offset:32768
ds_read_b64_tr_b16 v[130:131], v199 offset:33280
v_mfma_f32_32x32x16_bf16 v[16:31], v[136:139], a[52:55], v[16:31]
s_add_u32 s64, s6, 147456
s_add_u32 m0, s70, 81920
v_mfma_f32_32x32x16_bf16 v[48:63], v[140:143], a[52:55], v[48:63]
buffer_load_dwordx4 v193, s[24:27], s64 offen lds
v_mfma_f32_32x32x16_bf16 v[32:47], v[140:143], a[48:51], v[32:47]
buffer_load_dwordx4 v193, s[12:15], s64 offen offset:1024 lds
s_waitcnt lgkmcnt(2)
v_mfma_f32_32x32x16_bf16 v[0:15], v[144:147], a[56:59], v[0:15]
ds_read_b64_tr_b16 v[132:133], v199 offset:34816
ds_read_b64_tr_b16 v[134:135], v199 offset:35328
v_mfma_f32_32x32x16_bf16 v[16:31], v[144:147], a[60:63], v[16:31]
v_mfma_f32_32x32x16_bf16 v[48:63], v[148:151], a[60:63], v[48:63]
v_mfma_f32_32x32x16_bf16 v[32:47], v[148:151], a[56:59], v[32:47]
; ==== bsel (1 iteration(s))
s_and_b32 s66, s83, 1
s_cmp_eq_u32 s66, 1
s_cbranch_scc1 L_four
; ==== bs (1 iteration(s))
s_waitcnt lgkmcnt(2)
v_mfma_f32_32x32x16_bf16 v[64:79], v[128:131], a[64:67], 0
ds_read_b64_tr_b16 v[136:137], v199 offset:36864
ds_read_b64_tr_b16 v[138:139], v199 offset:37376
v_mfma_f32_32x32x16_bf16 v[80:95], v[128:131], a[68:71], 0
s_waitcnt vmcnt(7)
v_cmp_eq_u64 vcc, s[62:63], v[206:207]
s_nop 3
s_cmp_eq_u64 vcc, -1
s_cbranch_scc1 L_ok11
s_mov_b32 s17, 0x20000
L_poll10:
s_sleep 1
buffer_load_dwordx2 v[206:207], v208, s[40:43], 0 offen sc1
s_waitcnt vmcnt(0)
v_cmp_eq_u64 vcc, s[62:63], v[206:207]
s_nop 4
s_cmp_eq_u64 vcc, -1
s_cbranch_scc1 L_ok11
s_sub_u32 s17, s17, 1
s_cmp_lg_u32 s17, 0
s_cbranch_scc1 L_poll10
L_ok11:
s_waitcnt vmcnt(5)
v_cmp_eq_u64 vcc, s[62:63], v[210:211]
s_nop 3
s_cmp_eq_u64 vcc, -1
s_cbranch_scc1 L_ok13
s_mov_b32 s17, 0x20000
L_poll12:
s_sleep 1
buffer_load_dwordx2 v[210:211], v212, s[40:43], 0 offen sc1
s_waitcnt vmcnt(0)
v_cmp_eq_u64 vcc, s[62:63], v[210:211]
s_nop 4
s_cmp_eq_u64 vcc, -1
s_cbranch_scc1 L_ok13
s_sub_u32 s17, s17, 1
s_cmp_lg_u32 s17, 0
s_cbranch_scc1 L_poll12
L_ok13:
s_waitcnt vmcnt(3)
v_cmp_eq_u64 vcc, s[62:63], v[214:215]
s_nop 3
s_cmp_eq_u64 vcc, -1
s_cbranch_scc1 L_ok15
s_mov_b32 s17, 0x20000
L_poll14:
s_sleep 1
buffer_load_dwordx2 v[214:215], v216, s[40:43], 0 offen sc1
s_waitcnt vmcnt(0)
v_cmp_eq_u64 vcc, s[62:63], v[214:215]
s_nop 4
s_cmp_eq_u64 vcc, -1
s_cbranch_scc1 L_ok15
s_sub_u32 s17, s17, 1
s_cmp_lg_u32 s17, 0
s_cbranch_scc1 L_poll14
L_ok15:
s_waitcnt lgkmcnt(2)
v_mfma_f32_32x32x16_bf16 v[64:79], v[132:135], a[72:75], v[64:79]
ds_read_b64_tr_b16 v[140:141], v199 offset:38912
ds_read_b64_tr_b16 v[142:143], v199 offset:39424
ds_write_b128 v203, v[16:19]
v_mfma_f32_32x32x16_bf16 v[80:95], v[132:135], a[76:79], v[80:95]
s_waitcnt vmcnt(2)
v_cmp_eq_u64 vcc, s[62:63], v[218:219]
s_nop 3
s_cmp_eq_u64 vcc, -1
s_cbranch_scc1 L_ok17
s_mov_b32 s17, 0x20000
L_poll16:
s_sleep 1
buffer_load_dwordx2 v[218:219], v220, s[40:43], 0 offen sc1
s_waitcnt vmcnt(0)
v_cmp_eq_u64 vcc, s[62:63], v[218:219]
s_nop 4
s_cmp_eq_u64 vcc, -1
s_cbranch_scc1 L_ok17
s_sub_u32 s17, s17, 1
s_cmp_lg_u32 s17, 0
s_cbranch_scc1 L_poll16
L_ok17:
s_and_b32 s10, s57, 3
s_lshr_b32 s11, s57, 2
s_lshl_b32 s66, s58, 2
s_add_u32 s11, s11, s66
v_lshrrev_b32 v219, 2, v178
v_and_b32 v220, 3, v178
s_waitcnt lgkmcnt(3)
v_mfma_f32_32x32x16_bf16 v[64:79], v[136:139], a[80:83], v[64:79]
s_barrier
ds_read_b64_tr_b16 v[144:145], v198
ds_read_b64_tr_b16 v[146:147], v198 offset:512
s_add_u32 s64, s6, 516096
s_add_u32 m0, s70, 49152
v_lshlrev_b32 v218, 12, v219
v_lshl_add_u32 v218, v220, 4, v218
s_lshl_b32 s66, s56, 8
v_mfma_f32_32x32x16_bf16 v[80:95], v[136:139], a[84:87], v[80:95]
buffer_load_dwordx4 v193, s[24:27], s64 offen lds
ds_write_b128 v203, v[20:23] offset:1024
s_lshl_b32 s67, s10, 6
s_add_u32 s66, s66, s67
s_waitcnt lgkmcnt(4)
v_mfma_f32_32x32x16_bf16 v[64:79], v[140:143], a[88:91], v[64:79]
ds_read_b64_tr_b16 v[148:149], v198 offset:2048
ds_read_b64_tr_b16 v[150:151], v198 offset:2560
buffer_load_dwordx4 v193, s[12:15], s64 offen offset:1024 lds
s_mul_i32 s67, s58, 589824
s_add_u32 s66, s66, s67
s_add_u32 s66, s66, 4780032
v_add_u32 v218, s66, v218
v_mfma_f32_32x32x16_bf16 v[80:95], v[140:143], a[92:95], v[80:95]
ds_write_b128 v203, v[24:27] offset:2048
v_lshlrev_b32 v221, 4, v178
s_mul_i32 s16, s11, 36864
s_cmp_gt_u32 s11, 8
s_cselect_b32 s16, 2113929216, s16
s_waitcnt lgkmcnt(4)
v_mfma_f32_32x32x16_bf16 v[64:79], v[144:147], a[96:99], v[64:79]
ds_read_b64_tr_b16 v[128:129], v198 offset:4096
ds_read_b64_tr_b16 v[130:131], v198 offset:4608
v_mfma_f32_32x32x16_bf16 v[80:95], v[144:147], a[100:103], v[80:95]
ds_write_b128 v203, v[28:31] offset:3072
s_waitcnt lgkmcnt(4)
v_mfma_f32_32x32x16_bf16 v[64:79], v[148:151], a[104:107], v[64:79]
s_add_u32 s94, s16, 0
buffer_load_dwordx4 a[64:67], v221, s[52:55], s94 offen
ds_read_b64_tr_b16 v[132:133], v198 offset:6144
ds_read_b64_tr_b16 v[134:135], v198 offset:6656
ds_write_b128 v203, v[48:51] offset:4096
v_mfma_f32_32x32x16_bf16 v[80:95], v[148:151], a[108:111], v[80:95]
s_add_u32 s94, s16, 1024
buffer_load_dwordx4 a[68:71], v221, s[52:55], s94 offen
s_waitcnt lgkmcnt(4)
v_mfma_f32_32x32x16_bf16 v[64:79], v[128:131], a[112:115], v[64:79]
s_add_u32 s94, s16, 2048
buffer_load_dwordx4 a[72:75], v221, s[52:55], s94 offen
s_barrier
ds_read_b64_tr_b16 v[136:137], v198 offset:16384
ds_read_b64_tr_b16 v[138:139], v198 offset:16896
s_add_u32 s64, s6, 221184
s_add_u32 m0, s70, 65536
v_mfma_f32_32x32x16_bf16 v[80:95], v[128:131], a[116:119], v[80:95]
s_add_u32 s94, s16, 3072
buffer_load_dwordx4 a[76:79], v221, s[52:55], s94 offen
buffer_load_dwordx4 v193, s[24:27], s64 offen lds
ds_write_b128 v203, v[52:55] offset:5120
s_waitcnt lgkmcnt(4)
v_mfma_f32_32x32x16_bf16 v[64:79], v[132:135], a[120:123], v[64:79]
s_add_u32 s94, s16, 4096
buffer_load_dwordx4 a[80:83], v221, s[52:55], s94 offen
ds_read_b64_tr_b16 v[140:141], v198 offset:18432
ds_read_b64_tr_b16 v[142:143], v198 offset:18944
buffer_load_dwordx4 v193, s[12:15], s64 offen offset:1024 lds
v_mfma_f32_32x32x16_bf16 v[80:95], v[132:135], a[124:127], v[80:95]
s_add_u32 s94, s16, 5120
buffer_load_dwordx4 a[84:87], v221, s[52:55], s94 offen
ds_write_b128 v203, v[56:59] offset:6144
s_waitcnt lgkmcnt(4)
v_mfma_f32_32x32x16_bf16 v[64:79], v[136:139], a[128:131], v[64:79]
s_add_u32 s94, s16, 6144
buffer_load_dwordx4 a[88:91], v221, s[52:55], s94 offen
ds_read_b64_tr_b16 v[144:145], v198 offset:20480
ds_read_b64_tr_b16 v[146:147], v198 offset:20992
v_mfma_f32_32x32x16_bf16 v[80:95], v[136:139], a[132:135], v[80:95]
s_add_u32 s94, s16, 7168
buffer_load_dwordx4 a[92:95], v221, s[52:55], s94 offen
ds_write_b128 v203, v[60:63] offset:7168
s_waitcnt lgkmcnt(4)
v_mfma_f32_32x32x16_bf16 v[64:79], v[140:143], a[136:139], v[64:79]
s_add_u32 s94, s16, 8192
buffer_load_dwordx4 a[96:99], v221, s[52:55], s94 offen
ds_read_b64_tr_b16 v[148:149], v198 offset:22528
ds_read_b64_tr_b16 v[150:151], v198 offset:23040
v_mfma_f32_32x32x16_bf16 v[80:95], v[140:143], a[140:143], v[80:95]
s_add_u32 s94, s16, 9216
buffer_load_dwordx4 a[100:103], v221, s[52:55], s94 offen
s_waitcnt lgkmcnt(3)
v_mfma_f32_32x32x16_bf16 v[64:79], v[144:147], a[144:147], v[64:79]
s_add_u32 s94, s16, 10240
buffer_load_dwordx4 a[104:107], v221, s[52:55], s94 offen
s_waitcnt lgkmcnt(2)
s_barrier
ds_read_b64_tr_b16 v[128:129], v198 offset:32768
ds_read_b64_tr_b16 v[130:131], v198 offset:33280
s_add_u32 s64, s6, 589824
s_add_u32 m0, s70, 0
v_mfma_f32_32x32x16_bf16 v[80:95], v[144:147], a[148:151], v[80:95]
s_add_u32 s94, s16, 11264
buffer_load_dwordx4 a[108:111], v221, s[52:55], s94 offen
buffer_load_dwordx4 v193, s[24:27], s64 offen lds
s_waitcnt lgkmcnt(2)
v_mfma_f32_32x32x16_bf16 v[64:79], v[148:151], a[152:155], v[64:79]
s_add_u32 s94, s16, 12288
buffer_load_dwordx4 a[112:115], v221, s[52:55], s94 offen
ds_read_b64_tr_b16 v[132:133], v198 offset:34816
ds_read_b64_tr_b16 v[134:135], v198 offset:35328
buffer_load_dwordx4 v193, s[12:15], s64 offen offset:1024 lds
v_mfma_f32_32x32x16_bf16 v[80:95], v[148:151], a[156:159], v[80:95]
s_add_u32 s94, s16, 13312
buffer_load_dwordx4 a[116:119], v221, s[52:55], s94 offen
s_waitcnt lgkmcnt(2)
v_mfma_f32_32x32x16_bf16 v[64:79], v[128:131], a[160:163], v[64:79]
s_add_u32 s94, s16, 14336
buffer_load_dwordx4 a[120:123], v221, s[52:55], s94 offen
ds_read_b64_tr_b16 v[136:137], v198 offset:36864
ds_read_b64_tr_b16 v[138:139], v198 offset:37376
s_add_u32 s85, s8, 2
s_lshl_b32 s85, s85, 13
v_mfma_f32_32x32x16_bf16 v[80:95], v[128:131], a[164:167], v[80:95]
s_add_u32 s94, s16, 15360
buffer_load_dwordx4 a[124:127], v221, s[52:55], s94 offen
ds_read_b128 v[152:155], v204
ds_read_b128 v[156:159], v204 offset:1024
s_waitcnt lgkmcnt(4)
v_mfma_f32_32x32x16_bf16 v[64:79], v[132:135], a[168:171], v[64:79]
s_add_u32 s94, s16, 16384
buffer_load_dwordx4 a[128:131], v221, s[52:55], s94 offen
ds_read_b64_tr_b16 v[140:141], v198 offset:38912
ds_read_b64_tr_b16 v[142:143], v198 offset:39424
s_waitcnt lgkmcnt(3)
v_add_f32 v152, v152, v0
v_add_f32 v153, v153, v1
v_mfma_f32_32x32x16_bf16 v[80:95], v[132:135], a[172:175], v[80:95]
s_add_u32 s94, s16, 17408
buffer_load_dwordx4 a[132:135], v221, s[52:55], s94 offen
v_add_f32 v154, v154, v2
v_add_f32 v155, v155, v3
s_waitcnt lgkmcnt(2)
v_add_f32 v156, v156, v4
v_mfma_f32_32x32x16_bf16 v[64:79], v[136:139], a[176:179], v[64:79]
s_add_u32 s94, s16, 18432
buffer_load_dwordx4 a[136:139], v221, s[52:55], s94 offen
s_waitcnt vmcnt(25)
s_barrier
ds_read_b64_tr_b16 v[144:145], v199 offset:49152
ds_read_b64_tr_b16 v[146:147], v199 offset:49664
s_add_u32 s64, s6, 294912
s_add_u32 m0, s70, 16384
v_mfma_f32_32x32x16_bf16 v[80:95], v[136:139], a[180:183], v[80:95]
s_add_u32 s94, s16, 19456
buffer_load_dwordx4 a[140:143], v221, s[52:55], s94 offen
buffer_load_dwordx4 v193, s[24:27], s64 offen lds
v_add_f32 v157, v157, v5
v_add_f32 v158, v158, v6
s_waitcnt lgkmcnt(2)
v_mfma_f32_32x32x16_bf16 v[64:79], v[140:143], a[184:187], v[64:79]
s_add_u32 s94, s16, 20480
buffer_load_dwordx4 a[144:147], v221, s[52:55], s94 offen
ds_read_b64_tr_b16 v[148:149], v199 offset:51200
ds_read_b64_tr_b16 v[150:151], v199 offset:51712
buffer_load_dwordx4 v193, s[12:15], s64 offen offset:1024 lds
v_mfma_f32_32x32x16_bf16 v[80:95], v[140:143], a[188:191], v[80:95]
s_add_u32 s94, s16, 21504
buffer_load_dwordx4 a[148:151], v221, s[52:55], s94 offen
v_add_f32 v159, v159, v7
v_cvt_pk_bf16_f32 v170, v152, v153
v_cvt_pk_bf16_f32 v171, v154, v155
s_waitcnt lgkmcnt(2)
v_mfma_f32_32x32x16_bf16 v[64:79], v[144:147], a[192:195], v[64:79]
s_add_u32 s94, s16, 22528
buffer_load_dwordx4 a[152:155], v221, s[52:55], s94 offen
ds_read_b64_tr_b16 v[128:129], v199 offset:53248
ds_read_b64_tr_b16 v[130:131], v199 offset:53760
v_cvt_pk_bf16_f32 v172, v156, v157
v_cvt_pk_bf16_f32 v173, v158, v159
v_mfma_f32_32x32x16_bf16 v[80:95], v[144:147], a[196:199], v[80:95]
s_add_u32 s94, s16, 23552
buffer_load_dwordx4 a[156:159], v221, s[52:55], s94 offen
buffer_store_dwordx4 v[170:173], v202, s[40:43], s85 offen sc1
ds_read_b128 v[160:163], v204 offset:2048
s_waitcnt lgkmcnt(3)
v_mfma_f32_32x32x16_bf16 v[64:79], v[148:151], a[200:203], v[64:79]
s_add_u32 s94, s16, 24576
buffer_load_dwordx4 a[160:163], v221, s[52:55], s94 offen
ds_read_b64_tr_b16 v[132:133], v199 offset:55296
ds_read_b64_tr_b16 v[134:135], v199 offset:55808
ds_read_b128 v[164:167], v204 offset:3072
v_mfma_f32_32x32x16_bf16 v[80:95], v[148:151], a[204:207], v[80:95]
s_add_u32 s94, s16, 25600
buffer_load_dwordx4 a[164:167], v221, s[52:55], s94 offen
s_waitcnt lgkmcnt(3)
v_add_f32 v160, v160, v8
v_add_f32 v161, v161, v9
v_add_f32 v162, v162, v10
v_mfma_f32_32x32x16_bf16 v[64:79], v[128:131], a[208:211], v[64:79]
s_add_u32 s94, s16, 26624
buffer_load_dwordx4 a[168:171], v221, s[52:55], s94 offen
s_waitcnt vmcnt(34)
s_barrier
ds_read_b64_tr_b16 v[136:137], v198 offset:49152
ds_read_b64_tr_b16 v[138:139], v198 offset:49664
v_add_f32 v163, v163, v11
v_mfma_f32_32x32x16_bf16 v[80:95], v[128:131], a[212:215], v[80:95]
s_add_u32 s94, s16, 27648
buffer_load_dwordx4 a[172:175], v221, s[52:55], s94 offen
s_waitcnt lgkmcnt(2)
v_add_f32 v164, v164, v12
v_add_f32 v165, v165, v13
v_add_f32 v166, v166, v14
v_mfma_f32_32x32x16_bf16 v[64:79], v[132:135], a[216:219], v[64:79]
s_add_u32 s94, s16, 28672
buffer_load_dwordx4 a[176:179], v221, s[52:55], s94 offen
ds_read_b64_tr_b16 v[140:141], v198 offset:51200
ds_read_b64_tr_b16 v[142:143], v198 offset:51712
v_add_f32 v167, v167, v15
v_cvt_pk_bf16_f32 v174, v160, v161
v_mfma_f32_32x32x16_bf16 v[80:95], v[132:135], a[220:223], v[80:95]
s_add_u32 s94, s16, 29696
buffer_load_dwordx4 a[180:183], v221, s[52:55], s94 offen
v_cvt_pk_bf16_f32 v175, v162, v163
v_cvt_pk_bf16_f32 v176, v164, v165
v_cvt_pk_bf16_f32 v177, v166, v167
s_waitcnt lgkmcnt(2)
v_mfma_f32_32x32x16_bf16 v[64:79], v[136:139], a[224:227], v[64:79]
s_add_u32 s94, s16, 30720
buffer_load_dwordx4 a[184:187], v221, s[52:55], s94 offen
ds_read_b64_tr_b16 v[144:145], v198 offset:53248
ds_read_b64_tr_b16 v[146:147], v198 offset:53760
buffer_store_dwordx4 v[174:177], v202, s[40:43], s85 offen offset:1024 sc1
v_mfma_f32_32x32x16_bf16 v[80:95], v[136:139], a[228:231], v[80:95]
s_add_u32 s94, s16, 31744
buffer_load_dwordx4 a[188:191], v221, s[52:55], s94 offen
s_add_u32 s85, s85, 8192
ds_read_b128 v[152:155], v204 offset:4096
s_waitcnt lgkmcnt(3)
v_mfma_f32_32x32x16_bf16 v[64:79], v[140:143], a[232:235], v[64:79]
s_add_u32 s94, s16, 32768
buffer_load_dwordx4 a[192:195], v221, s[52:55], s94 offen
ds_read_b64_tr_b16 v[148:149], v198 offset:55296
ds_read_b64_tr_b16 v[150:151], v198 offset:55808
ds_read_b128 v[156:159], v204 offset:5120
s_waitcnt lgkmcnt(3)
v_add_f32 v152, v152, v32
v_mfma_f32_32x32x16_bf16 v[80:95], v[140:143], a[236:239], v[80:95]
s_add_u32 s94, s16, 33792
buffer_load_dwordx4 a[196:199], v221, s[52:55], s94 offen
v_add_f32 v153, v153, v33
v_add_f32 v154, v154, v34
v_add_f32 v155, v155, v35
v_mfma_f32_32x32x16_bf16 v[64:79], v[144:147], a[240:243], v[64:79]
s_add_u32 s94, s16, 34816
buffer_load_dwordx4 a[200:203], v221, s[52:55], s94 offen
s_waitcnt vmcnt(36)
s_barrier
ds_read_b64_tr_b16 v[128:129], v199 offset:32768
ds_read_b64_tr_b16 v[130:131], v199 offset:33280
s_waitcnt lgkmcnt(2)
v_add_f32 v156, v156, v36
v_mfma_f32_32x32x16_bf16 v[80:95], v[144:147], a[244:247], v[80:95]
s_add_u32 s94, s16, 35840
buffer_load_dwordx4 a[204:207], v221, s[52:55], s94 offen
v_add_f32 v157, v157, v37
v_add_f32 v158, v158, v38
v_add_f32 v159, v159, v39
v_mfma_f32_32x32x16_bf16 v[64:79], v[148:151], a[248:251], v[64:79]
s_mov_b32 s94, 0
buffer_load_dwordx4 a[208:211], v218, s[40:43], s94 offen sc1
ds_read_b64_tr_b16 v[132:133], v199 offset:34816
ds_read_b64_tr_b16 v[134:135], v199 offset:35328
v_cvt_pk_bf16_f32 v170, v152, v153
v_cvt_pk_bf16_f32 v171, v154, v155
v_mfma_f32_32x32x16_bf16 v[80:95], v[148:151], a[252:255], v[80:95]
s_mov_b32 s94, 65536
buffer_load_dwordx4 a[212:215], v218, s[40:43], s94 offen sc1
v_cvt_pk_bf16_f32 v172, v156, v157
v_cvt_pk_bf16_f32 v173, v158, v159
s_waitcnt lgkmcnt(2)
v_mfma_f32_32x32x16_bf16 v[64:79], v[128:131], v[224:227], v[64:79]
s_mov_b32 s94, 131072
buffer_load_dwordx4 a[216:219], v218, s[40:43], s94 offen sc1
ds_read_b64_tr_b16 v[136:137], v199 offset:36864
ds_read_b64_tr_b16 v[138:139], v199 offset:37376
buffer_store_dwordx4 v[170:173], v202, s[40:43], s85 offen sc1
v_mfma_f32_32x32x16_bf16 v[80:95], v[128:131], v[228:231], v[80:95]
s_mov_b32 s94, 196608
buffer_load_dwordx4 a[220:223], v218, s[40:43], s94 offen sc1
ds_read_b128 v[160:163], v204 offset:6144
ds_read_b128 v[164:167], v204 offset:7168
s_waitcnt lgkmcnt(1)
v_add_f32 v160, v160, v40
v_mfma_f32_32x32x16_bf16 v[64:79], v[132:135], v[232:235], v[64:79]
s_mov_b32 s94, 262144
buffer_load_dwordx4 a[224:227], v218, s[40:43], s94 offen sc1
ds_read_b64_tr_b16 v[140:141], v199 offset:38912
ds_read_b64_tr_b16 v[142:143], v199 offset:39424
v_add_f32 v161, v161, v41
v_add_f32 v162, v162, v42
v_mfma_f32_32x32x16_bf16 v[80:95], v[132:135], v[236:239], v[80:95]
s_mov_b32 s94, 327680
buffer_load_dwordx4 a[228:231], v218, s[40:43], s94 offen sc1
v_add_f32 v163, v163, v43
s_waitcnt lgkmcnt(2)
v_add_f32 v164, v164, v44
v_add_f32 v165, v165, v45
v_mfma_f32_32x32x16_bf16 v[64:79], v[136:139], v[240:243], v[64:79]
s_mov_b32 s94, 393216
buffer_load_dwordx4 a[232:235], v218, s[40:43], s94 offen sc1
s_waitcnt vmcnt(35)
s_barrier
ds_read_b64_tr_b16 v[144:145], v198
ds_read_b64_tr_b16 v[146:147], v198 offset:512
v_add_f32 v166, v166, v46
v_mfma_f32_32x32x16_bf16 v[80:95], v[136:139], v[244:247], v[80:95]
s_mov_b32 s94, 458752
buffer_load_dwordx4 a[236:239], v218, s[40:43], s94 offen sc1
v_add_f32 v167, v167, v47
v_cvt_pk_bf16_f32 v174, v160, v161
v_cvt_pk_bf16_f32 v175, v162, v163
s_waitcnt lgkmcnt(2)
v_mfma_f32_32x32x16_bf16 v[64:79], v[140:143], v[248:251], v[64:79]
s_mov_b32 s94, 524288
buffer_load_dwordx4 a[240:243], v218, s[40:43], s94 offen sc1
ds_read_b64_tr_b16 v[148:149], v198 offset:2048
ds_read_b64_tr_b16 v[150:151], v198 offset:2560
v_cvt_pk_bf16_f32 v176, v164, v165
v_cvt_pk_bf16_f32 v177, v166, v167
v_mfma_f32_32x32x16_bf16 v[80:95], v[140:143], v[252:255], v[80:95]
buffer_store_dwordx4 v[174:177], v202, s[40:43], s85 offen offset:1024 sc1
s_waitcnt lgkmcnt(2)
v_mfma_f32_32x32x16_bf16 v[64:79], v[144:147], a[0:3], v[64:79]
ds_read_b64_tr_b16 v[128:129], v198 offset:4096
ds_read_b64_tr_b16 v[130:131], v198 offset:4608
v_mfma_f32_32x32x16_bf16 v[80:95], v[144:147], a[4:7], v[80:95]
s_waitcnt lgkmcnt(2)
v_mfma_f32_32x32x16_bf16 v[64:79], v[148:151], a[8:11], v[64:79]
ds_read_b64_tr_b16 v[132:133], v198 offset:6144
ds_read_b64_tr_b16 v[134:135], v198 offset:6656
v_mfma_f32_32x32x16_bf16 v[80:95], v[148:151], a[12:15], v[80:95]
s_waitcnt lgkmcnt(2)
v_mfma_f32_32x32x16_bf16 v[64:79], v[128:131], a[16:19], v[64:79]
s_waitcnt vmcnt(28)
s_barrier
ds_read_b64_tr_b16 v[136:137], v198 offset:16384
ds_read_b64_tr_b16 v[138:139], v198 offset:16896
v_mfma_f32_32x32x16_bf16 v[80:95], v[128:131], a[20:23], v[80:95]
s_waitcnt lgkmcnt(2)
v_mfma_f32_32x32x16_bf16 v[64:79], v[132:135], a[24:27], v[64:79]
ds_read_b64_tr_b16 v[140:141], v198 offset:18432
ds_read_b64_tr_b16 v[142:143], v198 offset:18944
v_mfma_f32_32x32x16_bf16 v[80:95], v[132:135], a[28:31], v[80:95]
s_waitcnt lgkmcnt(2)
v_mfma_f32_32x32x16_bf16 v[64:79], v[136:139], a[32:35], v[64:79]
ds_read_b64_tr_b16 v[144:145], v198 offset:20480
ds_read_b64_tr_b16 v[146:147], v198 offset:20992
v_mfma_f32_32x32x16_bf16 v[80:95], v[136:139], a[36:39], v[80:95]
s_waitcnt lgkmcnt(2)
v_mfma_f32_32x32x16_bf16 v[64:79], v[140:143], a[40:43], v[64:79]
ds_read_b64_tr_b16 v[148:149], v198 offset:22528
ds_read_b64_tr_b16 v[150:151], v198 offset:23040
v_mfma_f32_32x32x16_bf16 v[80:95], v[140:143], a[44:47], v[80:95]
s_waitcnt lgkmcnt(2)
v_mfma_f32_32x32x16_bf16 v[64:79], v[144:147], a[48:51], v[64:79]
v_mfma_f32_32x32x16_bf16 v[80:95], v[144:147], a[52:55], v[80:95]
s_waitcnt lgkmcnt(0)
v_mfma_f32_32x32x16_bf16 v[64:79], v[148:151], a[56:59], v[64:79]
v_mfma_f32_32x32x16_bf16 v[80:95], v[148:151], a[60:63], v[80:95]
s_waitcnt lgkmcnt(0)
s_barrier
s_nop 9
ds_write_b128 v203, v[80:83]
ds_write_b128 v203, v[84:87] offset:1024
ds_write_b128 v203, v[88:91] offset:2048
ds_write_b128 v203, v[92:95] offset:3072
s_waitcnt lgkmcnt(0)
s_barrier
s_lshl_b32 s85, s9, 13
ds_read_b128 v[152:155], v204
ds_read_b128 v[156:159], v204 offset:1024
s_waitcnt lgkmcnt(1)
v_add_f32 v152, v152, v64
v_add_f32 v153, v153, v65
v_add_f32 v154, v154, v66
v_add_f32 v155, v155, v67
s_waitcnt lgkmcnt(0)
v_add_f32 v156, v156, v68
v_add_f32 v157, v157, v69
v_add_f32 v158, v158, v70
v_add_f32 v159, v159, v71
v_cvt_pk_bf16_f32 v170, v152, v153
v_cvt_pk_bf16_f32 v171, v154, v155
v_cvt_pk_bf16_f32 v172, v156, v157
v_cvt_pk_bf16_f32 v173, v158, v159
buffer_store_dwordx4 v[170:173], v202, s[40:43], s85 offen sc1
ds_read_b128 v[160:163], v204 offset:2048
ds_read_b128 v[164:167], v204 offset:3072
s_waitcnt lgkmcnt(1)
v_add_f32 v160, v160, v72
v_add_f32 v161, v161, v73
v_add_f32 v162, v162, v74
v_add_f32 v163, v163, v75
s_waitcnt lgkmcnt(0)
v_add_f32 v164, v164, v76
v_add_f32 v165, v165, v77
v_add_f32 v166, v166, v78
v_add_f32 v167, v167, v79
v_cvt_pk_bf16_f32 v174, v160, v161
v_cvt_pk_bf16_f32 v175, v162, v163
v_cvt_pk_bf16_f32 v176, v164, v165
v_cvt_pk_bf16_f32 v177, v166, v167
buffer_store_dwordx4 v[174:177], v202, s[40:43], s85 offen offset:1024 sc1
s_branch L_bdone
; ==== bend (1 iteration(s))
L_four:
s_waitcnt vmcnt(7)
v_cmp_eq_u64 vcc, s[62:63], v[206:207]
s_nop 3
s_cmp_eq_u64 vcc, -1
s_cbranch_scc1 L_ok19
s_mov_b32 s17, 0x20000
L_poll18:
s_sleep 1
buffer_load_dwordx2 v[206:207], v208, s[40:43], 0 offen sc1
s_waitcnt vmcnt(0)
v_cmp_eq_u64 vcc, s[62:63], v[206:207]
s_nop 4
s_cmp_eq_u64 vcc, -1
s_cbranch_scc1 L_ok19
s_sub_u32 s17, s17, 1
s_cmp_lg_u32 s17, 0
s_cbranch_scc1 L_poll18
L_ok19:
s_waitcnt vmcnt(5)
v_cmp_eq_u64 vcc, s[62:63], v[210:211]
s_nop 3
s_cmp_eq_u64 vcc, -1
s_cbranch_scc1 L_ok21
s_mov_b32 s17, 0x20000
L_poll20:
s_sleep 1
buffer_load_dwordx2 v[210:211], v212, s[40:43], 0 offen sc1
s_waitcnt vmcnt(0)
v_cmp_eq_u64 vcc, s[62:63], v[210:211]
s_nop 4
s_cmp_eq_u64 vcc, -1
s_cbranch_scc1 L_ok21
s_sub_u32 s17, s17, 1
s_cmp_lg_u32 s17, 0
s_cbranch_scc1 L_poll20
L_ok21:
s_waitcnt vmcnt(3)
v_cmp_eq_u64 vcc, s[62:63], v[214:215]
s_nop 3
s_cmp_eq_u64 vcc, -1
s_cbranch_scc1 L_ok23
s_mov_b32 s17, 0x20000
L_poll22:
s_sleep 1
buffer_load_dwordx2 v[214:215], v216, s[40:43], 0 offen sc1
s_waitcnt vmcnt(0)
v_cmp_eq_u64 vcc, s[62:63], v[214:215]
s_nop 4
s_cmp_eq_u64 vcc, -1
s_cbranch_scc1 L_ok23
s_sub_u32 s17, s17, 1
s_cmp_lg_u32 s17, 0
s_cbranch_scc1 L_poll22
L_ok23:
s_waitcnt vmcnt(2)
v_cmp_eq_u64 vcc, s[62:63], v[218:219]
s_nop 3
s_cmp_eq_u64 vcc, -1
s_cbranch_scc1 L_ok25
s_mov_b32 s17, 0x20000
L_poll24:
s_sleep 1
buffer_load_dwordx2 v[218:219], v220, s[40:43], 0 offen sc1
s_waitcnt vmcnt(0)
v_cmp_eq_u64 vcc, s[62:63], v[218:219]
s_nop 4
s_cmp_eq_u64 vcc, -1
s_cbranch_scc1 L_ok25
s_sub_u32 s17, s17, 1
s_cmp_lg_u32 s17, 0
s_cbranch_scc1 L_poll24
L_ok25:
s_and_b32 s10, s57, 3
s_lshr_b32 s11, s57, 2
s_lshl_b32 s66, s58, 2
s_add_u32 s11, s11, s66
v_lshrrev_b32 v219, 2, v178
v_and_b32 v220, 3, v178
v_lshlrev_b32 v218, 12, v219
v_lshl_add_u32 v218, v220, 4, v218
s_lshl_b32 s66, s56, 8
s_lshl_b32 s67, s10, 6
s_add_u32 s66, s66, s67
s_mul_i32 s67, s58, 589824
s_add_u32 s66, s66, s67
s_add_u32 s66, s66, 4780032
v_add_u32 v218, s66, v218
v_lshlrev_b32 v221, 4, v178
s_mul_i32 s16, s11, 36864
s_cmp_gt_u32 s11, 8
s_cselect_b32 s16, 2113929216, s16
s_add_u32 s94, s16, 0
buffer_load_dwordx4 a[64:67], v221, s[52:55], s94 offen
s_add_u32 s94, s16, 1024
buffer_load_dwordx4 a[68:71], v221, s[52:55], s94 offen
s_add_u32 s94, s16, 2048
buffer_load_dwordx4 a[72:75], v221, s[52:55], s94 offen
s_add_u32 s94, s16, 3072
buffer_load_dwordx4 a[76:79], v221, s[52:55], s94 offen
s_add_u32 s94, s16, 4096
buffer_load_dwordx4 a[80:83], v221, s[52:55], s94 offen
s_add_u32 s94, s16, 5120
buffer_load_dwordx4 a[84:87], v221, s[52:55], s94 offen
s_add_u32 s94, s16, 6144
buffer_load_dwordx4 a[88:91], v221, s[52:55], s94 offen
s_add_u32 s94, s16, 7168
buffer_load_dwordx4 a[92:95], v221, s[52:55], s94 offen
s_add_u32 s94, s16, 8192
buffer_load_dwordx4 a[96:99], v221, s[52:55], s94 offen
s_add_u32 s94, s16, 9216
buffer_load_dwordx4 a[100:103], v221, s[52:55], s94 offen
s_add_u32 s94, s16, 10240
buffer_load_dwordx4 a[104:107], v221, s[52:55], s94 offen
s_add_u32 s94, s16, 11264
buffer_load_dwordx4 a[108:111], v221, s[52:55], s94 offen
s_add_u32 s94, s16, 12288
buffer_load_dwordx4 a[112:115], v221, s[52:55], s94 offen
s_add_u32 s94, s16, 13312
buffer_load_dwordx4 a[116:119], v221, s[52:55], s94 offen
s_add_u32 s94, s16, 14336
buffer_load_dwordx4 a[120:123], v221, s[52:55], s94 offen
s_add_u32 s94, s16, 15360
buffer_load_dwordx4 a[124:127], v221, s[52:55], s94 offen
s_add_u32 s94, s16, 16384
buffer_load_dwordx4 a[128:131], v221, s[52:55], s94 offen
s_add_u32 s94, s16, 17408
buffer_load_dwordx4 a[132:135], v221, s[52:55], s94 offen
s_add_u32 s94, s16, 18432
buffer_load_dwordx4 a[136:139], v221, s[52:55], s94 offen
s_add_u32 s94, s16, 19456
buffer_load_dwordx4 a[140:143], v221, s[52:55], s94 offen
s_add_u32 s94, s16, 20480
buffer_load_dwordx4 a[144:147], v221, s[52:55], s94 offen
s_add_u32 s94, s16, 21504
buffer_load_dwordx4 a[148:151], v221, s[52:55], s94 offen
s_add_u32 s94, s16, 22528
buffer_load_dwordx4 a[152:155], v221, s[52:55], s94 offen
s_add_u32 s94, s16, 23552
buffer_load_dwordx4 a[156:159], v221, s[52:55], s94 offen
s_add_u32 s94, s16, 24576
buffer_load_dwordx4 a[160:163], v221, s[52:55], s94 offen
s_add_u32 s94, s16, 25600
buffer_load_dwordx4 a[164:167], v221, s[52:55], s94 offen
s_add_u32 s94, s16, 26624
buffer_load_dwordx4 a[168:171], v221, s[52:55], s94 offen
s_add_u32 s94, s16, 27648
buffer_load_dwordx4 a[172:175], v221, s[52:55], s94 offen
s_add_u32 s94, s16, 28672
buffer_load_dwordx4 a[176:179], v221, s[52:55], s94 offen
s_add_u32 s94, s16, 29696
buffer_load_dwordx4 a[180:183], v221, s[52:55], s94 offen
s_add_u32 s94, s16, 30720
buffer_load_dwordx4 a[184:187], v221, s[52:55], s94 offen
s_add_u32 s94, s16, 31744
buffer_load_dwordx4 a[188:191], v221, s[52:55], s94 offen
s_add_u32 s94, s16, 32768
buffer_load_dwordx4 a[192:195], v221, s[52:55], s94 offen
s_add_u32 s94, s16, 33792
buffer_load_dwordx4 a[196:199], v221, s[52:55], s94 offen
s_add_u32 s94, s16, 34816
buffer_load_dwordx4 a[200:203], v221, s[52:55], s94 offen
s_add_u32 s94, s16, 35840
buffer_load_dwordx4 a[204:207], v221, s[52:55], s94 offen
s_mov_b32 s94, 0
buffer_load_dwordx4 a[208:211], v218, s[40:43], s94 offen sc1
s_mov_b32 s94, 65536
buffer_load_dwordx4 a[212:215], v218, s[40:43], s94 offen sc1
s_mov_b32 s94, 131072
buffer_load_dwordx4 a[216:219], v218, s[40:43], s94 offen sc1
s_mov_b32 s94, 196608
buffer_load_dwordx4 a[220:223], v218, s[40:43], s94 offen sc1
s_mov_b32 s94, 262144
buffer_load_dwordx4 a[224:227], v218, s[40:43], s94 offen sc1
s_mov_b32 s94, 327680
buffer_load_dwordx4 a[228:231], v218, s[40:43], s94 offen sc1
s_mov_b32 s94, 393216
buffer_load_dwordx4 a[232:235], v218, s[40:43], s94 offen sc1
s_mov_b32 s94, 458752
buffer_load_dwordx4 a[236:239], v218, s[40:43], s94 offen sc1
s_mov_b32 s94, 524288
buffer_load_dwordx4 a[240:243], v218, s[40:43], s94 offen sc1
s_waitcnt lgkmcnt(0)
s_barrier
ds_write_b128 v203, v[16:19]
ds_write_b128 v203, v[20:23] offset:1024
ds_write_b128 v203, v[24:27] offset:2048
ds_write_b128 v203, v[28:31] offset:3072
ds_write_b128 v203, v[48:51] offset:4096
ds_write_b128 v203, v[52:55] offset:5120
ds_write_b128 v203, v[56:59] offset:6144
ds_write_b128 v203, v[60:63] offset:7168
s_waitcnt vmcnt(45) lgkmcnt(0)
s_barrier
s_add_u32 s85, s8, 2
s_lshl_b32 s85, s85, 13
ds_read_b128 v[152:155], v204
ds_read_b128 v[156:159], v204 offset:1024
s_waitcnt lgkmcnt(1)
v_add_f32 v152, v152, v0
v_add_f32 v153, v153, v1
v_add_f32 v154, v154, v2
v_add_f32 v155, v155, v3
s_waitcnt lgkmcnt(0)
v_add_f32 v156, v156, v4
v_add_f32 v157, v157, v5
v_add_f32 v158, v158, v6
v_add_f32 v159, v159, v7
v_cvt_pk_bf16_f32 v170, v152, v153
v_cvt_pk_bf16_f32 v171, v154, v155
v_cvt_pk_bf16_f32 v172, v156, v157
v_cvt_pk_bf16_f32 v173, v158, v159
buffer_store_dwordx4 v[170:173], v202, s[40:43], s85 offen sc1
ds_read_b128 v[160:163], v204 offset:2048
ds_read_b128 v[164:167], v204 offset:3072
s_waitcnt lgkmcnt(1)
v_add_f32 v160, v160, v8
v_add_f32 v161, v161, v9
v_add_f32 v162, v162, v10
v_add_f32 v163, v163, v11
s_waitcnt lgkmcnt(0)
v_add_f32 v164, v164, v12
v_add_f32 v165, v165, v13
v_add_f32 v166, v166, v14
v_add_f32 v167, v167, v15
v_cvt_pk_bf16_f32 v174, v160, v161
v_cvt_pk_bf16_f32 v175, v162, v163
v_cvt_pk_bf16_f32 v176, v164, v165
v_cvt_pk_bf16_f32 v177, v166, v167
buffer_store_dwordx4 v[174:177], v202, s[40:43], s85 offen offset:1024 sc1
s_add_u32 s85, s85, 8192
ds_read_b128 v[152:155], v204 offset:4096
ds_read_b128 v[156:159], v204 offset:5120
s_waitcnt lgkmcnt(1)
v_add_f32 v152, v152, v32
v_add_f32 v153, v153, v33
v_add_f32 v154, v154, v34
v_add_f32 v155, v155, v35
s_waitcnt lgkmcnt(0)
v_add_f32 v156, v156, v36
v_add_f32 v157, v157, v37
v_add_f32 v158, v158, v38
v_add_f32 v159, v159, v39
v_cvt_pk_bf16_f32 v170, v152, v153
v_cvt_pk_bf16_f32 v171, v154, v155
v_cvt_pk_bf16_f32 v172, v156, v157
v_cvt_pk_bf16_f32 v173, v158, v159
buffer_store_dwordx4 v[170:173], v202, s[40:43], s85 offen sc1
ds_read_b128 v[160:163], v204 offset:6144
ds_read_b128 v[164:167], v204 offset:7168
s_waitcnt lgkmcnt(1)
v_add_f32 v160, v160, v40
v_add_f32 v161, v161, v41
v_add_f32 v162, v162, v42
v_add_f32 v163, v163, v43
s_waitcnt lgkmcnt(0)
v_add_f32 v164, v164, v44
v_add_f32 v165, v165, v45
v_add_f32 v166, v166, v46
v_add_f32 v167, v167, v47
v_cvt_pk_bf16_f32 v174, v160, v161
v_cvt_pk_bf16_f32 v175, v162, v163
v_cvt_pk_bf16_f32 v176, v164, v165
v_cvt_pk_bf16_f32 v177, v166, v167
buffer_store_dwordx4 v[174:177], v202, s[40:43], s85 offen offset:1024 sc1
L_bdone:
; ==== tail (1 iteration(s))
v_lshrrev_b32 v139, 2, v178
v_and_b32 v137, 3, v178
v_lshlrev_b32 v151, 4, v178
s_cmp_lg_u32 s22, 0
s_cbranch_scc1 L_noep26
s_mov_b32 s66, 0
s_cmp_lg_u32 s58, 0
s_cbranch_scc1 L_skip27
s_mov_b64 exec, 1
v_mov_b32 v148, s62
v_mov_b32 v149, s63
v_mov_b32 v150, s66
buffer_store_dwordx2 v[148:149], v150, s[40:43], 0 offen sc1
s_mov_b64 exec, -1
L_skip27:
L_noep26:
v_lshrrev_b32 v140, 2, v139
v_and_b32 v141, 1, v139
v_lshl_add_u32 v140, v140, 1, v141
v_and_b32 v141, 2, v139
s_mov_b32 s66, 144
v_mad_u32_u24 v140, v141, s66, v140
v_lshlrev_b32 v140, 6, v140
v_lshl_add_u32 v140, v137, 4, v140
s_mul_i32 s66, s58, 4608
s_add_u32 s66, s66, 0
v_add_u32 v138, s66, v140
v_lshrrev_b32 v139, 4, v178
v_and_b32 v140, 15, v178
v_lshrrev_b32 v141, 2, v140
v_and_b32 v142, 3, v140
v_lshrrev_b32 v143, 1, v139
v_and_b32 v144, 1, v139
v_lshlrev_b32 v144, 5, v144
v_lshl_add_u32 v144, v142, 3, v144
v_lshl_add_u32 v145, v143, 3, v141
v_lshl_add_u32 v144, v145, 6, v144
v_add_u32 v146, 0, v144
s_waitcnt vmcnt(0)
s_lshl_b32 s66, s56, 4
s_add_u32 s66, s66, s57
s_lshl_b32 s66, s66, 2
s_add_u32 s66, s66, s58
s_lshl_b32 s66, s66, 3
s_add_u32 s66, s66, 35840
s_mov_b64 exec, 1
v_mov_b32 v112, s62
v_mov_b32 v113, s63
v_mov_b32 v114, s66
buffer_store_dwordx2 v[112:113], v114, s[40:43], 0 offen sc1
s_mov_b64 exec, -1
ds_write_b128 v138, a[208:211]
ds_write_b128 v138, a[212:215] offset:512
ds_write_b128 v138, a[216:219] offset:1024
ds_write_b128 v138, a[220:223] offset:1536
ds_write_b128 v138, a[224:227] offset:2048
ds_write_b128 v138, a[228:231] offset:2560
ds_write_b128 v138, a[232:235] offset:3072
ds_write_b128 v138, a[236:239] offset:3584
ds_write_b128 v138, a[240:243] offset:4096
s_waitcnt lgkmcnt(0)
s_barrier
s_cmp_gt_u32 s11, 8
s_cbranch_scc1 L_done28
ds_read_b64_tr_b16 v[100:101], v146
ds_read_b64_tr_b16 v[102:103], v146 offset:256
ds_read_b64_tr_b16 v[104:105], v146 offset:18432
ds_read_b64_tr_b16 v[106:107], v146 offset:18688
ds_read_b64_tr_b16 v[108:109], v146 offset:1024
ds_read_b64_tr_b16 v[110:111], v146 offset:1280
s_waitcnt lgkmcnt(4)
v_mfma_f32_32x32x16_bf16 v[0:15], a[64:67], v[100:103], 0
ds_read_b64_tr_b16 v[100:101], v146 offset:19456
ds_read_b64_tr_b16 v[102:103], v146 offset:19712
s_waitcnt lgkmcnt(4)
v_mfma_f32_32x32x16_bf16 v[16:31], a[136:139], v[104:107], 0
ds_read_b64_tr_b16 v[104:105], v146 offset:2048
ds_read_b64_tr_b16 v[106:107], v146 offset:2304
s_waitcnt lgkmcnt(4)
v_mfma_f32_32x32x16_bf16 v[0:15], a[68:71], v[108:111], v[0:15]
ds_read_b64_tr_b16 v[108:109], v146 offset:20480
ds_read_b64_tr_b16 v[110:111], v146 offset:20736
s_waitcnt lgkmcnt(4)
v_mfma_f32_32x32x16_bf16 v[16:31], a[140:143], v[100:103], v[16:31]
ds_read_b64_tr_b16 v[100:101], v146 offset:3072
ds_read_b64_tr_b16 v[102:103], v146 offset:3328
s_waitcnt lgkmcnt(4)
v_mfma_f32_32x32x16_bf16 v[0:15], a[72:75], v[104:107], v[0:15]
ds_read_b64_tr_b16 v[104:105], v146 offset:21504
ds_read_b64_tr_b16 v[106:107], v146 offset:21760
s_waitcnt lgkmcnt(4)
v_mfma_f32_32x32x16_bf16 v[16:31], a[144:147], v[108:111], v[16:31]
ds_read_b64_tr_b16 v[108:109], v146 offset:4096
ds_read_b64_tr_b16 v[110:111], v146 offset:4352
s_waitcnt lgkmcnt(4)
v_mfma_f32_32x32x16_bf16 v[0:15], a[76:79], v[100:103], v[0:15]
ds_read_b64_tr_b16 v[100:101], v146 offset:22528
ds_read_b64_tr_b16 v[102:103], v146 offset:22784
s_waitcnt lgkmcnt(4)
v_mfma_f32_32x32x16_bf16 v[16:31], a[148:151], v[104:107], v[16:31]
ds_read_b64_tr_b16 v[104:105], v146 offset:5120
ds_read_b64_tr_b16 v[106:107], v146 offset:5376
s_waitcnt lgkmcnt(4)
v_mfma_f32_32x32x16_bf16 v[0:15], a[80:83], v[108:111], v[0:15]
s_mul_i32 s66, s56, 147456
s_lshl_b32 s67, s11, 13
s_add_u32 s66, s66, s67
s_lshl_b32 s67, s10, 11
s_add_u32 s66, s66, s67
s_add_u32 s66, s66, 26013696
v_add_u32 v147, s66, v151
v_and_b32 v148, 31, v178
v_lshlrev_b32 v148, 2, v148
s_lshl_b32 s66, s10, 7
s_add_u32 s66, s66, 149504
v_add_u32 v148, s66, v148
ds_read_b32 v149, v148
ds_read_b64_tr_b16 v[108:109], v146 offset:23552
ds_read_b64_tr_b16 v[110:111], v146 offset:23808
s_waitcnt lgkmcnt(5)
v_mfma_f32_32x32x16_bf16 v[16:31], a[152:155], v[100:103], v[16:31]
ds_read_b64_tr_b16 v[100:101], v146 offset:6144
ds_read_b64_tr_b16 v[102:103], v146 offset:6400
s_waitcnt lgkmcnt(5)
v_mfma_f32_32x32x16_bf16 v[0:15], a[84:87], v[104:107], v[0:15]
ds_read_b64_tr_b16 v[104:105], v146 offset:24576
ds_read_b64_tr_b16 v[106:107], v146 offset:24832
s_waitcnt lgkmcnt(4)
v_mfma_f32_32x32x16_bf16 v[16:31], a[156:159], v[108:111], v[16:31]
ds_read_b64_tr_b16 v[108:109], v146 offset:7168
ds_read_b64_tr_b16 v[110:111], v146 offset:7424
s_waitcnt lgkmcnt(4)
v_mfma_f32_32x32x16_bf16 v[0:15], a[88:91], v[100:103], v[0:15]
ds_read_b64_tr_b16 v[100:101], v146 offset:25600
ds_read_b64_tr_b16 v[102:103], v146 offset:25856
s_waitcnt lgkmcnt(4)
v_mfma_f32_32x32x16_bf16 v[16:31], a[160:163], v[104:107], v[16:31]
ds_read_b64_tr_b16 v[104:105], v146 offset:8192
ds_read_b64_tr_b16 v[106:107], v146 offset:8448
s_waitcnt lgkmcnt(4)
v_mfma_f32_32x32x16_bf16 v[0:15], a[92:95], v[108:111], v[0:15]
ds_read_b64_tr_b16 v[108:109], v146 offset:26624
ds_read_b64_tr_b16 v[110:111], v146 offset:26880
s_waitcnt lgkmcnt(4)
v_mfma_f32_32x32x16_bf16 v[16:31], a[164:167], v[100:103], v[16:31]
ds_read_b64_tr_b16 v[100:101], v146 offset:9216
ds_read_b64_tr_b16 v[102:103], v146 offset:9472
s_waitcnt lgkmcnt(4)
v_mfma_f32_32x32x16_bf16 v[0:15], a[96:99], v[104:107], v[0:15]
ds_read_b64_tr_b16 v[104:105], v146 offset:27648
ds_read_b64_tr_b16 v[106:107], v146 offset:27904
s_waitcnt lgkmcnt(4)
v_mfma_f32_32x32x16_bf16 v[16:31], a[168:171], v[108:111], v[16:31]
ds_read_b64_tr_b16 v[108:109], v146 offset:10240
ds_read_b64_tr_b16 v[110:111], v146 offset:10496
s_waitcnt lgkmcnt(4)
v_mfma_f32_32x32x16_bf16 v[0:15], a[100:103], v[100:103], v[0:15]
ds_read_b64_tr_b16 v[100:101], v146 offset:28672
ds_read_b64_tr_b16 v[102:103], v146 offset:28928
s_waitcnt lgkmcnt(4)
v_mfma_f32_32x32x16_bf16 v[16:31], a[172:175], v[104:107], v[16:31]
ds_read_b64_tr_b16 v[104:105], v146 offset:11264
ds_read_b64_tr_b16 v[106:107], v146 offset:11520
s_waitcnt lgkmcnt(4)
v_mfma_f32_32x32x16_bf16 v[0:15], a[104:107], v[108:111], v[0:15]
ds_read_b64_tr_b16 v[108:109], v146 offset:29696
ds_read_b64_tr_b16 v[110:111], v146 offset:29952
s_waitcnt lgkmcnt(4)
v_mfma_f32_32x32x16_bf16 v[16:31], a[176:179], v[100:103], v[16:31]
ds_read_b64_tr_b16 v[100:101], v146 offset:12288
ds_read_b64_tr_b16 v[102:103], v146 offset:12544
s_waitcnt lgkmcnt(4)
v_mfma_f32_32x32x16_bf16 v[0:15], a[108:111], v[104:107], v[0:15]
ds_read_b64_tr_b16 v[104:105], v146 offset:30720
ds_read_b64_tr_b16 v[106:107], v146 offset:30976
s_waitcnt lgkmcnt(4)
v_mfma_f32_32x32x16_bf16 v[16:31], a[180:183], v[108:111], v[16:31]
ds_read_b64_tr_b16 v[108:109], v146 offset:13312
ds_read_b64_tr_b16 v[110:111], v146 offset:13568
s_waitcnt lgkmcnt(4)
v_mfma_f32_32x32x16_bf16 v[0:15], a[112:115], v[100:103], v[0:15]
ds_read_b64_tr_b16 v[100:101], v146 offset:31744
ds_read_b64_tr_b16 v[102:103], v146 offset:32000
s_waitcnt lgkmcnt(4)
v_mfma_f32_32x32x16_bf16 v[16:31], a[184:187], v[104:107], v[16:31]
ds_read_b64_tr_b16 v[104:105], v146 offset:14336
ds_read_b64_tr_b16 v[106:107], v146 offset:14592
s_waitcnt lgkmcnt(4)
v_mfma_f32_32x32x16_bf16 v[0:15], a[116:119], v[108:111], v[0:15]
s_lshl_b32 s66, s56, 9
s_add_u32 s66, s66, 35840
v_lshlrev_b32 v118, 3, v178
v_add_u32 v118, s66, v118
buffer_load_dwordx2 v[116:117], v118, s[40:43], 0 offen sc1
ds_read_b64_tr_b16 v[108:109], v146 offset:32768
ds_read_b64_tr_b16 v[110:111], v146 offset:33024
s_waitcnt lgkmcnt(4)
v_mfma_f32_32x32x16_bf16 v[16:31], a[188:191], v[100:103], v[16:31]
ds_read_b64_tr_b16 v[100:101], v146 offset:15360
ds_read_b64_tr_b16 v[102:103], v146 offset:15616
s_waitcnt lgkmcnt(4)
v_mfma_f32_32x32x16_bf16 v[0:15], a[120:123], v[104:107], v[0:15]
ds_read_b64_tr_b16 v[104:105], v146 offset:33792
ds_read_b64_tr_b16 v[106:107], v146 offset:34048
s_waitcnt lgkmcnt(4)
v_mfma_f32_32x32x16_bf16 v[16:31], a[192:195], v[108:111], v[16:31]
ds_read_b64_tr_b16 v[108:109], v146 offset:16384
ds_read_b64_tr_b16 v[110:111], v146 offset:16640
s_waitcnt lgkmcnt(4)
v_mfma_f32_32x32x16_bf16 v[0:15], a[124:127], v[100:103], v[0:15]
ds_read_b64_tr_b16 v[100:101], v146 offset:34816
ds_read_b64_tr_b16 v[102:103], v146 offset:35072
s_waitcnt lgkmcnt(4)
v_mfma_f32_32x32x16_bf16 v[16:31], a[196:199], v[104:107], v[16:31]
ds_read_b64_tr_b16 v[104:105], v146 offset:17408
ds_read_b64_tr_b16 v[106:107], v146 offset:17664
s_waitcnt lgkmcnt(4)
v_mfma_f32_32x32x16_bf16 v[0:15], a[128:131], v[108:111], v[0:15]
ds_read_b64_tr_b16 v[108:109], v146 offset:35840
ds_read_b64_tr_b16 v[110:111], v146 offset:36096
s_waitcnt lgkmcnt(4)
v_mfma_f32_32x32x16_bf16 v[16:31], a[200:203], v[100:103], v[16:31]
s_waitcnt lgkmcnt(2)
v_mfma_f32_32x32x16_bf16 v[0:15], a[132:135], v[104:107], v[0:15]
s_waitcnt lgkmcnt(0)
v_mfma_f32_32x32x16_bf16 v[16:31], a[204:207], v[108:111], v[16:31]
s_waitcnt vmcnt(0)
v_cmp_eq_u64 vcc, s[62:63], v[116:117]
s_nop 3
s_cmp_eq_u64 vcc, -1
s_cbranch_scc1 L_ok30
s_mov_b32 s17, 0x20000
L_poll29:
s_sleep 1
buffer_load_dwordx2 v[116:117], v118, s[40:43], 0 offen sc1
s_waitcnt vmcnt(0)
v_cmp_eq_u64 vcc, s[62:63], v[116:117]
s_nop 4
s_cmp_eq_u64 vcc, -1
s_cbranch_scc1 L_ok30
s_sub_u32 s17, s17, 1
s_cmp_lg_u32 s17, 0
s_cbranch_scc1 L_poll29
L_ok30:
s_mov_b32 s67, 0
buffer_load_dwordx4 v[32:35], v147, s[40:43], s67 offen sc1
buffer_load_dwordx4 v[36:39], v147, s[40:43], s67 offen offset:1024 sc1
s_mov_b32 s67, 2359296
buffer_load_dwordx4 v[48:51], v147, s[40:43], s67 offen sc1
buffer_load_dwordx4 v[52:55], v147, s[40:43], s67 offen offset:1024 sc1
s_mov_b32 s67, 4718592
buffer_load_dwordx4 v[64:67], v147, s[40:43], s67 offen sc1
buffer_load_dwordx4 v[68:71], v147, s[40:43], s67 offen offset:1024 sc1
s_mov_b32 s67, 7077888
buffer_load_dwordx4 v[80:83], v147, s[40:43], s67 offen sc1
buffer_load_dwordx4 v[84:87], v147, s[40:43], s67 offen offset:1024 sc1
s_mov_b32 s67, 73728
buffer_load_dwordx4 v[40:43], v147, s[40:43], s67 offen sc1
buffer_load_dwordx4 v[44:47], v147, s[40:43], s67 offen offset:1024 sc1
s_mov_b32 s67, 2433024
buffer_load_dwordx4 v[56:59], v147, s[40:43], s67 offen sc1
buffer_load_dwordx4 v[60:63], v147, s[40:43], s67 offen offset:1024 sc1
s_mov_b32 s67, 4792320
buffer_load_dwordx4 v[72:75], v147, s[40:43], s67 offen sc1
buffer_load_dwordx4 v[76:79], v147, s[40:43], s67 offen offset:1024 sc1
s_mov_b32 s67, 7151616
buffer_load_dwordx4 v[88:91], v147, s[40:43], s67 offen sc1
buffer_load_dwordx4 v[92:95], v147, s[40:43], s67 offen offset:1024 sc1
v_mul_f32 v149, s76, v149
v_exp_f32 v149, v149
s_nop 0
v_add_f32 v149, 1.0, v149
v_rcp_f32 v149, v149
v_and_b32 v150, 31, v178
s_mov_b32 s66, 144
v_mul_u32_u24 v150, s66, v150
v_lshrrev_b32 v145, 5, v178
v_lshl_add_u32 v150, v145, 3, v150
s_mul_i32 s66, s58, 4608
s_add_u32 s66, s66, 40960
v_add_u32 v150, s66, v150
v_lshrrev_b32 v139, 2, v178
v_and_b32 v140, 3, v178
s_mov_b32 s67, 144
v_mul_u32_u24 v141, s67, v139
v_lshl_add_u32 v141, v140, 4, v141
v_add_u32 v141, s66, v141
s_mov_b32 s66, 1152
v_mul_u32_u24 v142, s66, v139
v_lshl_add_u32 v142, v140, 4, v142
v_mov_b32 v148, v149
s_waitcnt vmcnt(15)
v_lshlrev_b32 v100, 16, v32
v_and_b32 v101, 4294901760, v32
v_lshlrev_b32 v102, 16, v33
v_and_b32 v103, 4294901760, v33
v_lshlrev_b32 v104, 16, v34
v_and_b32 v105, 4294901760, v34
v_lshlrev_b32 v106, 16, v35
v_and_b32 v107, 4294901760, v35
v_pk_add_f32 v[120:121], v[0:1], v[16:17]
v_pk_add_f32 v[122:123], v[2:3], v[18:19]
v_pk_add_f32 v[124:125], v[4:5], v[20:21]
v_pk_add_f32 v[126:127], v[6:7], v[22:23]
s_waitcnt vmcnt(13)
v_lshlrev_b32 v108, 16, v48
v_and_b32 v109, 4294901760, v48
v_lshlrev_b32 v110, 16, v49
v_and_b32 v111, 4294901760, v49
v_lshlrev_b32 v112, 16, v50
v_and_b32 v113, 4294901760, v50
v_lshlrev_b32 v114, 16, v51
v_and_b32 v115, 4294901760, v51
v_pk_add_f32 v[100:101], v[100:101], v[108:109]
v_pk_add_f32 v[102:103], v[102:103], v[110:111]
v_pk_add_f32 v[104:105], v[104:105], v[112:113]
v_pk_add_f32 v[106:107], v[106:107], v[114:115]
s_waitcnt vmcnt(11)
v_lshlrev_b32 v108, 16, v64
v_and_b32 v109, 4294901760, v64
v_lshlrev_b32 v110, 16, v65
v_and_b32 v111, 4294901760, v65
v_lshlrev_b32 v112, 16, v66
v_and_b32 v113, 4294901760, v66
v_lshlrev_b32 v114, 16, v67
v_and_b32 v115, 4294901760, v67
v_pk_add_f32 v[100:101], v[100:101], v[108:109]
v_pk_add_f32 v[102:103], v[102:103], v[110:111]
v_pk_add_f32 v[104:105], v[104:105], v[112:113]
v_pk_add_f32 v[106:107], v[106:107], v[114:115]
s_waitcnt vmcnt(9)
v_lshlrev_b32 v108, 16, v80
v_and_b32 v109, 4294901760, v80
v_lshlrev_b32 v110, 16, v81
v_and_b32 v111, 4294901760, v81
v_lshlrev_b32 v112, 16, v82
v_and_b32 v113, 4294901760, v82
v_lshlrev_b32 v114, 16, v83
v_and_b32 v115, 4294901760, v83
v_pk_add_f32 v[100:101], v[100:101], v[108:109]
v_pk_add_f32 v[102:103], v[102:103], v[110:111]
v_pk_add_f32 v[104:105], v[104:105], v[112:113]
v_pk_add_f32 v[106:107], v[106:107], v[114:115]
v_pk_add_f32 v[108:109], v[120:121], v[100:101] neg_lo:[0,1] neg_hi:[0,1]
v_pk_add_f32 v[110:111], v[122:123], v[102:103] neg_lo:[0,1] neg_hi:[0,1]
v_pk_add_f32 v[112:113], v[124:125], v[104:105] neg_lo:[0,1] neg_hi:[0,1]
v_pk_add_f32 v[114:115], v[126:127], v[106:107] neg_lo:[0,1] neg_hi:[0,1]
v_pk_fma_f32 v[100:101], v[148:149], v[108:109], v[100:101]
v_pk_fma_f32 v[102:103], v[148:149], v[110:111], v[102:103]
v_pk_fma_f32 v[104:105], v[148:149], v[112:113], v[104:105]
v_pk_fma_f32 v[106:107], v[148:149], v[114:115], v[106:107]
v_cvt_pk_bf16_f32 v108, v100, v101
v_cvt_pk_bf16_f32 v109, v102, v103
v_cvt_pk_bf16_f32 v110, v104, v105
v_cvt_pk_bf16_f32 v111, v106, v107
ds_write_b64 v150, v[108:109]
ds_write_b64 v150, v[110:111] offset:16
v_lshlrev_b32 v100, 16, v36
v_and_b32 v101, 4294901760, v36
v_lshlrev_b32 v102, 16, v37
v_and_b32 v103, 4294901760, v37
v_lshlrev_b32 v104, 16, v38
v_and_b32 v105, 4294901760, v38
v_lshlrev_b32 v106, 16, v39
v_and_b32 v107, 4294901760, v39
v_pk_add_f32 v[120:121], v[8:9], v[24:25]
v_pk_add_f32 v[122:123], v[10:11], v[26:27]
v_pk_add_f32 v[124:125], v[12:13], v[28:29]
v_pk_add_f32 v[126:127], v[14:15], v[30:31]
v_lshlrev_b32 v108, 16, v52
v_and_b32 v109, 4294901760, v52
v_lshlrev_b32 v110, 16, v53
v_and_b32 v111, 4294901760, v53
v_lshlrev_b32 v112, 16, v54
v_and_b32 v113, 4294901760, v54
v_lshlrev_b32 v114, 16, v55
v_and_b32 v115, 4294901760, v55
v_pk_add_f32 v[100:101], v[100:101], v[108:109]
v_pk_add_f32 v[102:103], v[102:103], v[110:111]
v_pk_add_f32 v[104:105], v[104:105], v[112:113]
v_pk_add_f32 v[106:107], v[106:107], v[114:115]
v_lshlrev_b32 v108, 16, v68
v_and_b32 v109, 4294901760, v68
v_lshlrev_b32 v110, 16, v69
v_and_b32 v111, 4294901760, v69
v_lshlrev_b32 v112, 16, v70
v_and_b32 v113, 4294901760, v70
v_lshlrev_b32 v114, 16, v71
v_and_b32 v115, 4294901760, v71
v_pk_add_f32 v[100:101], v[100:101], v[108:109]
v_pk_add_f32 v[102:103], v[102:103], v[110:111]
v_pk_add_f32 v[104:105], v[104:105], v[112:113]
v_pk_add_f32 v[106:107], v[106:107], v[114:115]
s_waitcnt vmcnt(8)
v_lshlrev_b32 v108, 16, v84
v_and_b32 v109, 4294901760, v84
v_lshlrev_b32 v110, 16, v85
v_and_b32 v111, 4294901760, v85
v_lshlrev_b32 v112, 16, v86
v_and_b32 v113, 4294901760, v86
v_lshlrev_b32 v114, 16, v87
v_and_b32 v115, 4294901760, v87
v_pk_add_f32 v[100:101], v[100:101], v[108:109]
v_pk_add_f32 v[102:103], v[102:103], v[110:111]
v_pk_add_f32 v[104:105], v[104:105], v[112:113]
v_pk_add_f32 v[106:107], v[106:107], v[114:115]
v_pk_add_f32 v[108:109], v[120:121], v[100:101] neg_lo:[0,1] neg_hi:[0,1]
v_pk_add_f32 v[110:111], v[122:123], v[102:103] neg_lo:[0,1] neg_hi:[0,1]
v_pk_add_f32 v[112:113], v[124:125], v[104:105] neg_lo:[0,1] neg_hi:[0,1]
v_pk_add_f32 v[114:115], v[126:127], v[106:107] neg_lo:[0,1] neg_hi:[0,1]
v_pk_fma_f32 v[100:101], v[148:149], v[108:109], v[100:101]
v_pk_fma_f32 v[102:103], v[148:149], v[110:111], v[102:103]
v_pk_fma_f32 v[104:105], v[148:149], v[112:113], v[104:105]
v_pk_fma_f32 v[106:107], v[148:149], v[114:115], v[106:107]
v_cvt_pk_bf16_f32 v108, v100, v101
v_cvt_pk_bf16_f32 v109, v102, v103
v_cvt_pk_bf16_f32 v110, v104, v105
v_cvt_pk_bf16_f32 v111, v106, v107
ds_write_b64 v150, v[108:109] offset:32
ds_write_b64 v150, v[110:111] offset:48
s_waitcnt vmcnt(7)
v_lshlrev_b32 v100, 16, v40
v_and_b32 v101, 4294901760, v40
v_lshlrev_b32 v102, 16, v41
v_and_b32 v103, 4294901760, v41
v_lshlrev_b32 v104, 16, v42
v_and_b32 v105, 4294901760, v42
v_lshlrev_b32 v106, 16, v43
v_and_b32 v107, 4294901760, v43
v_pk_add_f32 v[120:121], v[0:1], v[16:17] neg_lo:[0,1] neg_hi:[0,1]
v_pk_add_f32 v[122:123], v[2:3], v[18:19] neg_lo:[0,1] neg_hi:[0,1]
v_pk_add_f32 v[124:125], v[4:5], v[20:21] neg_lo:[0,1] neg_hi:[0,1]
v_pk_add_f32 v[126:127], v[6:7], v[22:23] neg_lo:[0,1] neg_hi:[0,1]
s_waitcnt vmcnt(5)
v_lshlrev_b32 v108, 16, v56
v_and_b32 v109, 4294901760, v56
v_lshlrev_b32 v110, 16, v57
v_and_b32 v111, 4294901760, v57
v_lshlrev_b32 v112, 16, v58
v_and_b32 v113, 4294901760, v58
v_lshlrev_b32 v114, 16, v59
v_and_b32 v115, 4294901760, v59
v_pk_add_f32 v[100:101], v[100:101], v[108:109]
v_pk_add_f32 v[102:103], v[102:103], v[110:111]
v_pk_add_f32 v[104:105], v[104:105], v[112:113]
v_pk_add_f32 v[106:107], v[106:107], v[114:115]
s_waitcnt vmcnt(3)
v_lshlrev_b32 v108, 16, v72
v_and_b32 v109, 4294901760, v72
v_lshlrev_b32 v110, 16, v73
v_and_b32 v111, 4294901760, v73
v_lshlrev_b32 v112, 16, v74
v_and_b32 v113, 4294901760, v74
v_lshlrev_b32 v114, 16, v75
v_and_b32 v115, 4294901760, v75
v_pk_add_f32 v[100:101], v[100:101], v[108:109]
v_pk_add_f32 v[102:103], v[102:103], v[110:111]
v_pk_add_f32 v[104:105], v[104:105], v[112:113]
v_pk_add_f32 v[106:107], v[106:107], v[114:115]
s_waitcnt vmcnt(1)
v_lshlrev_b32 v108, 16, v88
v_and_b32 v109, 4294901760, v88
v_lshlrev_b32 v110, 16, v89
v_and_b32 v111, 4294901760, v89
v_lshlrev_b32 v112, 16, v90
v_and_b32 v113, 4294901760, v90
v_lshlrev_b32 v114, 16, v91
v_and_b32 v115, 4294901760, v91
v_pk_add_f32 v[100:101], v[100:101], v[108:109]
v_pk_add_f32 v[102:103], v[102:103], v[110:111]
v_pk_add_f32 v[104:105], v[104:105], v[112:113]
v_pk_add_f32 v[106:107], v[106:107], v[114:115]
v_pk_add_f32 v[108:109], v[120:121], v[100:101] neg_lo:[0,1] neg_hi:[0,1]
v_pk_add_f32 v[110:111], v[122:123], v[102:103] neg_lo:[0,1] neg_hi:[0,1]
v_pk_add_f32 v[112:113], v[124:125], v[104:105] neg_lo:[0,1] neg_hi:[0,1]
v_pk_add_f32 v[114:115], v[126:127], v[106:107] neg_lo:[0,1] neg_hi:[0,1]
v_pk_fma_f32 v[100:101], v[148:149], v[108:109], v[100:101]
v_pk_fma_f32 v[102:103], v[148:149], v[110:111], v[102:103]
v_pk_fma_f32 v[104:105], v[148:149], v[112:113], v[104:105]
v_pk_fma_f32 v[106:107], v[148:149], v[114:115], v[106:107]
v_cvt_pk_bf16_f32 v108, v100, v101
v_cvt_pk_bf16_f32 v109, v102, v103
v_cvt_pk_bf16_f32 v110, v104, v105
v_cvt_pk_bf16_f32 v111, v106, v107
ds_write_b64 v150, v[108:109] offset:64
ds_write_b64 v150, v[110:111] offset:80
v_lshlrev_b32 v100, 16, v44
v_and_b32 v101, 4294901760, v44
v_lshlrev_b32 v102, 16, v45
v_and_b32 v103, 4294901760, v45
v_lshlrev_b32 v104, 16, v46
v_and_b32 v105, 4294901760, v46
v_lshlrev_b32 v106, 16, v47
v_and_b32 v107, 4294901760, v47
v_pk_add_f32 v[120:121], v[8:9], v[24:25] neg_lo:[0,1] neg_hi:[0,1]
v_pk_add_f32 v[122:123], v[10:11], v[26:27] neg_lo:[0,1] neg_hi:[0,1]
v_pk_add_f32 v[124:125], v[12:13], v[28:29] neg_lo:[0,1] neg_hi:[0,1]
v_pk_add_f32 v[126:127], v[14:15], v[30:31] neg_lo:[0,1] neg_hi:[0,1]
v_lshlrev_b32 v108, 16, v60
v_and_b32 v109, 4294901760, v60
v_lshlrev_b32 v110, 16, v61
v_and_b32 v111, 4294901760, v61
v_lshlrev_b32 v112, 16, v62
v_and_b32 v113, 4294901760, v62
v_lshlrev_b32 v114, 16, v63
v_and_b32 v115, 4294901760, v63
v_pk_add_f32 v[100:101], v[100:101], v[108:109]
v_pk_add_f32 v[102:103], v[102:103], v[110:111]
v_pk_add_f32 v[104:105], v[104:105], v[112:113]
v_pk_add_f32 v[106:107], v[106:107], v[114:115]
v_lshlrev_b32 v108, 16, v76
v_and_b32 v109, 4294901760, v76
v_lshlrev_b32 v110, 16, v77
v_and_b32 v111, 4294901760, v77
v_lshlrev_b32 v112, 16, v78
v_and_b32 v113, 4294901760, v78
v_lshlrev_b32 v114, 16, v79
v_and_b32 v115, 4294901760, v79
v_pk_add_f32 v[100:101], v[100:101], v[108:109]
v_pk_add_f32 v[102:103], v[102:103], v[110:111]
v_pk_add_f32 v[104:105], v[104:105], v[112:113]
v_pk_add_f32 v[106:107], v[106:107], v[114:115]
s_waitcnt vmcnt(0)
v_lshlrev_b32 v108, 16, v92
v_and_b32 v109, 4294901760, v92
v_lshlrev_b32 v110, 16, v93
v_and_b32 v111, 4294901760, v93
v_lshlrev_b32 v112, 16, v94
v_and_b32 v113, 4294901760, v94
v_lshlrev_b32 v114, 16, v95
v_and_b32 v115, 4294901760, v95
v_pk_add_f32 v[100:101], v[100:101], v[108:109]
v_pk_add_f32 v[102:103], v[102:103], v[110:111]
v_pk_add_f32 v[104:105], v[104:105], v[112:113]
v_pk_add_f32 v[106:107], v[106:107], v[114:115]
v_pk_add_f32 v[108:109], v[120:121], v[100:101] neg_lo:[0,1] neg_hi:[0,1]
v_pk_add_f32 v[110:111], v[122:123], v[102:103] neg_lo:[0,1] neg_hi:[0,1]
v_pk_add_f32 v[112:113], v[124:125], v[104:105] neg_lo:[0,1] neg_hi:[0,1]
v_pk_add_f32 v[114:115], v[126:127], v[106:107] neg_lo:[0,1] neg_hi:[0,1]
v_pk_fma_f32 v[100:101], v[148:149], v[108:109], v[100:101]
v_pk_fma_f32 v[102:103], v[148:149], v[110:111], v[102:103]
v_pk_fma_f32 v[104:105], v[148:149], v[112:113], v[104:105]
v_pk_fma_f32 v[106:107], v[148:149], v[114:115], v[106:107]
v_cvt_pk_bf16_f32 v108, v100, v101
v_cvt_pk_bf16_f32 v109, v102, v103
v_cvt_pk_bf16_f32 v110, v104, v105
v_cvt_pk_bf16_f32 v111, v106, v107
ds_write_b64 v150, v[108:109] offset:96
ds_write_b64 v150, v[110:111] offset:112
s_lshl_b32 s66, s56, 7
s_lshl_b32 s67, s10, 5
s_add_u32 s66, s66, s67
s_mul_i32 s66, s66, 1152
s_lshl_b32 s67, s11, 6
s_add_u32 s66, s66, s67
s_waitcnt lgkmcnt(0)
ds_read_b128 v[32:35], v141
ds_read_b128 v[36:39], v141 offset:2304
ds_read_b128 v[40:43], v141 offset:64
ds_read_b128 v[44:47], v141 offset:2368
s_add_u32 s67, s66, 0
s_waitcnt lgkmcnt(3)
buffer_store_dwordx4 v[32:35], v142, s[48:51], s67 offen sc1
s_add_u32 s67, s66, 18432
s_waitcnt lgkmcnt(2)
buffer_store_dwordx4 v[36:39], v142, s[48:51], s67 offen sc1
s_add_u32 s67, s66, 576
s_waitcnt lgkmcnt(1)
buffer_store_dwordx4 v[40:43], v142, s[48:51], s67 offen sc1
s_add_u32 s67, s66, 19008
s_waitcnt lgkmcnt(0)
buffer_store_dwordx4 v[44:47], v142, s[48:51], s67 offen sc1
L_done28:
; ==== end (1 iteration(s))
s_waitcnt vmcnt(0) lgkmcnt(0)
s_endpgm
L_end:
)ASM" : "+{v0}"(tid)
   : "{s[4:5]}"(x), "{s[6:7]}"(wr), "{s[8:9]}"(wi), "{s[10:11]}"(wg), "{s[12:13]}"(wu), "{s[14:15]}"(wd), "{s[16:17]}"(gw), "{s[18:19]}"(ws), "{s[20:21]}"(out), "{s22}"(blockIdx.x)
   : "v1", "v2", "v3", "v4", "v5", "v6", "v7", "v8", "v9", "v10", "v11", "v12", "v13", "v14", "v15", "v16", "v17", "v18", "v19", "v20", "v21", "v22", "v23", "v24", "v25", "v26", "v27", "v28", "v29", "v30", "v31", "v32", "v33", "v34", "v35", "v36", "v37", "v38", "v39", "v40", "v41", "v42", "v43", "v44", "v45", "v46", "v47", "v48", "v49", "v50", "v51", "v52", "v53", "v54", "v55", "v56", "v57", "v58", "v59", "v60", "v61", "v62", "v63", "v64", "v65", "v66", "v67", "v68", "v69", "v70", "v71", "v72", "v73", "v74", "v75", "v76", "v77", "v78", "v79", "v80", "v81", "v82", "v83", "v84", "v85", "v86", "v87", "v88", "v89", "v90", "v91", "v92", "v93", "v94", "v95", "v96", "v97", "v98", "v99", "v100", "v101", "v102", "v103", "v104", "v105", "v106", "v107", "v108", "v109", "v110", "v111", "v112", "v113", "v114", "v115", "v116", "v117", "v118", "v119", "v120", "v121", "v122", "v123", "v124", "v125", "v126", "v127", "v128", "v129", "v130", "v131", "v132", "v133", "v134", "v135", "v136", "v137", "v138", "v139", "v140", "v141", "v142", "v143", "v144", "v145", "v146", "v147", "v148", "v149", "v150", "v151", "v152", "v153", "v154", "v155", "v156", "v157", "v158", "v159", "v160", "v161", "v162", "v163", "v164", "v165", "v166", "v167", "v168", "v169", "v170", "v171", "v172", "v173", "v174", "v175", "v176", "v177", "v178", "v179", "v180", "v181", "v182", "v183", "v184", "v185", "v186", "v187", "v188", "v189", "v190", "v191", "v192", "v193", "v194", "v195", "v196", "v197", "v198", "v199", "v200", "v201", "v202", "v203", "v204", "v205", "v206", "v207", "v208", "v209", "v210", "v211", "v212", "v213", "v214", "v215", "v216", "v217", "v218", "v219", "v220", "v221", "v222", "v223", "v224", "v225", "v226", "v227", "v228", "v229", "v230", "v231", "v232", "v233", "v234", "v235", "v236", "v237", "v238", "v239", "v240", "v241", "v242", "v243", "v244", "v245", "v246", "v247", "v248", "v249", "v250", "v251", "v252", "v253", "v254", "v255", "a0", "a1", "a2", "a3", "a4", "a5", "a6", "a7", "a8", "a9", "a10", "a11", "a12", "a13", "a14", "a15", "a16", "a17", "a18", "a19", "a20", "a21", "a22", "a23", "a24", "a25", "a26", "a27", "a28", "a29", "a30", "a31", "a32", "a33", "a34", "a35", "a36", "a37", "a38", "a39", "a40", "a41", "a42", "a43", "a44", "a45", "a46", "a47", "a48", "a49", "a50", "a51", "a52", "a53", "a54", "a55", "a56", "a57", "a58", "a59", "a60", "a61", "a62", "a63", "a64", "a65", "a66", "a67", "a68", "a69", "a70", "a71", "a72", "a73", "a74", "a75", "a76", "a77", "a78", "a79", "a80", "a81", "a82", "a83", "a84", "a85", "a86", "a87", "a88", "a89", "a90", "a91", "a92", "a93", "a94", "a95", "a96", "a97", "a98", "a99", "a100", "a101", "a102", "a103", "a104", "a105", "a106", "a107", "a108", "a109", "a110", "a111", "a112", "a113", "a114", "a115", "a116", "a117", "a118", "a119", "a120", "a121", "a122", "a123", "a124", "a125", "a126", "a127", "a128", "a129", "a130", "a131", "a132", "a133", "a134", "a135", "a136", "a137", "a138", "a139", "a140", "a141", "a142", "a143", "a144", "a145", "a146", "a147", "a148", "a149", "a150", "a151", "a152", "a153", "a154", "a155", "a156", "a157", "a158", "a159", "a160", "a161", "a162", "a163", "a164", "a165", "a166", "a167", "a168", "a169", "a170", "a171", "a172", "a173", "a174", "a175", "a176", "a177", "a178", "a179", "a180", "a181", "a182", "a183", "a184", "a185", "a186", "a187", "a188", "a189", "a190", "a191", "a192", "a193", "a194", "a195", "a196", "a197", "a198", "a199", "a200", "a201", "a202", "a203", "a204", "a205", "a206", "a207", "a208", "a209", "a210", "a211", "a212", "a213", "a214", "a215", "a216", "a217", "a218", "a219", "a220", "a221", "a222", "a223", "a224", "a225", "a226", "a227", "a228", "a229", "a230", "a231", "a232", "a233", "a234", "a235", "a236", "a237", "a238", "a239", "a240", "a241", "a242", "a243", "a244", "a245", "a246", "a247", "a248", "a249", "a250", "a251", "a252", "a253", "a254", "a255", "s0", "s1", "s2", "s3", "s23", "s24", "s25", "s26", "s27", "s28", "s29", "s30", "s31", "s33", "s34", "s35", "s36", "s37", "s38", "s39", "s40", "s41", "s42", "s43", "s44", "s45", "s46", "s47", "s48", "s49", "s50", "s51", "s52", "s53", "s54", "s55", "s56", "s57", "s58", "s59", "s60", "s61", "s62", "s63", "s64", "s65", "s66", "s67", "s68", "s69", "s70", "s71", "s72", "s73", "s74", "s75", "s76", "s77", "s78", "s79", "s80", "s81", "s82", "s83", "s84", "s85", "s86", "s87", "s88", "s89", "s90", "s91", "s92", "s93", "s94", "s95", "s96", "s97", "vcc", "scc", "memory");
}


extern "C" void kernel_launch(void* const* d_in, const int* in_sizes, int n_in, void* d_out, int out_size, void* d_ws, size_t ws_size, hipStream_t stream) {
  const u16* x = (const u16*)d_in[0];
  const u16* wr = (const u16*)d_in[1];
  const u16* wi = (const u16*)d_in[2];
  const u16* wg = (const u16*)d_in[4];
  const u16* wu = (const u16*)d_in[5];
  const u16* wd = (const u16*)d_in[6];
  const u16* gw = (const u16*)d_in[7];
  char* ws = (char*)d_ws;
  (void)hipFuncSetAttribute((const void*)k_mega, hipFuncAttributeMaxDynamicSharedMemorySize, 163840);
  k_mega<<<256, 256, 163840, stream>>>(x, wr, wi, wg, wu, wd, gw, ws, (u16*)d_out);
}
```
